```python
import torch

def _softmax(x, axis=-1):
    return torch.softmax(x, dim=axis)
def _gelu(x):
    return torch.nn.functional.gelu(x, approximate="tanh")
def _silu(x):
    return torch.nn.functional.silu(x)
def _sigmoid(x):
    return torch.sigmoid(x)
def _layernorm(x, axis=-1, eps=1e-5):
    m = x.mean(dim=axis, keepdim=True)
    v = x.var(dim=axis, keepdim=True, unbiased=False)
    return (x - m) / torch.sqrt(v + eps)
def _scatter_add(tgt, idx, src, axis=0):
    return tgt.clone().index_add_(axis, idx, src)
def _segment_sum(src, seg, n):
    out = torch.zeros((n,) + src.shape[1:], dtype=src.dtype)
    return out.index_add_(0, seg, src)
def _pad(x, pw):
    flat = tuple(v for pair in reversed(pw) for v in pair)
    return torch.nn.functional.pad(x, flat)

kernel_name = 'interleave_layers__mha_block__glu_gated_geglu__prompt'

def setup_inputs():

    _w = (lambda a: (lambda t: t.to(torch.bfloat16) if t.is_floating_point() else t)(torch.as_tensor(a)))
    def _A_setup():
        B, S, D, H = 1, 2048, 640, 32
        return {
            "x": _w((lambda *s: torch.randn(*s, dtype=torch.bfloat16) * 0.05)(B, S, D)),
            "wqkv": _w((lambda *s: torch.randn(*s, dtype=torch.bfloat16) * 0.05)(D, 3 * D)),
            "wo": _w((lambda *s: torch.randn(*s, dtype=torch.bfloat16) * 0.05)(D, D)),
        }
    _A_in = _A_setup()

    def _B_setup():
        B, S, Dm, Df = 1, 2048, 640, 1728
        return {
            "x": _w((lambda *s: torch.randn(*s, dtype=torch.bfloat16) * 0.05)(B, S, Dm)),
            "w_gate": _w((lambda *s: torch.randn(*s, dtype=torch.bfloat16) * 0.8)(Dm, Df) * (Dm ** -0.5)),
            "w_up": _w((lambda *s: torch.randn(*s, dtype=torch.bfloat16) * 0.8)(Dm, Df) * (Dm ** -0.5)),
            "w_down": _w((lambda *s: torch.randn(*s, dtype=torch.bfloat16) * 0.8)(Df, Dm) * (Df ** -0.5)),
        }
    _B_in = _B_setup()

    return {
        "A_x": _A_in["x"],
        "A_wqkv": _A_in["wqkv"],
        "A_wo": _A_in["wo"],
        "B_x": _B_in["x"],
        "B_w_gate": _B_in["w_gate"],
        "B_w_up": _B_in["w_up"],
        "B_w_down": _B_in["w_down"],
    }

def reference(A_x, A_wqkv, A_wo, B_x, B_w_gate, B_w_up, B_w_down):
    def _A_ref(x, wqkv, wo):
        B, S, D = x.shape
        H = 32
        Dh = D // H
        qkv = (lambda x, *d: x.permute(*d))((x @ wqkv).reshape(B, S, 3, H, Dh), 2, 0, 3, 1, 4)
        q, k, v = qkv[0], qkv[1], qkv[2]
        scores = torch.einsum("bhqd,bhkd->bhqk", q, k) * (Dh ** -0.5)
        p = _softmax(scores, axis=-1)
        o = torch.einsum("bhqk,bhkd->bhqd", p, v)
        return (lambda x, *d: x.permute(*d))(o, 0, 2, 1, 3).reshape(B, S, D) @ wo

    def _B_ref(x, w_gate, w_up, w_down):
        g = torch.einsum("bsd,df->bsf", x, w_gate)
        u = torch.einsum("bsd,df->bsf", x, w_up)
        h = _gelu(g) * u
        return torch.einsum("bsf,fd->bsd", h, w_down)

    x = A_x
    x = x + _A_ref(_layernorm(x, axis=-1), A_wqkv, A_wo)
    x = x + _B_ref(_layernorm(x, axis=-1), B_w_gate, B_w_up, B_w_down)
    x = x + _A_ref(_layernorm(x, axis=-1), A_wqkv, A_wo)
    x = x + _B_ref(_layernorm(x, axis=-1), B_w_gate, B_w_up, B_w_down)
    return x
```

```cpp
#include <hip/hip_runtime.h>
#include <hip/hip_bf16.h>
#include <stdint.h>

typedef __attribute__((ext_vector_type(8))) short bf16x8;
typedef __attribute__((ext_vector_type(4))) short bf16x4;
typedef __attribute__((ext_vector_type(4))) float f32x4;
typedef __attribute__((ext_vector_type(16))) float f32x16;
typedef __attribute__((ext_vector_type(4))) unsigned u32x4;
typedef __attribute__((ext_vector_type(2))) unsigned u32x2;
typedef unsigned short u16;
typedef __attribute__((address_space(3))) const char* lds_cptr;

#define DI __device__ __forceinline__

constexpr int SEQ = 2048, DM = 640, NH = 32, DH = 20, DF = 1728;
constexpr int NSEG = 8;
constexpr float LN_EPS = 1e-5f;
constexpr float QSCALE = 0.22360679774997896f * 1.4426950408889634f;

constexpr size_t WS_X1   = 0;
constexpr size_t WS_X2   = WS_X1 + (size_t)SEQ * DM * 2;
constexpr size_t WS_X3   = WS_X2 + (size_t)SEQ * DM * 2;
constexpr size_t WS_Q    = WS_X3 + (size_t)SEQ * DM * 2;
constexpr size_t WS_O    = WS_Q + (size_t)SEQ * DM * 2;
constexpr size_t WS_H    = WS_O + (size_t)SEQ * DM * 2;
constexpr int    KV_CHUNK = 26624;
constexpr size_t WS_KV   = WS_H + (size_t)SEQ * DF * 2;
constexpr size_t WS_ST   = WS_KV + (size_t)NH * 8 * KV_CHUNK + 4096;
constexpr size_t WS_CS   = WS_ST + (size_t)SEQ * NSEG * 8;

DI float bf2f(u16 v) { return __uint_as_float(((unsigned)v) << 16); }
DI unsigned cvtpk(float lo, float hi) { unsigned r; asm("v_cvt_pk_bf16_f32 %0, %1, %2" : "=v"(r) : "v"(lo), "v"(hi)); return r; }
template <int N> DI void wait_vm() { asm volatile("s_waitcnt vmcnt(%0)" ::"i"(N) : "memory"); }
DI void raw_barrier() { asm volatile("s_waitcnt lgkmcnt(0)" ::: "memory"); __builtin_amdgcn_s_barrier(); }
DI bf16x4 tr_read(const char* p) {
  typedef short v4i16_t __attribute__((ext_vector_type(4)));
  return __builtin_bit_cast(bf16x4, __builtin_amdgcn_ds_read_tr16_b64_v4i16((__attribute__((address_space(3))) v4i16_t*)(p)));
}
DI void glds16(const void* g, char* l) {
  __builtin_amdgcn_global_load_lds((const unsigned*)g, (unsigned*)l, 16, 0, 0);
}

__global__ __launch_bounds__(256) void prep_kernel(const u16* __restrict__ x0, const u16* __restrict__ wqkv,
                                                   const u16* __restrict__ wg, const u16* __restrict__ wu,
                                                   char* __restrict__ ws) {
  __shared__ float red[32][64];
  const int tid = threadIdx.x, b = blockIdx.x;
  if (b < 84) {
    const u16* W; int ld, c0; float* out = (float*)(ws + WS_CS);
    if (b < 30) { W = wqkv; ld = 3 * DM; c0 = b * 64; out += c0; }
    else if (b < 57) { W = wg; ld = DF; c0 = (b - 30) * 64; out += 1920 + c0; }
    else { W = wu; ld = DF; c0 = (b - 57) * 64; out += 1920 + DF + c0; }
    const int cg = tid & 7, rs = tid >> 3;
    float s[8] = {0, 0, 0, 0, 0, 0, 0, 0};
    for (int r = 0; r < 20; ++r) {
      bf16x8 v = *(const bf16x8*)(W + (size_t)(rs * 20 + r) * ld + c0 + cg * 8);
#pragma unroll
      for (int j = 0; j < 8; ++j) s[j] += bf2f((u16)v[j]);
    }
#pragma unroll
    for (int j = 0; j < 8; ++j) red[rs][cg * 8 + j] = s[j];
    __syncthreads();
    if (tid < 64) { float t = 0; for (int r = 0; r < 32; ++r) t += red[r][tid]; out[tid] = t; }
  } else if (b < 84 + 128) {
    const int lane = tid & 63, w = tid >> 6;
    float2* st = (float2*)(ws + WS_ST);
    for (int i = 0; i < 4; ++i) {
      const int row = (b - 84) * 16 + w * 4 + i;
      const u16* p = x0 + (size_t)row * DM + lane * 10;
      float v[10]; float s = 0;
#pragma unroll
      for (int j = 0; j < 5; ++j) { unsigned u = *(const unsigned*)(p + 2 * j); v[2 * j] = __uint_as_float(u << 16); v[2 * j + 1] = __uint_as_float(u & 0xffff0000u); }
#pragma unroll
      for (int j = 0; j < 10; ++j) s += v[j];
      s += __shfl_xor(s, 1); s += __shfl_xor(s, 2); s += __shfl_xor(s, 4);
      const float m = s * (1.0f / 80.0f); float q = 0;
#pragma unroll
      for (int j = 0; j < 10; ++j) { float d = v[j] - m; q += d * d; }
      q += __shfl_xor(q, 1); q += __shfl_xor(q, 2); q += __shfl_xor(q, 4);
      if ((lane & 7) == 0) st[(size_t)row * NSEG + (lane >> 3)] = make_float2(s, q);
    }
  } else {
    const int i = (b - 212) * 256 + tid;
    if (i < NH * 128 * 2) {
      const int hh = i & 1, step = (i >> 1) & 127, head = i >> 8;
      char* p = ws + WS_KV + (size_t)(head * 8 + (step >> 4)) * KV_CHUNK + 10240 + (step & 15) * 1024 + (hh * 32 + 20) * 16;
      *(u32x4*)p = (u32x4){0x3f803f80u, 0x3f803f80u, 0x3f803f80u, 0x3f803f80u};
    }
  }
}

enum { EPI_QKV = 0, EPI_RES = 1, EPI_GEGLU = 2 };

struct GemmArgs {
  const u16* A; const u16* B; const u16* B2;
  const u16* res;
  u16* out;
  char* ws;
};

template <int BM, int BN, int NWM, int NWN, int KTOT, int LDB, int NST, int EPI>
__global__ __launch_bounds__(NWM* NWN * 64) void gemm_kernel(GemmArgs g) {
  constexpr int NW = NWM * NWN, MT = BM / NWM / 16, NT = BN / NWN / 16, KT = KTOT / 64;
  constexpr int RS = BN * 2;
  constexpr bool BSWZ = (BN == 128);
  constexpr int IA = BM / 8 / NW, IB = (BN / 8 + NW - 1) / NW, L = IA + IB;
  constexpr int ABYTES = BM * 128, BBYTES = IB * NW * 1024, STB = ABYTES + BBYTES;
  constexpr int NTN = (EPI == EPI_QKV) ? (3 * DM / BN) : (EPI == EPI_GEGLU) ? (DF / (BN / 2)) : (DM / BN);
  __shared__ __attribute__((aligned(16))) char smem[NST * STB + BM * 8];
  float2* rowst = (float2*)(smem + NST * STB);

  const int tid = threadIdx.x, lane = tid & 63, wid = __builtin_amdgcn_readfirstlane(tid >> 6);
  const int wm = wid / NWN, wn = wid % NWN;
  int bid = blockIdx.x;
  {
    const int nwg = gridDim.x, q = nwg / 8, r = nwg % 8, xcd = bid % 8, off = bid / 8;
    bid = (xcd < r ? xcd * (q + 1) : r * (q + 1) + (xcd - r) * q) + off;
  }
  const int tm = bid / NTN, tn = bid % NTN;
  const int m0 = tm * BM;
  const int n0 = (EPI == EPI_GEGLU) ? tn * (BN / 2) : tn * BN;

  const u16* asrc[IA]; const u16* bsrc[IB];
#pragma unroll
  for (int i = 0; i < IA; ++i) {
    const int row = (wid * IA + i) * 8 + (lane >> 3), c = (lane & 7) ^ ((row >> 1) & 7);
    asrc[i] = g.A + (size_t)(m0 + row) * KTOT + c * 8;
  }
#pragma unroll
  for (int i = 0; i < IB; ++i) {
    int u = (wid * IB + i) * 64 + lane; if (u >= 8 * BN) u = 8 * BN - 1;
    const int prow = u / (BN / 8), c16 = u % (BN / 8);
    int ch = c16 >> 1; if (BSWZ) ch ^= (prow & 7);
    const int col = ch * 16 + (c16 & 1) * 8;
    const int k = (prow & 32) + 8 * (((prow >> 3) & 1) * 2 + ((prow >> 2) & 1)) + 4 * ((prow >> 4) & 1) + (prow & 3);
    if (EPI == EPI_GEGLU) bsrc[i] = (col < BN / 2 ? g.B + n0 + col : g.B2 + n0 + col - BN / 2) + (size_t)k * LDB;
    else bsrc[i] = g.B + (size_t)k * LDB + n0 + col;
  }
  auto stage = [&](int kt, int buf) {
    char* base = smem + buf * STB;
#pragma unroll
    for (int i = 0; i < IA; ++i) glds16(asrc[i] + kt * 64, base + (wid * IA + i) * 1024);
#pragma unroll
    for (int i = 0; i < IB; ++i) glds16(bsrc[i] + (size_t)kt * 64 * LDB, base + ABYTES + (wid * IB + i) * 1024);
  };

  if (EPI != EPI_RES) {
    if (tid < BM) {
      const float4* p = (const float4*)(g.ws + WS_ST + (size_t)(m0 + tid) * NSEG * 8);
      float s[8], q[8];
#pragma unroll
      for (int j = 0; j < 4; ++j) { float4 v = p[j]; s[2 * j] = v.x; q[2 * j] = v.y; s[2 * j + 1] = v.z; q[2 * j + 1] = v.w; }
      float st = 0;
#pragma unroll
      for (int j = 0; j < 8; ++j) st += s[j];
      const float mean = st * (1.0f / DM); float m2 = 0;
#pragma unroll
      for (int j = 0; j < 8; ++j) { float d = s[j] * (1.0f / 80.0f) - mean; m2 += q[j] + 80.0f * d * d; }
      rowst[tid] = make_float2(mean, rsqrtf(m2 * (1.0f / DM) + LN_EPS));
    }
  }

#pragma unroll
  for (int s = 0; s < NST - 1; ++s) stage(s, s);

  const int fr = lane & 15, fg = lane >> 4;
  int aoff[2];
#pragma unroll
  for (int ks = 0; ks < 2; ++ks) aoff[ks] = (wm * MT * 16 + fr) * 128 + (((ks * 4 + fg) ^ (fr >> 1)) * 16);
  int boff[NT];
  {
    const int pr = 8 * (fg >> 1) + 4 * (fg & 1) + (fr >> 2);
#pragma unroll
    for (int j = 0; j < NT; ++j) {
      int c;
      if (EPI == EPI_GEGLU) c = (j < NT / 2) ? wn * (NT / 2) + j : (BN / 32) + wn * (NT / 2) + (j - NT / 2);
      else c = wn * NT + j;
      if (BSWZ) c ^= (pr & 7);
      boff[j] = ABYTES + pr * RS + c * 32 + (lane & 3) * 8;
    }
  }
  f32x4 acc[MT][NT];
#pragma unroll
  for (int i = 0; i < MT; ++i)
#pragma unroll
    for (int j = 0; j < NT; ++j) acc[i][j] = (f32x4){0.f, 0.f, 0.f, 0.f};

  const bool swp = !(EPI == EPI_QKV && tn >= 2 * (DM / BN));

  auto compute = [&](int buf) {
    const char* base = smem + buf * STB;
#pragma unroll
    for (int ks = 0; ks < 2; ++ks) {
      bf16x8 a[MT], b[NT];
#pragma unroll
      for (int i = 0; i < MT; ++i) a[i] = *(const bf16x8*)(base + aoff[ks] + i * 2048);
#pragma unroll
      for (int j = 0; j < NT; ++j) {
        bf16x4 lo = tr_read(base + boff[j] + (32 * ks) * RS), hi = tr_read(base + boff[j] + (32 * ks + 16) * RS);
        b[j] = __builtin_shufflevector(lo, hi, 0, 1, 2, 3, 4, 5, 6, 7);
      }
      if (swp) {
#pragma unroll
        for (int i = 0; i < MT; ++i)
#pragma unroll
          for (int j = 0; j < NT; ++j) acc[i][j] = __builtin_amdgcn_mfma_f32_16x16x32_bf16(b[j], a[i], acc[i][j], 0, 0, 0);
      } else {
#pragma unroll
        for (int i = 0; i < MT; ++i)
#pragma unroll
          for (int j = 0; j < NT; ++j) acc[i][j] = __builtin_amdgcn_mfma_f32_16x16x32_bf16(a[i], b[j], acc[i][j], 0, 0, 0);
      }
    }
  };

  int buf = 0;
  for (int kt = 0; kt < KT - (NST - 2); ++kt) {
    wait_vm<(NST - 2) * L>();
    raw_barrier();
    if (kt + NST - 1 < KT) { int nb = buf - 1; if (nb < 0) nb += NST; stage(kt + NST - 1, nb); }
    compute(buf);
    if (++buf == NST) buf = 0;
  }
  if (NST >= 4) { wait_vm<L>(); raw_barrier(); compute(buf); if (++buf == NST) buf = 0; }
  if (NST >= 3) { wait_vm<0>(); raw_barrier(); compute(buf); }

  if (EPI == EPI_RES) {
    float2* st = (float2*)(g.ws + WS_ST);
#pragma unroll
    for (int i = 0; i < MT; ++i) {
      const int row = m0 + wm * MT * 16 + i * 16 + fr;
      float v[NT][4]; float s = 0;
#pragma unroll
      for (int j = 0; j < NT; ++j) {
        const int col = n0 + (wn * NT + j) * 16 + fg * 4;
        u32x2 r = *(const u32x2*)(g.res + (size_t)row * DM + col);
        float o0 = acc[i][j][0] + __uint_as_float(r[0] << 16), o1 = acc[i][j][1] + __uint_as_float(r[0] & 0xffff0000u);
        float o2 = acc[i][j][2] + __uint_as_float(r[1] << 16), o3 = acc[i][j][3] + __uint_as_float(r[1] & 0xffff0000u);
        u32x2 pk = {cvtpk(o0, o1), cvtpk(o2, o3)};
        *(u32x2*)(g.out + (size_t)row * DM + col) = pk;
        v[j][0] = __uint_as_float(pk[0] << 16); v[j][1] = __uint_as_float(pk[0] & 0xffff0000u);
        v[j][2] = __uint_as_float(pk[1] << 16); v[j][3] = __uint_as_float(pk[1] & 0xffff0000u);
        s += v[j][0] + v[j][1] + v[j][2] + v[j][3];
      }
      static_assert(EPI != EPI_RES || (NWN == 1 && BN == 80), "stat segments assume one wave spans the 80 columns");
      s += __shfl_xor(s, 16); s += __shfl_xor(s, 32);
      const float m = s * (1.0f / 80.0f); float q = 0;
#pragma unroll
      for (int j = 0; j < NT; ++j)
#pragma unroll
        for (int r = 0; r < 4; ++r) { float d = v[j][r] - m; q += d * d; }
      q += __shfl_xor(q, 16); q += __shfl_xor(q, 32);
      if (fg == 0) st[(size_t)row * NSEG + tn] = make_float2(s, q);
    }
  } else if (EPI == EPI_GEGLU) {
    const float* csg = (const float*)(g.ws + WS_CS) + 1920, * csu = csg + DF;
#pragma unroll
    for (int i = 0; i < MT; ++i) {
      const int rl = wm * MT * 16 + i * 16 + fr; const float2 ms = rowst[rl];
#pragma unroll
      for (int j = 0; j < NT / 2; ++j) {
        const int col = n0 + (wn * (NT / 2) + j) * 16 + fg * 4;
        const float4 cg = *(const float4*)(csg + col), cu = *(const float4*)(csu + col);
        const float cgv[4] = {cg.x, cg.y, cg.z, cg.w}, cuv[4] = {cu.x, cu.y, cu.z, cu.w};
        float h[4];
#pragma unroll
        for (int r = 0; r < 4; ++r) {
          const float gg = ms.y * (acc[i][j][r] - ms.x * cgv[r]);
          const float uu = ms.y * (acc[i][j + NT / 2][r] - ms.x * cuv[r]);
          const float t = gg * (1.0f + 0.044715f * gg * gg) * (2.0f * 0.7978845608028654f * 1.4426950408889634f);
          const float e = __builtin_amdgcn_exp2f(-t);
          h[r] = gg * __builtin_amdgcn_rcpf(1.0f + e) * uu;
        }
        *(u32x2*)(g.out + (size_t)(m0 + rl) * DF + col) = (u32x2){cvtpk(h[0], h[1]), cvtpk(h[2], h[3])};
      }
    }
  } else {
    const float* cs = (const float*)(g.ws + WS_CS);
    if (swp) {
      const bool isq = tn < DM / BN;
#pragma unroll
      for (int i = 0; i < MT; ++i) {
        const int rl = wm * MT * 16 + i * 16 + fr; const float2 ms = rowst[rl]; const int tok = m0 + rl;
#pragma unroll
        for (int j = 0; j < NT; ++j) {
          const int col = n0 + (wn * NT + j) * 16 + fg * 4;
          const float4 c4 = *(const float4*)(cs + col);
          float o0 = ms.y * (acc[i][j][0] - ms.x * c4.x), o1 = ms.y * (acc[i][j][1] - ms.x * c4.y);
          float o2 = ms.y * (acc[i][j][2] - ms.x * c4.z), o3 = ms.y * (acc[i][j][3] - ms.x * c4.w);
          if (isq) {
            *(u32x2*)(g.ws + WS_Q + ((size_t)tok * DM + col) * 2) = (u32x2){cvtpk(o0 * QSCALE, o1 * QSCALE), cvtpk(o2 * QSCALE, o3 * QSCALE)};
          } else {
            const int c = col - DM, head = c / DH, d0 = c % DH;
            char* p = g.ws + WS_KV + (size_t)(head * 8 + (tok >> 8)) * KV_CHUNK + ((tok >> 5) & 7) * 1280;
            p += (d0 < 16) ? (((d0 >> 3) * 32 + (tok & 31)) * 16 + (d0 & 4) * 2) : (1024 + (tok & 31) * 8);
            *(u32x2*)p = (u32x2){cvtpk(o0, o1), cvtpk(o2, o3)};
          }
        }
      }
    } else {
#pragma unroll
      for (int i = 0; i < MT; ++i) {
        const int rl = wm * MT * 16 + i * 16 + fg * 4; const int tok = m0 + rl;
        const float4 ma = *(const float4*)(rowst + rl), mb = *(const float4*)(rowst + rl + 2);
#pragma unroll
        for (int j = 0; j < NT; ++j) {
          const int col = n0 + (wn * NT + j) * 16 + fr;
          const float c = cs[col];
          float o0 = ma.y * (acc[i][j][0] - ma.x * c), o1 = ma.w * (acc[i][j][1] - ma.z * c);
          float o2 = mb.y * (acc[i][j][2] - mb.x * c), o3 = mb.w * (acc[i][j][3] - mb.z * c);
          const int cc = col - 2 * DM, head = cc / DH, d = cc % DH;
          const int kg = (tok >> 2) & 3;
          char* p = g.ws + WS_KV + (size_t)(head * 8 + (tok >> 8)) * KV_CHUNK + 10240 + ((tok >> 4) & 15) * 1024 +
                    ((kg & 1) * 32 + d) * 16 + (kg >> 1) * 8;
          *(u32x2*)p = (u32x2){cvtpk(o0, o1), cvtpk(o2, o3)};
        }
      }
    }
  }
}

constexpr int ATT_NBUF = 3;

template <bool SAFE>
DI bool attn_pass(const char* __restrict__ kvbase, char* smem, const bf16x8 qf1, const bf16x4 qf2, int lane, int wid,
                  f32x16& o) {
  const int r32 = lane & 31;
  auto stage = [&](int c, int buf) {
#pragma unroll
    for (int i = 0; i < 4; ++i) {
      const int piece = wid + i * 8;
      const int pc = piece < 26 ? piece : 25;
      glds16(kvbase + (size_t)c * KV_CHUNK + pc * 1024 + lane * 16, smem + buf * 32768 + pc * 1024);
    }
  };
  stage(0, 0); stage(1, 1);
  f32x16 negm;
#pragma unroll
  for (int i = 0; i < 16; ++i) { negm[i] = 0.f; o[i] = 0.f; }
  int buf = 0;
  for (int c = 0; c < 8; ++c) {
    if (c < 7) wait_vm<4>(); else wait_vm<0>();
    raw_barrier();
    if (c + 2 < 8) { int nb = buf - 1; if (nb < 0) nb += ATT_NBUF; stage(c + 2, nb); }
    const char* kb = smem + buf * 32768;
    const char* vb = kb + 10240;
    if (c == 0) {
      bf16x8 k1 = *(const bf16x8*)(kb + lane * 16);
      bf16x4 k2 = *(const bf16x4*)(kb + 1024 + r32 * 8);
      f32x16 s = __builtin_amdgcn_mfma_f32_32x32x16_bf16(k1, qf1, negm, 0, 0, 0);
      s = __builtin_amdgcn_mfma_f32_32x32x8bf16_1k(k2, qf2, s, 0, 0, 0);
      float m = s[0];
#pragma unroll
      for (int i = 1; i < 16; ++i) m = fmaxf(m, s[i]);
      m = fmaxf(m, __shfl_xor(m, 32));
#pragma unroll
      for (int i = 0; i < 16; ++i) negm[i] = -m;
    }
#pragma unroll 2
    for (int t = 0; t < 8; ++t) {
      bf16x8 k1 = *(const bf16x8*)(kb + t * 1280 + lane * 16);
      bf16x4 k2 = *(const bf16x4*)(kb + t * 1280 + 1024 + r32 * 8);
      bf16x8 v0 = *(const bf16x8*)(vb + t * 2048 + lane * 16);
      bf16x8 v1 = *(const bf16x8*)(vb + t * 2048 + 1024 + lane * 16);
      f32x16 s = __builtin_amdgcn_mfma_f32_32x32x16_bf16(k1, qf1, negm, 0, 0, 0);
      s = __builtin_amdgcn_mfma_f32_32x32x8bf16_1k(k2, qf2, s, 0, 0, 0);
      if (SAFE) {
        float m = s[0];
#pragma unroll
        for (int i = 1; i < 16; ++i) m = fmaxf(m, s[i]);
        if (__any(m > 0.f)) {
          m = fmaxf(m, __shfl_xor(m, 32));
          m = fmaxf(m, 0.f);
          const float f = __builtin_amdgcn_exp2f(-m);
#pragma unroll
          for (int i = 0; i < 16; ++i) { s[i] -= m; o[i] *= f; negm[i] -= m; }
        }
      }
      u32x4 p0, p1;
#pragma unroll
      for (int i = 0; i < 4; ++i) {
        p0[i] = cvtpk(__builtin_amdgcn_exp2f(s[2 * i]), __builtin_amdgcn_exp2f(s[2 * i + 1]));
        p1[i] = cvtpk(__builtin_amdgcn_exp2f(s[8 + 2 * i]), __builtin_amdgcn_exp2f(s[8 + 2 * i + 1]));
      }
      o = __builtin_amdgcn_mfma_f32_32x32x16_bf16(v0, __builtin_bit_cast(bf16x8, p0), o, 0, 0, 0);
      o = __builtin_amdgcn_mfma_f32_32x32x16_bf16(v1, __builtin_bit_cast(bf16x8, p1), o, 0, 0, 0);
    }
    if (++buf == ATT_NBUF) buf = 0;
  }
  bool bad = false;
#pragma unroll
  for (int i = 0; i < 9; ++i) bad |= !(fabsf(o[i]) < 3.0e38f);
#pragma unroll
  for (int i = 9; i < 12; ++i) bad |= (lane < 32) && !(fabsf(o[i]) < 3.0e38f);
  return bad;
}

__global__ __launch_bounds__(512) void attn_kernel(char* __restrict__ ws) {
  __shared__ __attribute__((aligned(16))) char smem[ATT_NBUF * 32768 + 16];
  const int tid = threadIdx.x, lane = tid & 63, wid = __builtin_amdgcn_readfirstlane(tid >> 6);
  const int bid = blockIdx.x, xcd = bid & 7, idx = bid >> 3;
  const int head = xcd * 4 + (idx >> 3), qb = idx & 7;
  const int r32 = lane & 31, hh = lane >> 5;
  const int q = qb * 256 + wid * 32 + r32;
  const u16* Q = (const u16*)(ws + WS_Q) + (size_t)q * DM + head * DH;
  bf16x8 qf1; bf16x4 qf2;
  {
    bf16x4 a = *(const bf16x4*)(Q + hh * 8), b = *(const bf16x4*)(Q + hh * 8 + 4);
    qf1 = __builtin_shufflevector(a, b, 0, 1, 2, 3, 4, 5, 6, 7);
    bf16x4 c = *(const bf16x4*)(Q + 16);
    qf2 = hh ? (bf16x4){0, 0, 0, 0} : c;
  }
  const char* kvbase = ws + WS_KV + (size_t)head * 8 * KV_CHUNK;
  f32x16 o;
  bool bad = attn_pass<false>(kvbase, smem, qf1, qf2, lane, wid, o);
  if (__syncthreads_or(bad)) {
    attn_pass<true>(kvbase, smem, qf1, qf2, lane, wid, o);
  }
  const float l = __shfl(o[8], r32 + 32);
  const float inv = 1.0f / l;
  u16* O = (u16*)(ws + WS_O) + (size_t)q * DM + head * DH;
  *(u32x2*)(O + hh * 4) = (u32x2){cvtpk(o[0] * inv, o[1] * inv), cvtpk(o[2] * inv, o[3] * inv)};
  *(u32x2*)(O + 8 + hh * 4) = (u32x2){cvtpk(o[4] * inv, o[5] * inv), cvtpk(o[6] * inv, o[7] * inv)};
  if (hh == 0) *(u32x2*)(O + 16) = (u32x2){cvtpk(o[8] * inv, o[9] * inv), cvtpk(o[10] * inv, o[11] * inv)};
}

extern "C" void kernel_launch(void* const* d_in, const int* in_sizes, int n_in, void* d_out, int out_size, void* d_ws,
                              size_t ws_size, hipStream_t stream) {
  const u16* x0 = (const u16*)d_in[0];
  const u16* wqkv = (const u16*)d_in[1];
  const u16* wo = (const u16*)d_in[2];
  const u16* wg = (const u16*)d_in[4];
  const u16* wu = (const u16*)d_in[5];
  const u16* wd = (const u16*)d_in[6];
  char* ws = (char*)d_ws;
  u16* X1 = (u16*)(ws + WS_X1); u16* X2 = (u16*)(ws + WS_X2); u16* X3 = (u16*)(ws + WS_X3);
  u16* Ob = (u16*)(ws + WS_O); u16* Hb = (u16*)(ws + WS_H);

  prep_kernel<<<84 + 128 + 32, 256, 0, stream>>>(x0, wqkv, wg, wu, ws);
  for (int layer = 0; layer < 2; ++layer) {
    const u16* xin = layer ? X2 : x0;
    u16* xmid = layer ? X3 : X1;
    u16* xout = layer ? (u16*)d_out : X2;
    GemmArgs a{xin, wqkv, nullptr, nullptr, nullptr, ws};
    gemm_kernel<128, 128, 2, 4, 640, 1920, 4, EPI_QKV><<<16 * 15, 512, 0, stream>>>(a);
    attn_kernel<<<256, 512, 0, stream>>>(ws);
    GemmArgs b{Ob, wo, nullptr, xin, xmid, ws};
    gemm_kernel<64, 80, 4, 1, 640, 640, 4, EPI_RES><<<32 * 8, 256, 0, stream>>>(b);
    GemmArgs c{xmid, wg, wu, nullptr, Hb, ws};
    gemm_kernel<128, 128, 2, 4, 640, 1728, 4, EPI_GEGLU><<<16 * 27, 512, 0, stream>>>(c);
    GemmArgs d{Hb, wd, nullptr, xmid, xout, ws};
    gemm_kernel<64, 80, 4, 1, 1728, 640, 4, EPI_RES><<<32 * 8, 256, 0, stream>>>(d);
  }
}
```

```cpp
#include <hip/hip_runtime.h>
#include <hip/hip_bf16.h>
#include <stdint.h>

typedef __attribute__((ext_vector_type(8))) short bf16x8;
typedef __attribute__((ext_vector_type(4))) short bf16x4;
typedef __attribute__((ext_vector_type(4))) float f32x4;
typedef __attribute__((ext_vector_type(16))) float f32x16;
typedef __attribute__((ext_vector_type(4))) unsigned u32x4;
typedef __attribute__((ext_vector_type(2))) unsigned u32x2;
typedef unsigned short u16;

#define DI __device__ __forceinline__

constexpr int SEQ = 2048, DM = 640, NH = 32, DH = 20, DF = 1728;
constexpr float LN_EPS = 1e-5f;
constexpr float QSCALE = 0.22360679774997896f * 1.4426950408889634f;

constexpr size_t WS_X1   = 0;
constexpr size_t WS_X2   = WS_X1 + (size_t)SEQ * DM * 2;
constexpr size_t WS_X3   = WS_X2 + (size_t)SEQ * DM * 2;
constexpr size_t WS_Q    = WS_X3 + (size_t)SEQ * DM * 2;
constexpr size_t WS_O    = WS_Q + (size_t)SEQ * DM * 2;
constexpr size_t WS_H    = WS_O + (size_t)SEQ * DM * 2;
constexpr int    KVP_CHUNK = 26624, KVP_TAIL = 8192, KVP_V = 10240;
constexpr size_t WS_KV   = WS_H + (size_t)SEQ * DF * 2;
constexpr size_t WS_PWO  = WS_KV + (size_t)16 * 16 * KVP_CHUNK;
constexpr size_t WS_PWD  = WS_PWO + (size_t)DM * DM * 2;
constexpr size_t WS_PGU  = WS_PWD + (size_t)DF * DM * 2;
constexpr size_t WS_PQKV = WS_PGU + (size_t)16 * 10 * 28 * 1024;
constexpr size_t WS_CGU  = WS_PQKV + (size_t)DM * 3 * DM * 2;
constexpr size_t WS_CQKV = WS_CGU + (size_t)16 * 20 * 224 * 4;
constexpr size_t WS_STAT = WS_CQKV + (size_t)15 * 20 * 128 * 4;

typedef __attribute__((ext_vector_type(2))) __bf16 bf16p;
typedef __attribute__((ext_vector_type(2))) float f32x2;
DI unsigned cvtpk(float lo, float hi) { return __builtin_bit_cast(unsigned, __builtin_convertvector((f32x2){lo, hi}, bf16p)); }
DI float dot2ones(unsigned a, float s) {
  return __builtin_amdgcn_fdot2_f32_bf16(__builtin_bit_cast(bf16p, a), __builtin_bit_cast(bf16p, 0x3f803f80u), s, false);
}
DI float bflo(unsigned v) { return __uint_as_float(v << 16); }
DI float bfhi(unsigned v) { return __uint_as_float(v & 0xffff0000u); }
#define STAMP(kid, i)
#define CSTAMP(kid, i)
template <int N> DI void wait_vm() { asm volatile("s_waitcnt vmcnt(%0)" ::"i"(N) : "memory"); }
DI void raw_barrier() {
  asm volatile("" ::: "memory");
  __builtin_amdgcn_s_waitcnt(0xC07F);
  __builtin_amdgcn_s_barrier();
  asm volatile("" ::: "memory");
}
DI u32x2 swap16(unsigned a, unsigned b) { return __builtin_amdgcn_permlane16_swap(a, b, false, false); }
DI u32x2 swap32(unsigned a, unsigned b) { return __builtin_amdgcn_permlane32_swap(a, b, false, false); }
DI bf16x4 tr_read(const char* p) {
  typedef short v4i16_t __attribute__((ext_vector_type(4)));
  return __builtin_bit_cast(bf16x4, __builtin_amdgcn_ds_read_tr16_b64_v4i16((__attribute__((address_space(3))) v4i16_t*)(p)));
}
DI void glds16(const void* g, const char* l) {
  unsigned sv; const unsigned lb = __builtin_amdgcn_readfirstlane((unsigned)(uintptr_t)l);
  asm volatile("s_mov_b32 %0, m0\n\ts_mov_b32 m0, %2\n\ts_nop 0\n\tglobal_load_lds_dwordx4 %1, off\n\ts_mov_b32 m0, %0"
               : "=&s"(sv) : "v"(g), "s"(lb) : "memory");
}
DI u32x2 gload8(const void* g) {
  u32x2 r; asm volatile("global_load_dwordx2 %0, %1, off" : "=v"(r) : "v"(g) : "memory"); return r;
}
DI unsigned gload4(const void* g) {
  unsigned r; asm volatile("global_load_dword %0, %1, off" : "=v"(r) : "v"(g) : "memory"); return r;
}
DI u32x4 gload16(const void* g) {
  u32x4 r; asm volatile("global_load_dwordx4 %0, %1, off" : "=v"(r) : "v"(g) : "memory"); return r;
}
template <int I> struct IC { static constexpr int v = I; };
template <int L, int NMAX> DI void wait_stages(int n) {
  if constexpr (NMAX == 0) wait_vm<0>();
  else { if (n >= NMAX) wait_vm<NMAX * L>(); else wait_stages<L, NMAX - 1>(n); }
}
template <int N, int I = 0, class F> DI void static_for(F&& f) {
  if constexpr (I < N) { f(IC<I>{}); static_for<N, I + 1>(f); }
}
#define SB() __builtin_amdgcn_sched_barrier(0)
template <int NS> DI unsigned foff(int row, int col) {
  return (unsigned)((((row >> 6) * NS + (col >> 6)) * 8 + ((row >> 4) & 3) * 2 + ((col >> 5) & 1)) * 1024 +
                    (((col >> 3) & 3) * 16 + (row & 15)) * 16 + (col & 7) * 2);
}

enum { EPI_QKV = 0, EPI_RES = 1, EPI_GEGLU = 2 };

struct GemmArgs {
  const char* A; const char* B;
  const char* res;
  char* out;
  char* ws;
  int res_raw, out_raw;
};

template <int BM, int BN, int KTOT, int NST, int EPI, bool RAW>
__global__ __launch_bounds__(512) void gemm_kernel(GemmArgs g) {
  constexpr bool RES = (EPI == EPI_RES), LNP = !RES && !RAW;
  constexpr int MT = RES ? 2 : 4, NT = RES ? 5 : BN / 32, KT = KTOT / 64;
  constexpr int PA = BM / 8, PB = BN / 8, P = PA + PB, LMAX = (P + 3) / 4;
  constexpr int ABYTES = BM * 128, STB = P * 1024;
  constexpr int RS = BN * 2;
  static_assert(PA % 4 == 0 && KT >= NST && NST >= 3, "config");
  __shared__ __attribute__((aligned(16))) char smem[NST * STB + BM * 8 + BN * 4];
  float2* rowst = (float2*)(smem + NST * STB);
  float* colsum = (float*)(smem + NST * STB + BM * 8);

  const int tid = threadIdx.x, lane = tid & 63, wid = __builtin_amdgcn_readfirstlane(tid >> 6);
  char* wsdbg = g.ws; constexpr int KID = (EPI == EPI_QKV) ? 0 : (EPI == EPI_GEGLU) ? 3 : (KTOT == 640) ? 2 : 4;
  if (wid < 4) STAMP(KID, 0);
  const int xcd = blockIdx.x & 7, idx = blockIdx.x >> 3;
  int tm, tn;
  if (RES) { tm = (xcd & 3) * 8 + (idx & 7); tn = (xcd >> 2) * 4 + (idx >> 3); }
  else { tm = (xcd & 1) * 8 + (idx & 7); tn = (xcd >> 1) * 4 + (idx >> 3); }
  if (EPI == EPI_QKV && tn >= 15) return;
  const int m0 = tm * BM, n0 = tn * BN;
  const int fr = lane & 15, fg = lane >> 4;

  if (wid >= 4) {
    const int lw = wid - 4;
    const char* src[LMAX];
#pragma unroll
    for (int i = 0; i < LMAX; ++i) {
      const int p = lw + 4 * i;
      if (RAW) {
        if (i < PA / 4) {
          const int row = p * 8 + (lane >> 3), c = (lane & 7) ^ ((row >> 1) & 7);
          src[i] = g.A + ((size_t)(m0 + row) * KTOT + c * 8) * 2;
        } else {
          const int u = (p - PA) * 64 + lane, prow = u >> 4, c16 = u & 15;
          const int ch = (c16 >> 1) ^ (prow & 7);
          const int k = (prow & 32) + 8 * (((prow >> 3) & 1) * 2 + ((prow >> 2) & 1)) + 4 * ((prow >> 4) & 1) + (prow & 3);
          src[i] = g.B + ((size_t)k * (3 * DM) + n0 + ch * 16 + (c16 & 1) * 8) * 2;
        }
      } else {
        if (i < PA / 4) src[i] = g.A + ((size_t)((m0 >> 6) + (p >> 3)) * KT * 8 + (p & 7)) * 1024 + lane * 16;
        else src[i] = g.B + ((size_t)tn * KT * PB + (p - PA < PB ? p - PA : 0)) * 1024 + lane * 16;
      }
    }
    constexpr int STR_A = RAW ? 128 : 8192, STR_B = RAW ? 64 * 3 * DM * 2 : PB * 1024;
    auto body = [&](auto LC) {
      constexpr int L = decltype(LC)::v;
      auto issue = [&](int buf) {
        char* base = smem + buf * STB + lw * 1024;
#pragma unroll
        for (int i = 0; i < L; ++i) {
          glds16(src[i], base + i * 4096);
          src[i] += (i < PA / 4) ? STR_A : STR_B;
        }
      };
      issue(0); issue(1);
      int nxt = 2, ibuf = 2 % NST;
      constexpr int NMAIN = LNP ? KT - NST + 1 : KT;
      for (int i = 0; i < NMAIN; ++i) {
        wait_stages<L, NST - 1>(nxt - i - 1);
        raw_barrier();
        const int lim = min(KT, i + NST);
#pragma unroll
        for (int r = 0; r < 2; ++r)
          if (nxt < lim) { issue(ibuf); ++nxt; if (++ibuf == NST) ibuf = 0; }
      }
      if (LNP) {
        const int t = tid - 256;
        const float* cp = (const float*)(g.ws + (EPI == EPI_QKV ? WS_CQKV : WS_CGU)) + tn * 20 * BN + (t < BN ? t : 0);
        const char* sp = g.ws + WS_STAT + (size_t)(m0 + (t & 127)) * 64;
        unsigned c[20]; u32x4 st[4];
#pragma unroll
        for (int k = 0; k < 20; ++k) c[k] = gload4(cp + k * BN);
#pragma unroll
        for (int k = 0; k < 4; ++k) st[k] = gload16(sp + k * 16);
        static_for<NST - 1>([&](auto I) { wait_vm<(NST - 2 - decltype(I)::v) * L + 24>(); raw_barrier(); });
        wait_vm<0>();
        float cs = 0.f;
#pragma unroll
        for (int k = 0; k < 20; ++k) { asm volatile("" : "+v"(c[k])); cs += __uint_as_float(c[k]); }
#pragma unroll
        for (int k = 0; k < 4; ++k) asm volatile("" : "+v"(st[k]));
        const f32x4 sv = (__builtin_bit_cast(f32x4, st[0]) + __builtin_bit_cast(f32x4, st[1])) + (__builtin_bit_cast(f32x4, st[2]) + __builtin_bit_cast(f32x4, st[3]));
        const float mean = (sv[0] + sv[2]) * (1.0f / KTOT);
        const float var = fmaxf((sv[1] + sv[3]) * (1.0f / KTOT) - mean * mean, 0.f);
        if (t < BN) colsum[t] = cs;
        if (t >= 128) rowst[t - 128] = make_float2(mean, rsqrtf(var + LN_EPS));
        raw_barrier();
      }
    };
    if (P % 4 == 0 || lw < P % 4) body(IC<LMAX>{}); else body(IC<LMAX - 1>{});
    return;
  }

  const int wm = wid >> 1, wn = wid & 1;
  f32x4 acc[MT][NT];
#pragma unroll
  for (int i = 0; i < MT; ++i)
#pragma unroll
    for (int j = 0; j < NT; ++j) acc[i][j] = (f32x4){0.f, 0.f, 0.f, 0.f};

  if (RES) {
    const int row = m0 + wid * 16 + fr;
    u32x2 resv[NT];
#pragma unroll
    for (int j = 0; j < NT; ++j) {
      const int col = n0 + j * 16 + fg * 4;
      resv[j] = *(const u32x2*)(g.res + (g.res_raw ? (unsigned)(row * DM + col) * 2 : foff<10>(row, col)));
    }
    const int aoff = (wm * 4 + wn) * 1024 + lane * 16, boff = ABYTES + wn * 1024 + lane * 16;
    bf16x8 a0[2], b0[NT], a1[2], b1[NT];
    auto rd = [&](bf16x8(&a)[2], bf16x8(&b)[NT], const char* base) {
#pragma unroll
      for (int i = 0; i < 2; ++i) a[i] = *(const bf16x8*)(base + aoff + i * 2048);
#pragma unroll
      for (int j = 0; j < NT; ++j) b[j] = *(const bf16x8*)(base + boff + j * 2048);
      SB();
    };
    auto mm = [&](bf16x8(&a)[2], bf16x8(&b)[NT]) {
#pragma unroll
      for (int j = 0; j < NT; ++j)
#pragma unroll
        for (int i = 0; i < 2; ++i) acc[i][j] = __builtin_amdgcn_mfma_f32_16x16x32_bf16(b[j], a[i], acc[i][j], 0, 0, 0);
      SB();
    };
    STAMP(KID, 1);
    raw_barrier();
    STAMP(KID, 2); CSTAMP(KID, 2);
    rd(a0, b0, smem);
    int buf = 0;
#pragma unroll 1
    for (int kt = 0; kt + 2 < KT; kt += 2) {
      if (++buf == NST) buf = 0;
      raw_barrier();
      rd(a1, b1, smem + buf * STB);
      mm(a0, b0);
      if (++buf == NST) buf = 0;
      raw_barrier();
      rd(a0, b0, smem + buf * STB);
      mm(a1, b1);
    }
    if (KT % 2 == 0) {
      if (++buf == NST) buf = 0;
      raw_barrier();
      rd(a1, b1, smem + buf * STB);
      mm(a0, b0);
      mm(a1, b1);
    } else {
      mm(a0, b0);
    }
    STAMP(KID, 3); CSTAMP(KID, 3);
    char* xb = smem + ((KT % NST) * STB);
#pragma unroll
    for (int j = 0; j < NT; ++j) *(f32x4*)(xb + ((wid ^ 1) * NT + j) * 1024 + lane * 16) = wn ? acc[0][j] : acc[1][j];
    raw_barrier();
    STAMP(KID, 4);
    float s = 0.f, q = 0.f;
    u32x2 v[NT];
#pragma unroll
    for (int j = 0; j < NT; ++j) {
      const f32x4 o = *(const f32x4*)(xb + (wid * NT + j) * 1024 + lane * 16) + (wn ? acc[1][j] : acc[0][j]);
      const u32x2 r = resv[j];
      v[j] = (u32x2){cvtpk(o[0] + bflo(r[0]), o[1] + bfhi(r[0])), cvtpk(o[2] + bflo(r[1]), o[3] + bfhi(r[1]))};
      const float e0 = bflo(v[j][0]), e1 = bfhi(v[j][0]), e2 = bflo(v[j][1]), e3 = bfhi(v[j][1]);
      s += (e0 + e1) + (e2 + e3);
      q += (e0 * e0 + e1 * e1) + (e2 * e2 + e3 * e3);
    }
#pragma unroll
    for (int jp = 0; jp < NT; jp += 2) {
      const int j1 = jp + 1 < NT ? jp + 1 : jp;
      const u32x2 x = swap16(v[jp][0], v[j1][0]), y = swap16(v[jp][1], v[j1][1]);
      const int col = n0 + (jp + (fg & 1)) * 16 + (fg >> 1) * 8;
      if (jp + 1 < NT || !(fg & 1))
        *(u32x4*)(g.out + (g.out_raw ? (unsigned)(row * DM + col) * 2 : foff<10>(row, col))) = (u32x4){x[0], y[0], x[1], y[1]};
    }
    s += __shfl_xor(s, 16); q += __shfl_xor(q, 16);
    s += __shfl_xor(s, 32); q += __shfl_xor(q, 32);
    if (lane < 16) *(float2*)(g.ws + WS_STAT + (size_t)(row * 8 + tn) * 8) = make_float2(s, q);
    STAMP(KID, 5);
    STAMP(KID, 6);
    return;
  } else {
    if (EPI == EPI_QKV && tn >= 10) {
      const int vc0 = n0 - 2 * DM, head = (vc0 + DH - 1) / DH + (tid >> 4);
      if (head * DH < vc0 + BN) {
        const int tok = m0 + ((tid >> 1) & 7) * 16;
        char* p = g.ws + WS_KV + (size_t)((head >> 1) * 16 + (tok >> 7)) * KVP_CHUNK + KVP_V + (head & 1) * 8192 + ((tok >> 4) & 7) * 1024 + ((tid & 1) * 32 + DH) * 16;
        *(u32x4*)p = (u32x4){0x3f803f80u, 0x3f803f80u, 0x3f803f80u, 0x3f803f80u};
      }
    }
    int aoff[2], boff[RAW ? NT : 1];
    const int pr = 8 * (fg >> 1) + 4 * (fg & 1) + (fr >> 2);
#pragma unroll
    for (int ks = 0; ks < 2; ++ks)
      aoff[ks] = RAW ? (wm * 64 + fr) * 128 + (((ks * 4 + fg) ^ (fr >> 1)) * 16) : (wm * 8 + ks) * 1024 + lane * 16;
    if (RAW) {
#pragma unroll
      for (int j = 0; j < NT; ++j) boff[j] = ABYTES + pr * RS + ((wn * NT + j) ^ (pr & 7)) * 32 + (lane & 3) * 8;
    } else boff[0] = ABYTES + wn * NT * 2048 + lane * 16;
    f32x4 srow[2], sgr[2], scol[2];
#pragma unroll
    for (int i = 0; i < 2; ++i) srow[i] = sgr[i] = scol[i] = (f32x4){0.f, 0.f, 0.f, 0.f};
    const bf16x8 ones = (bf16x8){0x3f80, 0x3f80, 0x3f80, 0x3f80, 0x3f80, 0x3f80, 0x3f80, 0x3f80};

    const bool swp = !(EPI == EPI_QKV && tn >= 10);
    auto mainloop = [&](auto SW, auto WN, auto WM) {
      constexpr bool SWP = decltype(SW)::v;
      constexpr int CWN = decltype(WN)::v, CWM = decltype(WM)::v;
      bf16x8 a0[MT], a1[MT], b0[NT], b1[NT];
      auto rdB = [&](const char* base, int j, int ks) {
        if (RAW) {
          const char* p = base + boff[RAW ? j : 0] + ks * 32 * RS;
          bf16x4 lo = tr_read(p), hi = tr_read(p + 16 * RS);
          return __builtin_shufflevector(lo, hi, 0, 1, 2, 3, 4, 5, 6, 7);
        } else return *(const bf16x8*)(base + boff[0] + j * 2048 + ks * 1024);
      };
      auto step = [&](bf16x8(&ac)[MT], bf16x8(&bc)[NT], bf16x8(&an)[MT], bf16x8(&bn)[NT], const char* nb, int nks, auto RD) {
        constexpr bool rd = decltype(RD)::v;
        constexpr int RPG = 3;
#pragma unroll
        for (int j = 0; j < NT; ++j) {
#pragma unroll
          for (int i = 0; i < MT; ++i) {
            acc[i][j] = SWP ? __builtin_amdgcn_mfma_f32_16x16x32_bf16(bc[j], ac[i], acc[i][j], 0, 0, 0)
                            : __builtin_amdgcn_mfma_f32_16x16x32_bf16(ac[i], bc[j], acc[i][j], 0, 0, 0);
            const int r = j * RPG + i;
            if (rd && i < RPG && r < MT + NT) {
              if (r == 0) bn[0] = rdB(nb, 0, nks);
              else if (r <= MT) an[r - 1] = *(const bf16x8*)(nb + aoff[nks] + (r - 1) * 2048);
              else bn[r - MT] = rdB(nb, r - MT, nks);
              SB();
            }
          }
          if (RAW) {
            if ((j >> 1) == CWM) scol[j & 1] = __builtin_amdgcn_mfma_f32_16x16x32_bf16(bc[j], ones, scol[j & 1], 0, 0, 0);
            else {
              srow[j & 1] = __builtin_amdgcn_mfma_f32_16x16x32_bf16(ones, ac[CWN * 2 + (j & 1)], srow[j & 1], 0, 0, 0);
              sgr[j & 1] = __builtin_amdgcn_mfma_f32_16x16x32_bf16(ac[CWN * 2 + (j & 1)], ac[CWN * 2 + (j & 1)], sgr[j & 1], 0, 0, 0);
            }
          }
          SB();
        }
      };
      STAMP(KID, 1);
      raw_barrier();
      STAMP(KID, 2); CSTAMP(KID, 2);
#pragma unroll
      for (int j = 0; j < NT; ++j) { b0[j] = rdB(smem, j, 0); if (j < MT) a0[j] = *(const bf16x8*)(smem + aoff[0] + j * 2048); }
      step(a0, b0, a1, b1, smem, 1, IC<1>{});
      int buf = 0;
#pragma unroll 1
      for (int kt = 1; kt < KT; ++kt) {
        if (++buf == NST) buf = 0;
        raw_barrier();
        step(a1, b1, a0, b0, smem + buf * STB, 0, IC<1>{});
        step(a0, b0, a1, b1, smem + buf * STB, 1, IC<1>{});
      }
      step(a1, b1, a0, b0, smem, 0, IC<0>{});
    };
    if (RAW) {
      if (swp) {
        if (wid == 0) mainloop(IC<1>{}, IC<0>{}, IC<0>{}); else if (wid == 1) mainloop(IC<1>{}, IC<1>{}, IC<0>{});
        else if (wid == 2) mainloop(IC<1>{}, IC<0>{}, IC<1>{}); else mainloop(IC<1>{}, IC<1>{}, IC<1>{});
      } else {
        if (wid == 0) mainloop(IC<0>{}, IC<0>{}, IC<0>{}); else if (wid == 1) mainloop(IC<0>{}, IC<1>{}, IC<0>{});
        else if (wid == 2) mainloop(IC<0>{}, IC<0>{}, IC<1>{}); else mainloop(IC<0>{}, IC<1>{}, IC<1>{});
      }
#pragma unroll
      for (int i = 0; i < 2; ++i) {
        const float sq = (fr & 2) ? ((fr & 1) ? sgr[i][3] : sgr[i][2]) : ((fr & 1) ? sgr[i][1] : sgr[i][0]);
        const float mean = srow[i][0] * (1.0f / KTOT);
        const float var = fmaxf(sq * (1.0f / KTOT) - mean * mean, 0.f);
        if (fg == (fr >> 2)) rowst[wm * 64 + (wn * 2 + i) * 16 + fr] = make_float2(mean, rsqrtf(var + LN_EPS));
        if (fr == 0) *(f32x4*)(colsum + (wn * NT + wm * 2 + i) * 16 + fg * 4) = scol[i];
      }
      raw_barrier();
    } else {
      if (swp) mainloop(IC<1>{}, IC<0>{}, IC<0>{}); else mainloop(IC<0>{}, IC<0>{}, IC<0>{});
      STAMP(KID, 3); CSTAMP(KID, 3);
      raw_barrier();
    }
    STAMP(KID, 4);

    if (EPI == EPI_GEGLU) {
      const int f1 = fg & 1;
      const int hbase = (tn >> 1) * 216 + (tn & 1) * 112;
      const float2 ms0 = rowst[wm * 64 + (lane >> 5) * 16 + fr], ms1 = rowst[wm * 64 + (2 + (lane >> 5)) * 16 + fr];
      const int orow = m0 + wm * 64 + ((fg & 1) * 2 + (fg >> 1)) * 16 + fr;
#pragma unroll
      for (int j = 0; j < NT; ++j) {
        const int cb = (wn * NT + j) * 16 + f1 * 4;
        const f32x4 cg = *(const f32x4*)(colsum + cb), cu = *(const f32x4*)(colsum + cb + 8);
        u32x2 hp[2];
#pragma unroll
        for (int ip = 0; ip < 2; ++ip) {
          const float2 ms = ip ? ms1 : ms0;
          float h[4];
#pragma unroll
          for (int r = 0; r < 4; ++r) {
            const u32x2 sw = swap32(__float_as_uint(acc[ip * 2][j][r]), __float_as_uint(acc[ip * 2 + 1][j][r]));
            const float gg = ms.y * (__uint_as_float(sw[0]) - ms.x * cg[r]);
            const float uu = ms.y * (__uint_as_float(sw[1]) - ms.x * cu[r]);
            const float t = gg * (1.0f + 0.044715f * gg * gg) * (2.0f * 0.7978845608028654f * 1.4426950408889634f);
            const float e = __builtin_amdgcn_exp2f(-t);
            h[r] = gg * __builtin_amdgcn_rcpf(1.0f + e) * uu;
          }
          hp[ip] = (u32x2){cvtpk(h[0], h[1]), cvtpk(h[2], h[3])};
        }
        const u32x2 x = swap16(hp[0][0], hp[1][0]), y = swap16(hp[0][1], hp[1][1]);
        const int jb = wn * NT + j;
        if (jb < 13 || !(tn & 1)) *(u32x4*)(g.out + foff<27>(orow, hbase + jb * 8)) = (u32x4){x[0], y[0], x[1], y[1]};
      }
    } else {
      if (swp) {
        const bool isq = tn < DM / BN;
        const float sc = isq ? QSCALE : 1.0f;
#pragma unroll
        for (int jp = 0; jp < NT; jp += 2) {
          const int G = ((isq ? n0 : n0 - DM) >> 3) + (wn * NT + jp + (fg & 1)) * 2 + (fg >> 1), pair = G / 5, gi = G % 5;
          const f32x4 c0 = *(const f32x4*)(colsum + (wn * NT + jp) * 16 + fg * 4), c1 = *(const f32x4*)(colsum + (wn * NT + jp + 1) * 16 + fg * 4);
#pragma unroll
          for (int i = 0; i < MT; ++i) {
            const int rl = wm * 64 + i * 16 + fr; const float2 ms = rowst[rl]; const int tok = m0 + rl;
            const float rs = ms.y * sc;
            const f32x4 o0 = rs * (acc[i][jp] - ms.x * c0), o1 = rs * (acc[i][jp + 1] - ms.x * c1);
            const u32x2 x = swap16(cvtpk(o0[0], o0[1]), cvtpk(o1[0], o1[1])), y = swap16(cvtpk(o0[2], o0[3]), cvtpk(o1[2], o1[3]));
            char* p;
            if (isq) {
              p = g.ws + WS_Q + (size_t)(pair * 64 + (tok >> 5)) * 2560;
              p += (gi == 2) ? 2048 + (tok & 31) * 16 : (gi > 2) * 1024 + (((gi + (gi > 2)) & 1) * 32 + (tok & 31)) * 16;
            } else {
              p = g.ws + WS_KV + (size_t)(pair * 16 + (tok >> 7)) * KVP_CHUNK;
              p += (gi == 2) ? KVP_TAIL + (tok & 127) * 16 : (gi > 2) * 4096 + ((tok >> 5) & 3) * 1024 + (((gi + (gi > 2)) & 1) * 32 + (tok & 31)) * 16;
            }
            *(u32x4*)p = (u32x4){x[0], y[0], x[1], y[1]};
          }
        }
      } else {
#pragma unroll
        for (int jp = 0; jp < NT; jp += 2) {
          const float c0 = colsum[(wn * NT + jp) * 16 + fr], c1 = colsum[(wn * NT + jp + 1) * 16 + fr];
          const int cc = n0 - 2 * DM + (wn * NT + jp + (lane >> 5)) * 16 + fr, head = cc / DH, d = cc % DH;
#pragma unroll
          for (int i = 0; i < MT; ++i) {
            const int rl = wm * 64 + i * 16 + fg * 4; const int tok = m0 + rl;
            const f32x4 ma = *(const f32x4*)(rowst + rl), mb = *(const f32x4*)(rowst + rl + 2);
            const f32x4 &p0 = acc[i][jp], &p1 = acc[i][jp + 1];
            const u32x2 x = swap32(cvtpk(ma[1] * (p0[0] - ma[0] * c0), ma[3] * (p0[1] - ma[2] * c0)), cvtpk(ma[1] * (p1[0] - ma[0] * c1), ma[3] * (p1[1] - ma[2] * c1)));
            const u32x2 y = swap32(cvtpk(mb[1] * (p0[2] - mb[0] * c0), mb[3] * (p0[3] - mb[2] * c0)), cvtpk(mb[1] * (p1[2] - mb[0] * c1), mb[3] * (p1[3] - mb[2] * c1)));
            char* p = g.ws + WS_KV + (size_t)((head >> 1) * 16 + (tok >> 7)) * KVP_CHUNK + KVP_V + (head & 1) * 8192 + ((tok >> 4) & 7) * 1024 +
                      ((fg & 1) * 32 + d) * 16;
            *(u32x4*)p = (u32x4){x[0], y[0], x[1], y[1]};
          }
        }
      }
    }
    STAMP(KID, 5);
    STAMP(KID, 6);
  }
}

constexpr int ATT_NBUF = 4, ATT_HALF = 14336, ATT_STB = 2 * ATT_HALF, ATT_V = 6144;
struct AttnArgs { char* ws; const u16* wqkv; const u16* wo; const u16* wg; const u16* wu; const u16* wd; };

#define EX(x) __builtin_amdgcn_exp2f(x)
#define E4(P, I, S, J) P[I] = cvtpk(EX(S[J]), EX(S[J + 1])); P[I + 1] = cvtpk(EX(S[J + 2]), EX(S[J + 3]));
#define MF16(a, b, c) __builtin_amdgcn_mfma_f32_32x32x16_bf16(a, b, c, 0, 0, 0)
#define MF8(a, b, c) __builtin_amdgcn_mfma_f32_32x32x8bf16_1k(a, b, c, 0, 0, 0)
#define BC8(x) __builtin_bit_cast(bf16x8, x)

DI float rowmax16(const f32x16& s) {
  float m = s[0];
#pragma unroll
  for (int i = 1; i < 16; ++i) m = fmaxf(m, s[i]);
  return fmaxf(m, __shfl_xor(m, 32));
}
DI bool nonfinite12(const f32x16& o, int lane) {
  bool bad = false;
#pragma unroll
  for (int i = 0; i < 9; ++i) bad |= !(fabsf(o[i]) < 3.0e38f);
#pragma unroll
  for (int i = 9; i < 12; ++i) bad |= (lane < 32) && !(fabsf(o[i]) < 3.0e38f);
  return bad;
}

struct PackTask { const u16* src; int ld; char* dst; float* cs; };
DI PackTask pack_task(const AttnArgs& a, int t, int lane) {
  const int np = lane & 15, kg = lane >> 4;
  PackTask r;
  if (t > 4919) t = 4919;
  if (t < 2240) {
    const int jp = t % 7, kk = (t / 7) % 20, tn = t / 140;
    const int jb = jp * 2 + (np >> 3), e = (np & 7) * 2;
    int col = (tn >> 1) * 216 + (tn & 1) * 112 + jb * 8 + (e & 7); if (col > DF - 2) col = DF - 2;
    r.src = (e < 8 ? a.wg : a.wu) + (size_t)(kk * 32 + kg * 8) * DF + col; r.ld = DF;
    r.dst = a.ws + WS_PGU + (size_t)(((tn * 10 + (kk >> 1)) * 14 + jb) * 2 + (kk & 1)) * 1024 + (kg * 16 + e) * 16;
    r.cs = (float*)(a.ws + WS_CGU) + (tn * 20 + kk) * 224 + jb * 16 + e;
  } else if (t < 3440) {
    t -= 2240;
    const int jp = t % 60, kk = t / 60;
    const int jb = jp * 2 + (np >> 3), e = (np & 7) * 2, tn = jb >> 3;
    r.src = a.wqkv + (size_t)(kk * 32 + kg * 8) * (3 * DM) + jb * 16 + e; r.ld = 3 * DM;
    r.dst = a.ws + WS_PQKV + (size_t)(((tn * 10 + (kk >> 1)) * 8 + (jb & 7)) * 2 + (kk & 1)) * 1024 + (kg * 16 + e) * 16;
    r.cs = (float*)(a.ws + WS_CQKV) + (tn * 20 + kk) * 128 + (jb & 7) * 16 + e;
  } else {
    t -= 3440;
    const bool d = t >= 400; if (d) t -= 400;
    const int jp = t % 20, kk = t / 20;
    const int jb = jp * 2 + (np >> 3), e = (np & 7) * 2, tn = jb / 5;
    r.src = (d ? a.wd : a.wo) + (size_t)(kk * 32 + kg * 8) * DM + jb * 16 + e; r.ld = DM;
    r.dst = a.ws + (d ? WS_PWD : WS_PWO) + (size_t)(((tn * (d ? 27 : 10) + (kk >> 1)) * 5 + jb % 5) * 2 + (kk & 1)) * 1024 + (kg * 16 + e) * 16;
    r.cs = nullptr;
  }
  return r;
}

template <bool PACK>
__global__ __launch_bounds__(512) void attn_kernel(AttnArgs a) {
  __shared__ __attribute__((aligned(16))) char smem[ATT_NBUF * ATT_STB];
  const int tid = threadIdx.x, lane = tid & 63, wid = __builtin_amdgcn_readfirstlane(tid >> 6);
  char* ws = a.ws; char* wsdbg = ws;
  STAMP(1, 0);
  const int bid = blockIdx.x, xcd = bid & 7, idx = bid >> 3;
  const int head = xcd * 4 + (idx >> 3), qb = idx & 7;
  const int r32 = lane & 31, hh = lane >> 5, half = wid >> 2;
  const int q0 = qb * 256 + (wid & 3) * 64 + r32;
  const char* kvbase = ws + WS_KV + (size_t)(head >> 1) * 16 * KVP_CHUNK + lane * 16;
  int so[4];
#pragma unroll
  for (int i = 0; i < 4; ++i) {
    int pc = wid + i * 8; if (pc > 27) pc = 27;
    const int hf = pc >= 14, q = pc - hf * 14;
    so[i] = hf * 8 * KVP_CHUNK + (q < 4 ? (head & 1) * 4096 + q * 1024 : q < 6 ? 4096 + q * 1024 : KVP_V + (head & 1) * 8192 + (q - 6) * 1024);
  }
  auto stage = [&](int s, int buf) {
#pragma unroll
    for (int i = 0; i < 4; ++i) {
      int pc = wid + i * 8; if (pc > 27) pc = 27;
      glds16(kvbase + (size_t)s * KVP_CHUNK + so[i], smem + buf * ATT_STB + pc * 1024);
    }
  };
  u32x4 qr1[2]; u32x2 qr2[2];
  {
    const char* Q = ws + WS_Q + (size_t)((head >> 1) * 64 + (q0 >> 5)) * 2560;
#pragma unroll
    for (int i = 0; i < 2; ++i) {
      qr1[i] = gload16(Q + i * 2560 + (head & 1) * 1024 + lane * 16);
      qr2[i] = gload8(Q + i * 2560 + 2048 + r32 * 16 + (head & 1) * 8);
    }
  }
  stage(0, 0); stage(1, 1); stage(2, 2);
  constexpr int PK = PACK ? 24 : 0;
  unsigned pv[PACK ? 3 : 1][8];
  if (PACK) {
#pragma unroll
    for (int r = 0; r < 3; ++r) {
      const PackTask t = pack_task(a, bid * 8 + wid + r * 2048, lane);
#pragma unroll
      for (int e = 0; e < 8; ++e) pv[r][e] = *(const unsigned*)(t.src + (size_t)e * t.ld);
    }
    asm volatile("" ::: "memory");
  }
  const char* kl = smem + half * ATT_HALF + lane * 16;
  const char* k8 = smem + half * ATT_HALF + 4096 + r32 * 16 + (head & 1) * 8;
  f32x16 oa, ob, nma, nmb, sa, sb;
#pragma unroll
  for (int i = 0; i < 16; ++i) { oa[i] = 0.f; ob[i] = 0.f; }
  wait_vm<8 + PK>();
#pragma unroll
  for (int i = 0; i < 2; ++i) asm volatile("" : "+v"(qr1[i]), "+v"(qr2[i]));
  const bf16x8 qa1 = BC8(qr1[0]), qb1 = BC8(qr1[1]);
  const bf16x4 qa2 = __builtin_bit_cast(bf16x4, hh ? (u32x2){0u, 0u} : qr2[0]), qb2 = __builtin_bit_cast(bf16x4, hh ? (u32x2){0u, 0u} : qr2[1]);
  raw_barrier();
  bf16x8 k1 = *(const bf16x8*)kl; bf16x4 k2 = *(const bf16x4*)k8;
  {
    sa = MF16(k1, qa1, oa); sa = MF8(k2, qa2, sa);
    sb = MF16(k1, qb1, ob); sb = MF8(k2, qb2, sb);
    k1 = *(const bf16x8*)(kl + 1024); k2 = *(const bf16x4*)(k8 + 512);
    const float ma = rowmax16(sa), mb = rowmax16(sb);
#pragma unroll
    for (int i = 0; i < 16; ++i) { nma[i] = -ma; nmb[i] = -mb; sa[i] -= ma; sb[i] -= mb; }
  }
  STAMP(1, 2); CSTAMP(1, 2);
#pragma unroll 1
  for (int c = 0; c < 8; ++c) {
    const int bo = (c & 3) * ATT_STB, bn = ((c + 1) & 3) * ATT_STB;
#pragma unroll
    for (int t = 0; t < 4; ++t) {
      if (t == 2 && c < 7) {
        if (c < 2) wait_vm<4 + PK>(); else if (c < 6) wait_vm<4>(); else wait_vm<0>();
        raw_barrier();
        if (PACK && c == 1) {
#pragma unroll
          for (int r = 0; r < 3; ++r) {
            const PackTask pt = pack_task(a, bid * 8 + wid + r * 2048, lane);
            u32x4 lo, hi; float s0 = 0.f, s1 = 0.f;
#pragma unroll
            for (int m = 0; m < 4; ++m) {
              lo[m] = __builtin_amdgcn_perm(pv[r][2 * m + 1], pv[r][2 * m], 0x05040100u);
              hi[m] = __builtin_amdgcn_perm(pv[r][2 * m + 1], pv[r][2 * m], 0x07060302u);
              s0 = dot2ones(lo[m], s0); s1 = dot2ones(hi[m], s1);
            }
            *(u32x4*)pt.dst = lo; *(u32x4*)(pt.dst + 16) = hi;
            s0 += __shfl_xor(s0, 16); s1 += __shfl_xor(s1, 16);
            s0 += __shfl_xor(s0, 32); s1 += __shfl_xor(s1, 32);
            if (pt.cs != nullptr && lane < 16) *(float2*)pt.cs = make_float2(s0, s1);
          }
          asm volatile("" ::: "memory");
        }
        if (c < 5) stage(c + 3, (c + 3) & 3);
      }
      const char* vp = kl + bo + ATT_V + t * 2048;
      const bf16x8 v0 = *(const bf16x8*)vp, v1 = *(const bf16x8*)(vp + 1024);
      const int kb = (t < 2) ? bo : bn, kt = (t + 2) & 3;
      u32x4 p0, p1, p2, p3; f32x16 na, nb;
      SB();
      na = MF16(k1, qa1, nma); SB();
      E4(p0, 0, sa, 0) SB();
      na = MF8(k2, qa2, na); SB();
      E4(p0, 2, sa, 4) SB();
      oa = MF16(v0, BC8(p0), oa); SB();
      E4(p1, 0, sa, 8) SB();
      nb = MF16(k1, qb1, nmb);
      k1 = *(const bf16x8*)(kl + kb + kt * 1024); SB();
      E4(p1, 2, sa, 12) SB();
      oa = MF16(v1, BC8(p1), oa); SB();
      E4(p2, 0, sb, 0) SB();
      nb = MF8(k2, qb2, nb);
      k2 = *(const bf16x4*)(k8 + kb + kt * 512); SB();
      E4(p2, 2, sb, 4) SB();
      ob = MF16(v0, BC8(p2), ob); SB();
      E4(p3, 0, sb, 8) E4(p3, 2, sb, 12) SB();
      ob = MF16(v1, BC8(p3), ob); SB();
      sa = na; sb = nb;
    }
  }
  float ma = -nma[0], mb = -nmb[0];
  STAMP(1, 3); CSTAMP(1, 3);
  if (__syncthreads_or(nonfinite12(oa, lane) || nonfinite12(ob, lane))) {
    stage(0, 0); stage(1, 1); stage(2, 2);
#pragma unroll
    for (int i = 0; i < 16; ++i) { oa[i] = 0.f; ob[i] = 0.f; }
    ma = -3.0e38f; mb = -3.0e38f;
#pragma unroll 1
    for (int c = 0; c < 8; ++c) {
      if (c < 6) wait_vm<8>(); else if (c == 6) wait_vm<4>(); else wait_vm<0>();
      raw_barrier();
      if (c + 3 < 8) stage(c + 3, (c + 3) & 3);
      const int bo = (c & 3) * ATT_STB;
#pragma unroll 1
      for (int t = 0; t < 4; ++t) {
        k1 = *(const bf16x8*)(kl + bo + t * 1024); k2 = *(const bf16x4*)(k8 + bo + t * 512);
        const char* vp = kl + bo + ATT_V + t * 2048;
        const bf16x8 v0 = *(const bf16x8*)vp, v1 = *(const bf16x8*)(vp + 1024);
        auto one = [&](const bf16x8& q1, const bf16x4& q2, f32x16& o, float& m) {
          f32x16 s;
#pragma unroll
          for (int i = 0; i < 16; ++i) s[i] = 0.f;
          s = MF16(k1, q1, s); s = MF8(k2, q2, s);
          const float mn = fmaxf(m, rowmax16(s)), f = EX(m - mn);
          m = mn;
          u32x4 p0, p1;
#pragma unroll
          for (int i = 0; i < 16; ++i) { s[i] -= mn; o[i] *= f; }
          E4(p0, 0, s, 0) E4(p0, 2, s, 4) E4(p1, 0, s, 8) E4(p1, 2, s, 12)
          o = MF16(v0, BC8(p0), o); o = MF16(v1, BC8(p1), o);
        };
        one(qa1, qa2, oa, ma); one(qb1, qb2, ob, mb);
      }
    }
    __syncthreads();
  }
  STAMP(1, 4);
  float* xo = (float*)smem + (wid ^ 4) * 13 * 64 + lane;
  const float* xi = (const float*)smem + wid * 13 * 64 + lane;
#pragma unroll
  for (int i = 0; i < 12; ++i) xo[i * 64] = half ? oa[i] : ob[i];
  xo[12 * 64] = half ? ma : mb;
  __syncthreads();
  {
    const float m1 = half ? mb : ma, m2 = xi[12 * 64], M = fmaxf(m1, m2);
    const float f1 = EX(m1 - M), f2 = EX(m2 - M);
    float o[12];
#pragma unroll
    for (int i = 0; i < 12; ++i) o[i] = (half ? ob[i] : oa[i]) * f1 + xi[i * 64] * f2;
    const float l = __shfl(o[8], r32 + 32);
    const float inv = 1.0f / l;
    const int q = q0 + half * 32, c0 = head * DH + hh * 4;
    char* O = ws + WS_O;
    *(u32x2*)(O + foff<10>(q, c0)) = (u32x2){cvtpk(o[0] * inv, o[1] * inv), cvtpk(o[2] * inv, o[3] * inv)};
    *(u32x2*)(O + foff<10>(q, c0 + 8)) = (u32x2){cvtpk(o[4] * inv, o[5] * inv), cvtpk(o[6] * inv, o[7] * inv)};
    if (hh == 0) *(u32x2*)(O + foff<10>(q, c0 + 16)) = (u32x2){cvtpk(o[8] * inv, o[9] * inv), cvtpk(o[10] * inv, o[11] * inv)};
  }
  STAMP(1, 5);
  STAMP(1, 6);
}

extern "C" void kernel_launch(void* const* d_in, const int* in_sizes, int n_in, void* d_out, int out_size, void* d_ws,
                              size_t ws_size, hipStream_t stream) {
  const char* x0 = (const char*)d_in[0];
  char* ws = (char*)d_ws;
  AttnArgs at{ws, (const u16*)d_in[1], (const u16*)d_in[2], (const u16*)d_in[4], (const u16*)d_in[5], (const u16*)d_in[6]};
  for (int layer = 0; layer < 2; ++layer) {
    const char* xin = layer ? ws + WS_X2 : x0;
    char* xmid = ws + (layer ? WS_X3 : WS_X1);
    char* xout = layer ? (char*)d_out : ws + WS_X2;
    if (layer == 0) {
      GemmArgs a{xin, (const char*)d_in[1], nullptr, nullptr, ws, 0, 0};
      gemm_kernel<128, 128, 640, 4, EPI_QKV, true><<<256, 512, 0, stream>>>(a);
      attn_kernel<true><<<256, 512, 0, stream>>>(at);
    } else {
      GemmArgs a{xin, ws + WS_PQKV, nullptr, nullptr, ws, 0, 0};
      gemm_kernel<128, 128, 640, 4, EPI_QKV, false><<<256, 512, 0, stream>>>(a);
      attn_kernel<false><<<256, 512, 0, stream>>>(at);
    }
    GemmArgs b{ws + WS_O, ws + WS_PWO, xin, xmid, ws, layer == 0, 0};
    gemm_kernel<64, 80, 640, 8, EPI_RES, false><<<256, 512, 0, stream>>>(b);
    GemmArgs c{xmid, ws + WS_PGU, nullptr, ws + WS_H, ws, 0, 0};
    gemm_kernel<128, 224, 640, 3, EPI_GEGLU, false><<<256, 512, 0, stream>>>(c);
    GemmArgs d{ws + WS_H, ws + WS_PWD, xmid, xout, ws, 0, layer == 1};
    gemm_kernel<64, 80, 1728, 8, EPI_RES, false><<<256, 512, 0, stream>>>(d);
  }
}
```

```cpp
#include <hip/hip_runtime.h>
#include <hip/hip_bf16.h>
#include <stdint.h>

typedef __attribute__((ext_vector_type(8))) short bf16x8;
typedef __attribute__((ext_vector_type(4))) short bf16x4;
typedef __attribute__((ext_vector_type(4))) float f32x4;
typedef __attribute__((ext_vector_type(16))) float f32x16;
typedef __attribute__((ext_vector_type(4))) unsigned u32x4;
typedef __attribute__((ext_vector_type(2))) unsigned u32x2;
typedef unsigned short u16;

#define DI __device__ __forceinline__

constexpr int SEQ = 2048, DM = 640, NH = 32, DH = 20, DF = 1728;
constexpr float LN_EPS = 1e-5f;
constexpr float QSCALE = 0.22360679774997896f * 1.4426950408889634f;

constexpr size_t WS_X1   = 0;
constexpr size_t WS_X2   = WS_X1 + (size_t)SEQ * DM * 2;
constexpr size_t WS_X3   = WS_X2 + (size_t)SEQ * DM * 2;
constexpr size_t WS_Q    = WS_X3 + (size_t)SEQ * DM * 2;
constexpr size_t WS_O    = WS_Q + (size_t)SEQ * DM * 2;
constexpr size_t WS_H    = WS_O + (size_t)SEQ * DM * 2;
constexpr int    KVP_CHUNK = 26624, KVP_TAIL = 8192, KVP_V = 10240;
constexpr size_t WS_KV   = WS_H + (size_t)SEQ * DF * 2;
constexpr size_t WS_PWO  = WS_KV + (size_t)16 * 16 * KVP_CHUNK;
constexpr size_t WS_PWD  = WS_PWO + (size_t)DM * DM * 2;
constexpr size_t WS_PGU  = WS_PWD + (size_t)DF * DM * 2;
constexpr size_t WS_PQKV = WS_PGU + (size_t)16 * 10 * 28 * 1024;
constexpr size_t WS_CGU  = WS_PQKV + (size_t)DM * 3 * DM * 2;
constexpr size_t WS_CQKV = WS_CGU + (size_t)16 * 4 * 224 * 4;
constexpr size_t WS_STAT = WS_CQKV + (size_t)15 * 4 * 128 * 4;

typedef __attribute__((ext_vector_type(2))) __bf16 bf16p;
typedef __attribute__((ext_vector_type(2))) float f32x2;
DI unsigned cvtpk(float lo, float hi) { return __builtin_bit_cast(unsigned, __builtin_convertvector((f32x2){lo, hi}, bf16p)); }
DI float dot2ones(unsigned a, float s) {
  return __builtin_amdgcn_fdot2_f32_bf16(__builtin_bit_cast(bf16p, a), __builtin_bit_cast(bf16p, 0x3f803f80u), s, false);
}
DI float bflo(unsigned v) { return __uint_as_float(v << 16); }
DI float bfhi(unsigned v) { return __uint_as_float(v & 0xffff0000u); }
#define KIDARG(k)
#define STAMP(kid, i)
#define CSTAMP(kid, i)
template <int N> DI void wait_vm() { asm volatile("s_waitcnt vmcnt(%0)" ::"i"(N) : "memory"); }
DI void raw_barrier() {
  asm volatile("" ::: "memory");
  __builtin_amdgcn_s_waitcnt(0xC07F);
  __builtin_amdgcn_s_barrier();
  asm volatile("" ::: "memory");
}
DI u32x2 swap16(unsigned a, unsigned b) { return __builtin_amdgcn_permlane16_swap(a, b, false, false); }
DI u32x2 swap32(unsigned a, unsigned b) { return __builtin_amdgcn_permlane32_swap(a, b, false, false); }
DI bf16x4 tr_read(const char* p) {
  typedef short v4i16_t __attribute__((ext_vector_type(4)));
  return __builtin_bit_cast(bf16x4, __builtin_amdgcn_ds_read_tr16_b64_v4i16((__attribute__((address_space(3))) v4i16_t*)(p)));
}
DI void glds16(const void* g, const char* l) {
  unsigned sv; const unsigned lb = __builtin_amdgcn_readfirstlane((unsigned)(uintptr_t)l);
  asm volatile("s_mov_b32 %0, m0\n\ts_mov_b32 m0, %2\n\ts_nop 0\n\tglobal_load_lds_dwordx4 %1, off\n\ts_mov_b32 m0, %0"
               : "=&s"(sv) : "v"(g), "s"(lb) : "memory");
}
DI u32x2 gload8(const void* g) {
  u32x2 r; asm volatile("global_load_dwordx2 %0, %1, off" : "=v"(r) : "v"(g) : "memory"); return r;
}
DI unsigned gload4(const void* g) {
  unsigned r; asm volatile("global_load_dword %0, %1, off" : "=v"(r) : "v"(g) : "memory"); return r;
}
DI u32x4 gload16(const void* g) {
  u32x4 r; asm volatile("global_load_dwordx4 %0, %1, off" : "=v"(r) : "v"(g) : "memory"); return r;
}
#define ST_MODE 14
template <int CLS> DI void st16(void* p, u32x4 v) {
  if ((ST_MODE >> CLS) & 1) asm volatile("global_store_dwordx4 %0, %1, off sc1\n\ts_nop 1" ::"v"(p), "v"(v));
  else *(u32x4*)p = v;
}
template <int IMM, bool WT = true> DI void stw(const void* base, unsigned off, u32x4 v) {
  if (WT) asm volatile("global_store_dwordx4 %0, %1, %2 offset:%3 sc1\n\ts_nop 1" ::"v"(off), "v"(v), "s"(base), "i"(IMM));
  else asm volatile("global_store_dwordx4 %0, %1, %2 offset:%3\n\ts_nop 1" ::"v"(off), "v"(v), "s"(base), "i"(IMM));
}
template <int CLS> DI void st8(void* p, u32x2 v) {
  if ((ST_MODE >> CLS) & 1) asm volatile("global_store_dwordx2 %0, %1, off sc1" ::"v"(p), "v"(v) : "memory");
  else *(u32x2*)p = v;
}
template <int I> struct IC { static constexpr int v = I; };
template <int L, int NMAX> DI void wait_stages(int n) {
  if constexpr (NMAX == 0) wait_vm<0>();
  else { if (n >= NMAX) wait_vm<NMAX * L>(); else wait_stages<L, NMAX - 1>(n); }
}
template <int N, int I = 0, class F> DI void static_for(F&& f) {
  if constexpr (I < N) { f(IC<I>{}); static_for<N, I + 1>(f); }
}
#define SB() __builtin_amdgcn_sched_barrier(0)
template <int NS> DI unsigned foff(int row, int col) {
  return (unsigned)((((row >> 6) * NS + (col >> 6)) * 8 + ((row >> 4) & 3) * 2 + ((col >> 5) & 1)) * 1024 +
                    (((col >> 3) & 3) * 16 + (row & 15)) * 16 + (col & 7) * 2);
}

enum { EPI_QKV = 0, EPI_RES = 1, EPI_GEGLU = 2 };

struct GemmArgs {
  const char* A; const char* B;
  const char* res;
  char* out;
  char* ws;
  int res_raw, out_raw;
};

template <int BM, int BN, int KTOT, int NST, int EPI, bool RAW>
__global__ __launch_bounds__(512) void gemm_kernel(GemmArgs g) {
  constexpr bool RES = (EPI == EPI_RES), LNP = !RES && !RAW;
  constexpr int MT = RES ? 2 : 4, NT = RES ? 5 : BN / 32, KT = KTOT / 64;
  constexpr int PA = BM / 8, PB = BN / 8, P = PA + PB, LMAX = (P + 3) / 4;
  constexpr int ABYTES = BM * 128, STB = P * 1024;
  constexpr int RS = BN * 2;
  static_assert(PA % 4 == 0 && KT >= NST && NST >= 3, "config");
  __shared__ __attribute__((aligned(16))) char smem[NST * STB + BM * 8 + BN * 4];
  float2* rowst = (float2*)(smem + NST * STB);
  float* colsum = (float*)(smem + NST * STB + BM * 8);

  const int tid = threadIdx.x, lane = tid & 63, wid = __builtin_amdgcn_readfirstlane(tid >> 6);
  if (wid < 4) STAMP(KID, 0);
  const int xcd = blockIdx.x & 7, idx = blockIdx.x >> 3;
  int tm, tn;
#define MAPX 15
  constexpr int MB = (EPI == EPI_QKV) ? 0 : (EPI == EPI_GEGLU) ? 2 : (KTOT == 640) ? 1 : 3;
  if ((MAPX >> MB) & 1) {
    if (RES) { tm = xcd * 4 + (idx & 3); tn = idx >> 2; }
    else { tm = xcd * 2 + (idx & 1); tn = idx >> 1; }
  } else {
    if (RES) { tm = (xcd & 3) * 8 + (idx & 7); tn = (xcd >> 2) * 4 + (idx >> 3); }
    else { tm = (xcd & 1) * 8 + (idx & 7); tn = (xcd >> 1) * 4 + (idx >> 3); }
  }
  if (EPI == EPI_QKV && tn >= 15) return;
  const int m0 = tm * BM, n0 = tn * BN;
  const int fr = lane & 15, fg = lane >> 4;

  if (wid >= 4) {
    const int lw = wid - 4;
    const char* src[LMAX];
#pragma unroll
    for (int i = 0; i < LMAX; ++i) {
      const int p = lw + 4 * i;
      if (RAW) {
        if (i < PA / 4) {
          const int row = p * 8 + (lane >> 3), c = (lane & 7) ^ ((row >> 1) & 7);
          src[i] = g.A + ((size_t)(m0 + row) * KTOT + c * 8) * 2;
        } else {
          const int u = (p - PA) * 64 + lane, prow = u >> 4, c16 = u & 15;
          const int ch = (c16 >> 1) ^ (prow & 7);
          const int k = (prow & 32) + 8 * (((prow >> 3) & 1) * 2 + ((prow >> 2) & 1)) + 4 * ((prow >> 4) & 1) + (prow & 3);
          src[i] = g.B + ((size_t)k * (3 * DM) + n0 + ch * 16 + (c16 & 1) * 8) * 2;
        }
      } else {
        if (i < PA / 4) src[i] = g.A + ((size_t)((m0 >> 6) + (p >> 3)) * KT * 8 + (p & 7)) * 1024 + lane * 16;
        else src[i] = g.B + ((size_t)tn * KT * PB + (p - PA < PB ? p - PA : 0)) * 1024 + lane * 16;
      }
    }
    constexpr int STR_A = RAW ? 128 : 8192, STR_B = RAW ? 64 * 3 * DM * 2 : PB * 1024;
    auto body = [&](auto LC) {
      constexpr int L = decltype(LC)::v;
      auto issue = [&](int buf) {
        char* base = smem + buf * STB + lw * 1024;
#pragma unroll
        for (int i = 0; i < L; ++i) {
          glds16(src[i], base + i * 4096);
          src[i] += (i < PA / 4) ? STR_A : STR_B;
        }
      };
#define RAMP 2
      constexpr int R0 = RAMP < NST - 1 ? RAMP : NST - 1;
#pragma unroll
      for (int r = 0; r < R0; ++r) issue(r);
      int nxt = R0, ibuf = R0 % NST;
      constexpr int NMAIN = LNP ? KT - NST + 1 : KT;
      for (int i = 0; i < NMAIN; ++i) {
        wait_stages<L, NST - 1>(nxt - i - 1);
        raw_barrier();
        const int lim = min(KT, i + NST);
#pragma unroll
        for (int r = 0; r < 2; ++r)
          if (nxt < lim) { issue(ibuf); ++nxt; if (++ibuf == NST) ibuf = 0; }
      }
      if (LNP) {
        const int t = tid - 256;
        const float* cp = (const float*)(g.ws + (EPI == EPI_QKV ? WS_CQKV : WS_CGU)) + tn * 4 * BN + (t < BN ? t : 0);
        const char* sp = g.ws + WS_STAT + (size_t)(m0 + (t & 127)) * 64;
        unsigned c[4]; u32x4 st[4];
#pragma unroll
        for (int k = 0; k < 4; ++k) c[k] = gload4(cp + k * BN);
#pragma unroll
        for (int k = 0; k < 4; ++k) st[k] = gload16(sp + k * 16);
        static_for<NST - 1>([&](auto I) { wait_vm<(NST - 2 - decltype(I)::v) * L + 8>(); raw_barrier(); });
        wait_vm<0>();
        float cs = 0.f;
#pragma unroll
        for (int k = 0; k < 4; ++k) { asm volatile("" : "+v"(c[k])); cs += __uint_as_float(c[k]); }
#pragma unroll
        for (int k = 0; k < 4; ++k) asm volatile("" : "+v"(st[k]));
        const f32x4 sv = (__builtin_bit_cast(f32x4, st[0]) + __builtin_bit_cast(f32x4, st[1])) + (__builtin_bit_cast(f32x4, st[2]) + __builtin_bit_cast(f32x4, st[3]));
        const float mean = (sv[0] + sv[2]) * (1.0f / KTOT);
        const float var = fmaxf((sv[1] + sv[3]) * (1.0f / KTOT) - mean * mean, 0.f);
        if (t < BN) colsum[t] = cs;
        if (t >= 128) rowst[t - 128] = make_float2(mean, rsqrtf(var + LN_EPS));
        raw_barrier();
      }
    };
    if (P % 4 == 0 || lw < P % 4) body(IC<LMAX>{}); else body(IC<LMAX - 1>{});
    return;
  }

  const int wm = wid >> 1, wn = wid & 1;
#define PF 0
#define PFL 128
  constexpr int NTILE = RES ? 8 : (EPI == EPI_QKV ? 15 : 16), LA = 32768 / PFL, LB = PB * 1024 / PFL, LS = LA + NTILE * LB;
  constexpr int NPF = (!RAW && ((PF >> MB) & 1)) ? (KT * LS + 8191) / 8192 : 0;
  unsigned pfd[NPF ? NPF : 1];
#pragma unroll
  for (int r = 0; r < NPF; ++r) {
    const int id = min(((r * 4 + wid) * 32 + idx) * 64 + lane, KT * LS - 1), st = id / LS, l = id % LS, l2 = l - LA;
    const char* p = l2 < 0 ? g.A + (size_t)((xcd * 4 + l / (LA / 4)) * KT + st) * 8192 + (l % (LA / 4)) * PFL
                           : g.B + (size_t)((l2 / LB) * KT + st) * (PB * 1024) + (l2 % LB) * PFL;
    pfd[r] = gload4(p);
  }
  auto pf_done = [&]() {
    if (NPF) {
      wait_vm<0>();
#pragma unroll
      for (int r = 0; r < NPF; ++r) asm volatile("" : "+v"(pfd[r]));
    }
  };
  f32x4 acc[MT][NT];
#pragma unroll
  for (int i = 0; i < MT; ++i)
#pragma unroll
    for (int j = 0; j < NT; ++j) acc[i][j] = (f32x4){0.f, 0.f, 0.f, 0.f};

  if (RES) {
    const int row = m0 + wid * 16 + fr;
    u32x2 resv[NT];
#pragma unroll
    for (int j = 0; j < NT; ++j) {
      const int col = n0 + j * 16 + fg * 4;
      resv[j] = *(const u32x2*)(g.res + (g.res_raw ? (unsigned)(row * DM + col) * 2 : foff<10>(row, col)));
    }
    const int aoff = (wm * 4 + wn) * 1024 + lane * 16, boff = ABYTES + wn * 1024 + lane * 16;
    bf16x8 a0[2], b0[NT], a1[2], b1[NT];
    auto rd = [&](bf16x8(&a)[2], bf16x8(&b)[NT], const char* base) {
#pragma unroll
      for (int i = 0; i < 2; ++i) a[i] = *(const bf16x8*)(base + aoff + i * 2048);
#pragma unroll
      for (int j = 0; j < NT; ++j) b[j] = *(const bf16x8*)(base + boff + j * 2048);
      SB();
    };
    auto mm = [&](bf16x8(&a)[2], bf16x8(&b)[NT]) {
#pragma unroll
      for (int j = 0; j < NT; ++j)
#pragma unroll
        for (int i = 0; i < 2; ++i) acc[i][j] = __builtin_amdgcn_mfma_f32_16x16x32_bf16(b[j], a[i], acc[i][j], 0, 0, 0);
      SB();
    };
    STAMP(KID, 1);
    raw_barrier();
    STAMP(KID, 2); CSTAMP(KID, 2);
    rd(a0, b0, smem);
    int buf = 0;
#pragma unroll 1
    for (int kt = 0; kt + 2 < KT; kt += 2) {
      if (++buf == NST) buf = 0;
      raw_barrier();
      rd(a1, b1, smem + buf * STB);
      mm(a0, b0);
      if (++buf == NST) buf = 0;
      raw_barrier();
      rd(a0, b0, smem + buf * STB);
      mm(a1, b1);
    }
    if (KT % 2 == 0) {
      if (++buf == NST) buf = 0;
      raw_barrier();
      rd(a1, b1, smem + buf * STB);
      mm(a0, b0);
      mm(a1, b1);
    } else {
      mm(a0, b0);
    }
    STAMP(KID, 3); CSTAMP(KID, 3);
    pf_done();
    char* xb = smem + ((KT % NST) * STB);
#pragma unroll
    for (int j = 0; j < NT; ++j) *(f32x4*)(xb + ((wid ^ 1) * NT + j) * 1024 + lane * 16) = wn ? acc[0][j] : acc[1][j];
    raw_barrier();
    STAMP(KID, 4);
    float s = 0.f, q = 0.f;
    u32x2 v[NT];
#pragma unroll
    for (int j = 0; j < NT; ++j) {
      const f32x4 o = *(const f32x4*)(xb + (wid * NT + j) * 1024 + lane * 16) + (wn ? acc[1][j] : acc[0][j]);
      const u32x2 r = resv[j];
      v[j] = (u32x2){cvtpk(o[0] + bflo(r[0]), o[1] + bfhi(r[0])), cvtpk(o[2] + bflo(r[1]), o[3] + bfhi(r[1]))};
      const float e0 = bflo(v[j][0]), e1 = bfhi(v[j][0]), e2 = bflo(v[j][1]), e3 = bfhi(v[j][1]);
      s += (e0 + e1) + (e2 + e3);
      q += (e0 * e0 + e1 * e1) + (e2 * e2 + e3 * e3);
    }
#pragma unroll
    for (int jp = 0; jp < NT; jp += 2) {
      const int j1 = jp + 1 < NT ? jp + 1 : jp;
      const u32x2 x = swap16(v[jp][0], v[j1][0]), y = swap16(v[jp][1], v[j1][1]);
      const int col = n0 + (jp + (fg & 1)) * 16 + (fg >> 1) * 8;
      if (jp + 1 < NT || !(fg & 1))
        st16<0>(g.out + (g.out_raw ? (unsigned)(row * DM + col) * 2 : foff<10>(row, col)), (u32x4){x[0], y[0], x[1], y[1]});
    }
    {
      const u32x2 t1 = swap16(__float_as_uint(s), __float_as_uint(q));
      const unsigned t2 = __float_as_uint(__uint_as_float(t1[0]) + __uint_as_float(t1[1]));
      const u32x2 t3 = swap32(t2, t2);
      if (lane < 32) *(float*)(g.ws + WS_STAT + (size_t)(row * 8 + tn) * 8 + fg * 4) = __uint_as_float(t3[0]) + __uint_as_float(t3[1]);
    }
    STAMP(KID, 5);
    STAMP(KID, 6);
    return;
  } else {
    if (EPI == EPI_QKV && tn >= 10) {
      const int vc0 = n0 - 2 * DM, head = (vc0 + DH - 1) / DH + (tid >> 4);
      if (head * DH < vc0 + BN) {
        const int tok = m0 + ((tid >> 1) & 7) * 16;
        char* p = g.ws + WS_KV + (size_t)((head >> 1) * 16 + (tok >> 7)) * KVP_CHUNK + KVP_V + (head & 1) * 8192 + ((tok >> 4) & 7) * 1024 + ((tid & 1) * 32 + DH) * 16;
        *(u32x4*)p = (u32x4){0x3f803f80u, 0x3f803f80u, 0x3f803f80u, 0x3f803f80u};
      }
    }
    int aoff[2], boff[RAW ? NT : 1];
    const int pr = 8 * (fg >> 1) + 4 * (fg & 1) + (fr >> 2);
#pragma unroll
    for (int ks = 0; ks < 2; ++ks)
      aoff[ks] = RAW ? (wm * 64 + fr) * 128 + (((ks * 4 + fg) ^ (fr >> 1)) * 16) : (wm * 8 + ks) * 1024 + lane * 16;
    if (RAW) {
#pragma unroll
      for (int j = 0; j < NT; ++j) boff[j] = ABYTES + pr * RS + ((wn * NT + j) ^ (pr & 7)) * 32 + (lane & 3) * 8;
    } else boff[0] = ABYTES + wn * NT * 2048 + lane * 16;
    f32x4 srow[2], sgr[2], scol[2];
#pragma unroll
    for (int i = 0; i < 2; ++i) srow[i] = sgr[i] = scol[i] = (f32x4){0.f, 0.f, 0.f, 0.f};
    const bf16x8 ones = (bf16x8){0x3f80, 0x3f80, 0x3f80, 0x3f80, 0x3f80, 0x3f80, 0x3f80, 0x3f80};

    const bool swp = !(EPI == EPI_QKV && tn >= 10);
    unsigned so[2][2];
    if (EPI == EPI_QKV) {
      const bool isq = tn < DM / BN;
#pragma unroll
      for (int jq = 0; jq < 2; ++jq) {
        if (swp) {
          const int G = ((isq ? n0 : n0 - DM) >> 3) + (wn * NT + jq * 2 + (fg & 1)) * 2 + (fg >> 1), pair = G / 5, gi = G % 5;
          const int mo = (((gi + (gi > 2)) & 1) * 32 + fr) * 16;
          if (isq) {
            so[jq][0] = (unsigned)WS_Q + (pair * 64 + (m0 >> 5) + wm * 2) * 2560 + (gi == 2 ? 2048 + fr * 16 : (gi > 2) * 1024 + mo);
            so[jq][1] = so[jq][0] + 2560;
          } else {
            so[jq][0] = (unsigned)WS_KV + (pair * 16 + (m0 >> 7)) * KVP_CHUNK + (gi == 2 ? KVP_TAIL + (wm * 64 + fr) * 16 : (gi > 2) * 4096 + wm * 2048 + mo);
            so[jq][1] = so[jq][0] + (gi == 2 ? 512 : 1024);
          }
        } else {
          const int cc = n0 - 2 * DM + (wn * NT + jq * 2 + (lane >> 5)) * 16 + fr, head = cc / DH, d = cc % DH;
          so[jq][0] = (unsigned)WS_KV + ((head >> 1) * 16 + (m0 >> 7)) * KVP_CHUNK + KVP_V + (head & 1) * 8192 + wm * 4096 + ((fg & 1) * 32 + d) * 16;
          so[jq][1] = so[jq][0] + 2048;
        }
        asm volatile("" : "+v"(so[jq][0]), "+v"(so[jq][1]));
      }
    }
    auto mainloop = [&](auto SW, auto WN, auto WM) {
      constexpr bool SWP = decltype(SW)::v;
      constexpr int CWN = decltype(WN)::v, CWM = decltype(WM)::v;
      bf16x8 a0[MT], a1[MT], b0[NT], b1[NT];
      auto rdB = [&](const char* base, int j, int ks) {
        if (RAW) {
          const char* p = base + boff[RAW ? j : 0] + ks * 32 * RS;
          bf16x4 lo = tr_read(p), hi = tr_read(p + 16 * RS);
          return __builtin_shufflevector(lo, hi, 0, 1, 2, 3, 4, 5, 6, 7);
        } else return *(const bf16x8*)(base + boff[0] + j * 2048 + ks * 1024);
      };
      auto step = [&](bf16x8(&ac)[MT], bf16x8(&bc)[NT], bf16x8(&an)[MT], bf16x8(&bn)[NT], const char* nb, int nks, auto RD) {
        constexpr bool rd = decltype(RD)::v;
        constexpr int RPG = 3;
#pragma unroll
        for (int j = 0; j < NT; ++j) {
#pragma unroll
          for (int i = 0; i < MT; ++i) {
            acc[i][j] = SWP ? __builtin_amdgcn_mfma_f32_16x16x32_bf16(bc[j], ac[i], acc[i][j], 0, 0, 0)
                            : __builtin_amdgcn_mfma_f32_16x16x32_bf16(ac[i], bc[j], acc[i][j], 0, 0, 0);
            const int r = j * RPG + i;
            if (rd && i < RPG && r < MT + NT) {
              if (r == 0) bn[0] = rdB(nb, 0, nks);
              else if (r <= MT) an[r - 1] = *(const bf16x8*)(nb + aoff[nks] + (r - 1) * 2048);
              else bn[r - MT] = rdB(nb, r - MT, nks);
              SB();
            }
          }
          if (RAW) {
            if ((j >> 1) == CWM) scol[j & 1] = __builtin_amdgcn_mfma_f32_16x16x32_bf16(bc[j], ones, scol[j & 1], 0, 0, 0);
            else {
              srow[j & 1] = __builtin_amdgcn_mfma_f32_16x16x32_bf16(ones, ac[CWN * 2 + (j & 1)], srow[j & 1], 0, 0, 0);
              sgr[j & 1] = __builtin_amdgcn_mfma_f32_16x16x32_bf16(ac[CWN * 2 + (j & 1)], ac[CWN * 2 + (j & 1)], sgr[j & 1], 0, 0, 0);
            }
          }
          SB();
        }
      };
      STAMP(KID, 1);
      raw_barrier();
      STAMP(KID, 2); CSTAMP(KID, 2);
#pragma unroll
      for (int j = 0; j < NT; ++j) { b0[j] = rdB(smem, j, 0); if (j < MT) a0[j] = *(const bf16x8*)(smem + aoff[0] + j * 2048); }
      step(a0, b0, a1, b1, smem, 1, IC<1>{});
      int buf = 0;
#pragma unroll 1
      for (int kt = 1; kt < KT; ++kt) {
        if (++buf == NST) buf = 0;
        raw_barrier();
        step(a1, b1, a0, b0, smem + buf * STB, 0, IC<1>{});
        step(a0, b0, a1, b1, smem + buf * STB, 1, IC<1>{});
      }
      step(a1, b1, a0, b0, smem, 0, IC<0>{});
    };
    if (RAW) {
      if (swp) {
        if (wid == 0) mainloop(IC<1>{}, IC<0>{}, IC<0>{}); else if (wid == 1) mainloop(IC<1>{}, IC<1>{}, IC<0>{});
        else if (wid == 2) mainloop(IC<1>{}, IC<0>{}, IC<1>{}); else mainloop(IC<1>{}, IC<1>{}, IC<1>{});
      } else {
        if (wid == 0) mainloop(IC<0>{}, IC<0>{}, IC<0>{}); else if (wid == 1) mainloop(IC<0>{}, IC<1>{}, IC<0>{});
        else if (wid == 2) mainloop(IC<0>{}, IC<0>{}, IC<1>{}); else mainloop(IC<0>{}, IC<1>{}, IC<1>{});
      }
      STAMP(KID, 3); CSTAMP(KID, 3);
#pragma unroll
      for (int i = 0; i < 2; ++i) {
        const float sq = (fr & 2) ? ((fr & 1) ? sgr[i][3] : sgr[i][2]) : ((fr & 1) ? sgr[i][1] : sgr[i][0]);
        const float mean = srow[i][0] * (1.0f / KTOT);
        const float var = fmaxf(sq * (1.0f / KTOT) - mean * mean, 0.f);
        if (fg == (fr >> 2)) rowst[wm * 64 + (wn * 2 + i) * 16 + fr] = make_float2(mean, rsqrtf(var + LN_EPS));
        if (fr == 0) *(f32x4*)(colsum + (wn * NT + wm * 2 + i) * 16 + fg * 4) = scol[i];
      }
      raw_barrier();
    } else {
      if (swp) mainloop(IC<1>{}, IC<0>{}, IC<0>{}); else mainloop(IC<0>{}, IC<0>{}, IC<0>{});
      STAMP(KID, 3); CSTAMP(KID, 3);
      pf_done();
      raw_barrier();
    }
    STAMP(KID, 4);

    if (EPI == EPI_GEGLU) {
      const int hbase = (tn >> 1) * 216 + (tn & 1) * 112;
      const int orow = m0 + wm * 64 + fg * 16 + fr;
      constexpr float C1 = -2.0f * 0.7978845608028654f * 1.4426950408889634f, C2 = C1 * 0.044715f;
      f32x2 nm[MT], k1[MT], k2[MT], iv[MT];
#pragma unroll
      for (int i = 0; i < MT; ++i) {
        const float2 ms = rowst[wm * 64 + i * 16 + fr];
        const float r2 = ms.y * ms.y, ir2 = __builtin_amdgcn_rcpf(r2);
        nm[i] = (f32x2){-ms.x, -ms.x}; k1[i] = (f32x2){C1 * ms.y, C1 * ms.y}; k2[i] = (f32x2){C2 * ms.y * r2, C2 * ms.y * r2}; iv[i] = (f32x2){ir2, ir2};
      }
      f32x2 cgs[NT], cus[NT];
#pragma unroll
      for (int j = 0; j < NT; ++j) { cgs[j] = *(const f32x2*)(colsum + (wn * NT + j) * 16 + fg * 2); cus[j] = *(const f32x2*)(colsum + (wn * NT + j) * 16 + fg * 2 + 8); }
      const unsigned ro = foff<27>(orow, 0);
#pragma unroll
      for (int j = 0; j < NT; ++j) {
        const f32x2 cg = cgs[j], cu = cus[j];
        unsigned d[MT];
#pragma unroll
        for (int i = 0; i < MT; ++i) {
          const f32x2 gg = __builtin_elementwise_fma(nm[i], cg, (f32x2){acc[i][j][0], acc[i][j][1]});
          const f32x2 uu = __builtin_elementwise_fma(nm[i], cu, (f32x2){acc[i][j][2], acc[i][j][3]});
          const f32x2 t = gg * __builtin_elementwise_fma(gg * gg, k2[i], k1[i]);
          const f32x2 e = (f32x2){__builtin_amdgcn_exp2f(t[0]), __builtin_amdgcn_exp2f(t[1])};
          const f32x2 q = __builtin_elementwise_fma(e, iv[i], iv[i]);
          const f32x2 h = (gg * uu) * (f32x2){__builtin_amdgcn_rcpf(q[0]), __builtin_amdgcn_rcpf(q[1])};
          d[i] = cvtpk(h[0], h[1]);
        }
        const u32x2 x = swap32(d[0], d[2]), y = swap32(d[1], d[3]);
        const u32x2 z = swap16(x[0], y[0]), w = swap16(x[1], y[1]);
        const int jb = wn * NT + j;
        if (jb < 13 || !(tn & 1)) stw<0, (ST_MODE >> 1) & 1>(g.out, ro + foff<27>(0, hbase + jb * 8), (u32x4){z[0], z[1], w[0], w[1]});
      }
    } else {
      if (swp) {
        const float sc = tn < DM / BN ? QSCALE : 1.0f;
        float2 ms[MT]; f32x4 cs[NT];
#pragma unroll
        for (int i = 0; i < MT; ++i) ms[i] = rowst[wm * 64 + i * 16 + fr];
#pragma unroll
        for (int j = 0; j < NT; ++j) cs[j] = *(const f32x4*)(colsum + (wn * NT + j) * 16 + fg * 4);
        static_for<8>([&](auto I) {
          constexpr int jq = decltype(I)::v >> 2, i = decltype(I)::v & 3;
          const float rs = ms[i].y * sc;
          const f32x4 o0 = rs * (acc[i][jq * 2] - ms[i].x * cs[jq * 2]), o1 = rs * (acc[i][jq * 2 + 1] - ms[i].x * cs[jq * 2 + 1]);
          const u32x2 x = swap16(cvtpk(o0[0], o0[1]), cvtpk(o1[0], o1[1])), y = swap16(cvtpk(o0[2], o0[3]), cvtpk(o1[2], o1[3]));
          stw<(i & 1) * 256>(g.ws, so[jq][i >> 1], (u32x4){x[0], y[0], x[1], y[1]});
        });
      } else {
        f32x4 ma[MT], mb[MT]; float cs[NT];
#pragma unroll
        for (int i = 0; i < MT; ++i) { ma[i] = *(const f32x4*)(rowst + wm * 64 + i * 16 + fg * 4); mb[i] = *(const f32x4*)(rowst + wm * 64 + i * 16 + fg * 4 + 2); }
#pragma unroll
        for (int j = 0; j < NT; ++j) cs[j] = colsum[(wn * NT + j) * 16 + fr];
        static_for<8>([&](auto I) {
          constexpr int jq = decltype(I)::v >> 2, i = decltype(I)::v & 3;
          const float c0 = cs[jq * 2], c1 = cs[jq * 2 + 1];
          const f32x4 &p0 = acc[i][jq * 2], &p1 = acc[i][jq * 2 + 1], &a = ma[i], &b = mb[i];
          const u32x2 x = swap32(cvtpk(a[1] * (p0[0] - a[0] * c0), a[3] * (p0[1] - a[2] * c0)), cvtpk(a[1] * (p1[0] - a[0] * c1), a[3] * (p1[1] - a[2] * c1)));
          const u32x2 y = swap32(cvtpk(b[1] * (p0[2] - b[0] * c0), b[3] * (p0[3] - b[2] * c0)), cvtpk(b[1] * (p1[2] - b[0] * c1), b[3] * (p1[3] - b[2] * c1)));
          stw<(i & 1) * 1024>(g.ws, so[jq][i >> 1], (u32x4){x[0], y[0], x[1], y[1]});
        });
      }
    }
    STAMP(KID, 5);
    STAMP(KID, 6);
  }
}

constexpr int ATT_NBUF = 4, ATT_HALF = 14336, ATT_STB = 2 * ATT_HALF, ATT_V = 6144;
struct AttnArgs { char* ws; const u16* wqkv; const u16* wo; const u16* wg; const u16* wu; const u16* wd;
};

#define EX(x) __builtin_amdgcn_exp2f(x)
#define E4(P, I, S, J) P[I] = cvtpk(EX(S[J]), EX(S[J + 1])); P[I + 1] = cvtpk(EX(S[J + 2]), EX(S[J + 3]));
#define MF16(a, b, c) __builtin_amdgcn_mfma_f32_32x32x16_bf16(a, b, c, 0, 0, 0)
#define MF8(a, b, c) __builtin_amdgcn_mfma_f32_32x32x8bf16_1k(a, b, c, 0, 0, 0)
#define BC8(x) __builtin_bit_cast(bf16x8, x)

DI float rowmax16(const f32x16& s) {
  float m = s[0];
#pragma unroll
  for (int i = 1; i < 16; ++i) m = fmaxf(m, s[i]);
  return fmaxf(m, __shfl_xor(m, 32));
}
DI bool nonfinite12(const f32x16& o, int lane) {
  bool bad = false;
#pragma unroll
  for (int i = 0; i < 9; ++i) bad |= !(fabsf(o[i]) < 3.0e38f);
#pragma unroll
  for (int i = 9; i < 12; ++i) bad |= (lane < 32) && !(fabsf(o[i]) < 3.0e38f);
  return bad;
}

struct PackJob { const u16* src; int ld, kk0, kmax, s2; char* dst; float* cs; };
DI PackJob pack_job(const AttnArgs& a, int J, int wid, int lane) {
  const int prow = wid * 4 + (lane >> 4), c16 = lane & 15, ch = (c16 >> 1) ^ (prow & 7);
  const int k = 8 * (((prow >> 3) & 1) * 2 + ((prow >> 2) & 1)) + 4 * ((prow >> 4) & 1) + (prow & 3);
  const int fr = lane & 15, fg = lane >> 4;
  PackJob r;
  if (J >= 243) J -= 243;
  r.kmax = 19;
  if (J < 108) {
    const int cg = J >> 2, kq = J & 3, b = cg * 8 + wid, rr = b % 27, tn = (b / 27) * 2 + (rr >= 14), jb = rr - (rr >= 14) * 14;
    r.src = ((c16 & 1) ? a.wu : a.wg) + (size_t)k * DF + cg * 64 + ch * 8; r.ld = DF; r.kk0 = kq * 5; r.s2 = 28;
    r.dst = a.ws + WS_PGU + (size_t)(tn * 280 + jb * 2) * 1024 + (fg * 16 + ((fr & 7) >> 1) * 4 + (fr >> 3) * 2 + (fr & 1)) * 16;
    r.cs = (float*)(a.ws + WS_CGU) + (tn * 4 + kq) * 224 + jb * 16 + fg * 4;
  } else if (J < 168) {
    const int tn = (J - 108) >> 2, kq = (J - 108) & 3;
    r.src = a.wqkv + (size_t)k * (3 * DM) + tn * 128 + ch * 16 + (c16 & 1) * 8; r.ld = 3 * DM; r.kk0 = kq * 5; r.s2 = 16;
    r.dst = a.ws + WS_PQKV + (size_t)(tn * 160 + wid * 2) * 1024 + lane * 16;
    r.cs = (float*)(a.ws + WS_CQKV) + (tn * 4 + kq) * 128 + wid * 16 + fg * 4;
  } else {
    const bool d = J >= 188;
    const int cg = d ? (J - 188) / 11 : (J - 168) >> 2, kq = d ? (J - 188) % 11 : (J - 168) & 3, jb = cg * 8 + wid;
    r.src = (d ? a.wd : a.wo) + (size_t)k * DM + cg * 128 + ch * 16 + (c16 & 1) * 8; r.ld = DM; r.kk0 = kq * 5; r.s2 = 10;
    if (d) r.kmax = 53;
    r.dst = a.ws + (d ? WS_PWD : WS_PWO) + (size_t)((jb / 5) * (d ? 270 : 100) + (jb % 5) * 2) * 1024 + lane * 16;
    r.cs = nullptr;
  }
  return r;
}

template <bool PACK>
__global__ __launch_bounds__(512) void attn_kernel(AttnArgs a) {
  __shared__ __attribute__((aligned(16))) char smem[ATT_NBUF * ATT_STB + (PACK ? 5 * 8192 : 0)];
  const int tid = threadIdx.x, lane = tid & 63, wid = __builtin_amdgcn_readfirstlane(tid >> 6);
  char* ws = a.ws;
  STAMP(KID, 0);
  const int bid = blockIdx.x, xcd = bid & 7, idx = bid >> 3;
  const int head = xcd * 4 + (idx >> 3), qb = idx & 7;
  const int r32 = lane & 31, hh = lane >> 5, half = wid >> 2;
  const int q0 = qb * 256 + (wid & 3) * 64 + r32;
  const char* kvbase = ws + WS_KV + (size_t)(head >> 1) * 16 * KVP_CHUNK + lane * 16;
  int so[4];
#pragma unroll
  for (int i = 0; i < 4; ++i) {
    int pc = wid + i * 8; if (pc > 27) pc = 27;
    const int hf = pc >= 14, q = pc - hf * 14;
    so[i] = hf * 8 * KVP_CHUNK + (q < 4 ? (head & 1) * 4096 + q * 1024 : q < 6 ? 4096 + q * 1024 : KVP_V + (head & 1) * 8192 + (q - 6) * 1024);
  }
  auto stage = [&](int s, int buf) {
#pragma unroll
    for (int i = 0; i < 4; ++i) {
      int pc = wid + i * 8; if (pc > 27) pc = 27;
      glds16(kvbase + (size_t)s * KVP_CHUNK + so[i], smem + buf * ATT_STB + pc * 1024);
    }
  };
  u32x4 qr1[2]; u32x2 qr2[2];
  {
    const char* Q = ws + WS_Q + (size_t)((head >> 1) * 64 + (q0 >> 5)) * 2560;
#pragma unroll
    for (int i = 0; i < 2; ++i) {
      qr1[i] = gload16(Q + i * 2560 + (head & 1) * 1024 + lane * 16);
      qr2[i] = gload8(Q + i * 2560 + 2048 + r32 * 16 + (head & 1) * 8);
    }
  }
  stage(0, 0); stage(1, 1); stage(2, 2);
  PackJob pj; const char* pp; bf16x4 plo, phi; f32x4 pcs = (f32x4){0.f, 0.f, 0.f, 0.f};
  if (PACK) {
    pj = pack_job(a, bid, wid, lane);
    const int pr = 8 * (lane >> 5) + 4 * ((lane >> 4) & 1) + ((lane & 15) >> 2);
    pp = smem + ATT_NBUF * ATT_STB + pr * 256 + (wid ^ (pr & 7)) * 32 + (lane & 3) * 8;
  }
  const char* kl = smem + half * ATT_HALF + lane * 16;
  const char* k8 = smem + half * ATT_HALF + 4096 + r32 * 16 + (head & 1) * 8;
  f32x16 oa, ob, nma, nmb, sa, sb;
#pragma unroll
  for (int i = 0; i < 16; ++i) { oa[i] = 0.f; ob[i] = 0.f; }
  wait_vm<8>();
#pragma unroll
  for (int i = 0; i < 2; ++i) asm volatile("" : "+v"(qr1[i]), "+v"(qr2[i]));
  const bf16x8 qa1 = BC8(qr1[0]), qb1 = BC8(qr1[1]);
  const bf16x4 qa2 = __builtin_bit_cast(bf16x4, hh ? (u32x2){0u, 0u} : qr2[0]), qb2 = __builtin_bit_cast(bf16x4, hh ? (u32x2){0u, 0u} : qr2[1]);
  raw_barrier();
  bf16x8 k1 = *(const bf16x8*)kl; bf16x4 k2 = *(const bf16x4*)k8;
  {
    sa = MF16(k1, qa1, oa); sa = MF8(k2, qa2, sa);
    sb = MF16(k1, qb1, ob); sb = MF8(k2, qb2, sb);
    k1 = *(const bf16x8*)(kl + 1024); k2 = *(const bf16x4*)(k8 + 512);
    const float ma = rowmax16(sa), mb = rowmax16(sb);
#pragma unroll
    for (int i = 0; i < 16; ++i) { nma[i] = -ma; nmb[i] = -mb; sa[i] -= ma; sb[i] -= mb; }
  }
  STAMP(KID, 2); CSTAMP(KID, 2);
#pragma unroll 1
  for (int c = 0; c < 8; ++c) {
    const int bo = (c & 3) * ATT_STB, bn = ((c + 1) & 3) * ATT_STB;
#pragma unroll
    for (int t = 0; t < 4; ++t) {
      if (t == 2 && c < 7) {
        if (c == 6) wait_vm<0>(); else if (!PACK || c == 0) wait_vm<4>(); else if (c == 1) wait_vm<5>(); else wait_vm<6>();
        raw_barrier();
        if (c < 5) {
          stage(c + 3, (c + 3) & 3);
          if (PACK) glds16(pj.src + (size_t)min(pj.kk0 + c, pj.kmax) * 32 * pj.ld, smem + ATT_NBUF * ATT_STB + c * 8192 + wid * 1024);
        }
      }
      if (PACK && t == 2 && c >= 3) { plo = tr_read(pp + (c - 3) * 8192); phi = tr_read(pp + (c - 3) * 8192 + 4096); }
      if (PACK && t == 3 && c >= 3) {
        const bf16x8 f = __builtin_shufflevector(plo, phi, 0, 1, 2, 3, 4, 5, 6, 7);
        const int kk = min(pj.kk0 + c - 3, pj.kmax);
        st16<3>(pj.dst + ((kk >> 1) * pj.s2 + (kk & 1)) * 1024, __builtin_bit_cast(u32x4, f));
        pcs = __builtin_amdgcn_mfma_f32_16x16x32_bf16(f, (bf16x8){0x3f80, 0x3f80, 0x3f80, 0x3f80, 0x3f80, 0x3f80, 0x3f80, 0x3f80}, pcs, 0, 0, 0);
      }
      const char* vp = kl + bo + ATT_V + t * 2048;
      const bf16x8 v0 = *(const bf16x8*)vp, v1 = *(const bf16x8*)(vp + 1024);
      const int kb = (t < 2) ? bo : bn, kt = (t + 2) & 3;
      u32x4 p0, p1, p2, p3; f32x16 na, nb;
      SB();
      na = MF16(k1, qa1, nma); SB();
      E4(p0, 0, sa, 0) SB();
      na = MF8(k2, qa2, na); SB();
      E4(p0, 2, sa, 4) SB();
      oa = MF16(v0, BC8(p0), oa); SB();
      E4(p1, 0, sa, 8) SB();
      nb = MF16(k1, qb1, nmb);
      k1 = *(const bf16x8*)(kl + kb + kt * 1024); SB();
      E4(p1, 2, sa, 12) SB();
      oa = MF16(v1, BC8(p1), oa); SB();
      E4(p2, 0, sb, 0) SB();
      nb = MF8(k2, qb2, nb);
      k2 = *(const bf16x4*)(k8 + kb + kt * 512); SB();
      SB();
      E4(p2, 2, sb, 4) SB();
      ob = MF16(v0, BC8(p2), ob); SB();
      E4(p3, 0, sb, 8) E4(p3, 2, sb, 12) SB();
      ob = MF16(v1, BC8(p3), ob); SB();
      sa = na; sb = nb;
    }
  }
  if (PACK && pj.cs != nullptr && (lane & 15) == 0) *(f32x4*)pj.cs = pcs;
  float ma = -nma[0], mb = -nmb[0];
  STAMP(KID, 3); CSTAMP(KID, 3);
  if (__syncthreads_or(nonfinite12(oa, lane) || nonfinite12(ob, lane))) {
    stage(0, 0); stage(1, 1); stage(2, 2);
#pragma unroll
    for (int i = 0; i < 16; ++i) { oa[i] = 0.f; ob[i] = 0.f; }
    ma = -3.0e38f; mb = -3.0e38f;
#pragma unroll 1
    for (int c = 0; c < 8; ++c) {
      if (c < 6) wait_vm<8>(); else if (c == 6) wait_vm<4>(); else wait_vm<0>();
      raw_barrier();
      if (c + 3 < 8) stage(c + 3, (c + 3) & 3);
      const int bo = (c & 3) * ATT_STB;
#pragma unroll 1
      for (int t = 0; t < 4; ++t) {
        k1 = *(const bf16x8*)(kl + bo + t * 1024); k2 = *(const bf16x4*)(k8 + bo + t * 512);
        const char* vp = kl + bo + ATT_V + t * 2048;
        const bf16x8 v0 = *(const bf16x8*)vp, v1 = *(const bf16x8*)(vp + 1024);
        auto one = [&](const bf16x8& q1, const bf16x4& q2, f32x16& o, float& m) {
          f32x16 s;
#pragma unroll
          for (int i = 0; i < 16; ++i) s[i] = 0.f;
          s = MF16(k1, q1, s); s = MF8(k2, q2, s);
          const float mn = fmaxf(m, rowmax16(s)), f = EX(m - mn);
          m = mn;
          u32x4 p0, p1;
#pragma unroll
          for (int i = 0; i < 16; ++i) { s[i] -= mn; o[i] *= f; }
          E4(p0, 0, s, 0) E4(p0, 2, s, 4) E4(p1, 0, s, 8) E4(p1, 2, s, 12)
          o = MF16(v0, BC8(p0), o); o = MF16(v1, BC8(p1), o);
        };
        one(qa1, qa2, oa, ma); one(qb1, qb2, ob, mb);
      }
    }
    __syncthreads();
  }
  STAMP(KID, 4);
  float* xo = (float*)smem + (wid ^ 4) * 13 * 64 + lane;
  const float* xi = (const float*)smem + wid * 13 * 64 + lane;
#pragma unroll
  for (int i = 0; i < 12; ++i) xo[i * 64] = half ? oa[i] : ob[i];
  xo[12 * 64] = half ? ma : mb;
  __syncthreads();
  {
    const float m1 = half ? mb : ma, m2 = xi[12 * 64], M = fmaxf(m1, m2);
    const float f1 = EX(m1 - M), f2 = EX(m2 - M);
    float o[12];
#pragma unroll
    for (int i = 0; i < 12; ++i) o[i] = (half ? ob[i] : oa[i]) * f1 + xi[i * 64] * f2;
    const float l = __shfl(o[8], r32 + 32);
    const float inv = 1.0f / l;
    const int q = q0 + half * 32, c0 = head * DH + hh * 4;
    char* O = ws + WS_O;
    st8<4>(O + foff<10>(q, c0), (u32x2){cvtpk(o[0] * inv, o[1] * inv), cvtpk(o[2] * inv, o[3] * inv)});
    st8<4>(O + foff<10>(q, c0 + 8), (u32x2){cvtpk(o[4] * inv, o[5] * inv), cvtpk(o[6] * inv, o[7] * inv)});
    if (hh == 0) st8<4>(O + foff<10>(q, c0 + 16), (u32x2){cvtpk(o[8] * inv, o[9] * inv), cvtpk(o[10] * inv, o[11] * inv)});
  }
  STAMP(KID, 5);
  STAMP(KID, 6);
}

extern "C" void kernel_launch(void* const* d_in, const int* in_sizes, int n_in, void* d_out, int out_size, void* d_ws,
                              size_t ws_size, hipStream_t stream) {
  const char* x0 = (const char*)d_in[0];
  char* ws = (char*)d_ws;
  for (int layer = 0; layer < 2; ++layer) {
    AttnArgs at{ws, (const u16*)d_in[1], (const u16*)d_in[2], (const u16*)d_in[4], (const u16*)d_in[5], (const u16*)d_in[6] KIDARG(layer * 5 + 1)};
    const char* xin = layer ? ws + WS_X2 : x0;
    char* xmid = ws + (layer ? WS_X3 : WS_X1);
    char* xout = layer ? (char*)d_out : ws + WS_X2;
    if (layer == 0) {
      GemmArgs a{xin, (const char*)d_in[1], nullptr, nullptr, ws, 0, 0 KIDARG(0)};
      gemm_kernel<128, 128, 640, 4, EPI_QKV, true><<<256, 512, 0, stream>>>(a);
      attn_kernel<true><<<256, 512, 0, stream>>>(at);
    } else {
      GemmArgs a{xin, ws + WS_PQKV, nullptr, nullptr, ws, 0, 0 KIDARG(5)};
      gemm_kernel<128, 128, 640, 4, EPI_QKV, false><<<256, 512, 0, stream>>>(a);
      attn_kernel<false><<<256, 512, 0, stream>>>(at);
    }
    GemmArgs b{ws + WS_O, ws + WS_PWO, xin, xmid, ws, layer == 0, 0 KIDARG(layer * 5 + 2)};
    gemm_kernel<64, 80, 640, 8, EPI_RES, false><<<256, 512, 0, stream>>>(b);
    GemmArgs c{xmid, ws + WS_PGU, nullptr, ws + WS_H, ws, 0, 0 KIDARG(layer * 5 + 3)};
    gemm_kernel<128, 224, 640, 3, EPI_GEGLU, false><<<256, 512, 0, stream>>>(c);
    GemmArgs d{ws + WS_H, ws + WS_PWD, xmid, xout, ws, 0, layer == 1 KIDARG(layer * 5 + 4)};
    gemm_kernel<64, 80, 1728, 8, EPI_RES, false><<<256, 512, 0, stream>>>(d);
  }
}
```

```cpp
#include <hip/hip_runtime.h>
#include <hip/hip_bf16.h>
#include <stdint.h>

typedef __attribute__((ext_vector_type(8))) short bf16x8;
typedef __attribute__((ext_vector_type(4))) short bf16x4;
typedef __attribute__((ext_vector_type(4))) float f32x4;
typedef __attribute__((ext_vector_type(16))) float f32x16;
typedef __attribute__((ext_vector_type(4))) unsigned u32x4;
typedef __attribute__((ext_vector_type(2))) unsigned u32x2;
typedef unsigned short u16;

#define DI __device__ __forceinline__

constexpr int SEQ = 2048, DM = 640, NH = 32, DH = 20, DF = 1728;
constexpr float LN_EPS = 1e-5f;
constexpr float QSCALE = 0.22360679774997896f * 1.4426950408889634f;

constexpr size_t WS_X1   = 0;
constexpr size_t WS_X2   = WS_X1 + (size_t)SEQ * DM * 2;
constexpr size_t WS_X3   = WS_X2 + (size_t)SEQ * DM * 2;
constexpr size_t WS_Q    = WS_X3 + (size_t)SEQ * DM * 2;
constexpr size_t WS_O    = WS_Q + (size_t)SEQ * DM * 2;
constexpr size_t WS_H    = WS_O + (size_t)SEQ * DM * 2;
constexpr int    KVP_CHUNK = 26624, KVP_TAIL = 8192, KVP_V = 10240;
constexpr size_t WS_KV   = WS_H + (size_t)SEQ * DF * 2;
constexpr size_t WS_PWO  = WS_KV + (size_t)16 * 16 * KVP_CHUNK;
constexpr size_t WS_PWD  = WS_PWO + (size_t)DM * DM * 2;
constexpr size_t WS_PGU  = WS_PWD + (size_t)DF * DM * 2;
constexpr size_t WS_PQKV = WS_PGU + (size_t)16 * 10 * 28 * 1024;
constexpr size_t WS_CGU  = WS_PQKV + (size_t)DM * 3 * DM * 2;
constexpr size_t WS_CQKV = WS_CGU + (size_t)16 * 4 * 224 * 4;
constexpr size_t WS_STAT = WS_CQKV + (size_t)15 * 4 * 128 * 4;
constexpr size_t WS_END  = WS_STAT + (size_t)SEQ * 64;

typedef __attribute__((ext_vector_type(2))) __bf16 bf16p;
typedef __attribute__((ext_vector_type(2))) float f32x2;
DI unsigned cvtpk(float lo, float hi) { return __builtin_bit_cast(unsigned, __builtin_convertvector((f32x2){lo, hi}, bf16p)); }
DI float dot2ones(unsigned a, float s) {
  return __builtin_amdgcn_fdot2_f32_bf16(__builtin_bit_cast(bf16p, a), __builtin_bit_cast(bf16p, 0x3f803f80u), s, false);
}
DI float bflo(unsigned v) { return __uint_as_float(v << 16); }
DI float bfhi(unsigned v) { return __uint_as_float(v & 0xffff0000u); }
#define KIDARG(k)
#define STAMP(kid, i)
#define CSTAMP(kid, i)
template <int N> DI void wait_vm() { asm volatile("s_waitcnt vmcnt(%0)" ::"i"(N) : "memory"); }
DI void raw_barrier() {
  asm volatile("" ::: "memory");
  __builtin_amdgcn_s_waitcnt(0xC07F);
  __builtin_amdgcn_s_barrier();
  asm volatile("" ::: "memory");
}
DI u32x2 swap16(unsigned a, unsigned b) { return __builtin_amdgcn_permlane16_swap(a, b, false, false); }
DI u32x2 swap32(unsigned a, unsigned b) { return __builtin_amdgcn_permlane32_swap(a, b, false, false); }
DI bf16x4 tr_read(const char* p) {
  typedef short v4i16_t __attribute__((ext_vector_type(4)));
  return __builtin_bit_cast(bf16x4, __builtin_amdgcn_ds_read_tr16_b64_v4i16((__attribute__((address_space(3))) v4i16_t*)(p)));
}
DI void glds16(const void* g, const char* l) {
  unsigned sv; const unsigned lb = __builtin_amdgcn_readfirstlane((unsigned)(uintptr_t)l);
  asm volatile("s_mov_b32 %0, m0\n\ts_mov_b32 m0, %2\n\ts_nop 0\n\tglobal_load_lds_dwordx4 %1, off\n\ts_mov_b32 m0, %0"
               : "=&s"(sv) : "v"(g), "s"(lb) : "memory");
}
DI void gtouch(const void* g, const char* l) {
  unsigned sv; const unsigned lb = __builtin_amdgcn_readfirstlane((unsigned)(uintptr_t)l);
  asm volatile("s_mov_b32 %0, m0\n\ts_mov_b32 m0, %2\n\ts_nop 0\n\tglobal_load_lds_dword %1, off\n\ts_mov_b32 m0, %0"
               : "=&s"(sv) : "v"(g), "s"(lb) : "memory");
}
#define KLABEL(pfx, id) asm volatile(pfx "%c0:" ::"i"(id))
template <int ID, bool END> DI unsigned long long kaddr() {
  unsigned long long a;
  if (END) asm volatile("s_getpc_b64 s[20:21]\n\ts_add_u32 s20, s20, kend_%c1@rel32@lo+4\n\ts_addc_u32 s21, s21, kend_%c1@rel32@hi+12\n\ts_mov_b64 %0, s[20:21]" : "=s"(a) : "i"(ID) : "s20", "s21", "scc");
  else asm volatile("s_getpc_b64 s[20:21]\n\ts_add_u32 s20, s20, kbeg_%c1@rel32@lo+4\n\ts_addc_u32 s21, s21, kbeg_%c1@rel32@hi+12\n\ts_mov_b64 %0, s[20:21]" : "=s"(a) : "i"(ID) : "s20", "s21", "scc");
  return a;
}
template <int ID> DI void touch_code(const char* dummy, int lane) {
  const unsigned long long lo = kaddr<ID, false>() & ~255ull;
  unsigned long long hi = kaddr<ID, true>();
  if (ID < 5) { const unsigned long long e = kaddr<(ID < 5 ? ID + 7 : ID), true>(); hi = e > hi ? e : hi; }
  for (unsigned o = lane * 128; o < (unsigned)(hi - lo); o += 8192) gtouch((const char*)lo + o, dummy);
}
#define TC 1
template <size_t BASE, int TSTR, int LGT, int NT, int B0> DI unsigned tile_line(int t0, int id) {
  return (unsigned)BASE + B0 + (unsigned)(min(t0 + (id & ((1 << LGT) - 1)), NT - 1) * TSTR + (id >> LGT) * 128);
}
template <size_t BASE, int TSTR, int LGT, int NT, int B0, int B1, int NSL> DI void touch_tiles(const char* ws, const char* dummy, int t0, int slot, int lane) {
  constexpr int N = ((B1 - B0) / 128) << LGT;
#pragma unroll
  for (int r = 0; r < (N + NSL * 64 - 1) / (NSL * 64); ++r)
    gtouch(ws + tile_line<BASE, TSTR, LGT, NT, B0>(t0, min((r * NSL + slot) * 64 + lane, N - 1)), dummy);
}
#define CS0 0
#define CS1 0
#define CS2 0
#define CS3 0
#define CS4 0
#define PFW_WD 0
#define PFA_WO 0
#define PFA_GU 0
#define PFA_C 8
#define PFA_C0 11
#define PFW_GU 0
#define PFW_K 4
#define PFG_WD 0
#define PFG_K 8
#define PFD_Q 0
#define PFD_K 20
DI u32x2 gload8(const void* g) {
  u32x2 r; asm volatile("global_load_dwordx2 %0, %1, off" : "=v"(r) : "v"(g) : "memory"); return r;
}
DI unsigned gload4(const void* g) {
  unsigned r; asm volatile("global_load_dword %0, %1, off" : "=v"(r) : "v"(g) : "memory"); return r;
}
DI u32x4 gload16(const void* g) {
  u32x4 r; asm volatile("global_load_dwordx4 %0, %1, off" : "=v"(r) : "v"(g) : "memory"); return r;
}
#define ST_MODE 14
template <int CLS> DI void st16(void* p, u32x4 v) {
  if ((ST_MODE >> CLS) & 1) asm volatile("global_store_dwordx4 %0, %1, off sc1\n\ts_nop 1" ::"v"(p), "v"(v));
  else *(u32x4*)p = v;
}
template <int IMM, bool WT = true> DI void stw(const void* base, unsigned off, u32x4 v) {
  if (WT) asm volatile("global_store_dwordx4 %0, %1, %2 offset:%3 sc1\n\ts_nop 1" ::"v"(off), "v"(v), "s"(base), "i"(IMM));
  else asm volatile("global_store_dwordx4 %0, %1, %2 offset:%3\n\ts_nop 1" ::"v"(off), "v"(v), "s"(base), "i"(IMM));
}
template <int CLS> DI void st8(void* p, u32x2 v) {
  if ((ST_MODE >> CLS) & 1) asm volatile("global_store_dwordx2 %0, %1, off sc1" ::"v"(p), "v"(v) : "memory");
  else *(u32x2*)p = v;
}
template <int I> struct IC { static constexpr int v = I; };
template <int L, int NMAX> DI void wait_stages(int n) {
  if constexpr (NMAX == 0) wait_vm<0>();
  else { if (n >= NMAX) wait_vm<NMAX * L>(); else wait_stages<L, NMAX - 1>(n); }
}
template <int N, int I = 0, class F> DI void static_for(F&& f) {
  if constexpr (I < N) { f(IC<I>{}); static_for<N, I + 1>(f); }
}
#define SB() __builtin_amdgcn_sched_barrier(0)
template <int NS> DI unsigned foff(int row, int col) {
  return (unsigned)((((row >> 6) * NS + (col >> 6)) * 8 + ((row >> 4) & 3) * 2 + ((col >> 5) & 1)) * 1024 +
                    (((col >> 3) & 3) * 16 + (row & 15)) * 16 + (col & 7) * 2);
}

enum { EPI_QKV = 0, EPI_RES = 1, EPI_GEGLU = 2 };

struct GemmArgs {
  const char* A; const char* B;
  const char* res;
  char* out;
  char* ws;
  int res_raw, out_raw;
};

template <int BM, int BN, int KTOT, int NST, int EPI, bool RAW>
__global__ __launch_bounds__(512) void gemm_kernel(GemmArgs g) {
  constexpr bool RES = (EPI == EPI_RES), LNP = !RES && !RAW;
  constexpr int MT = RES ? 2 : 4, NT = RES ? 5 : BN / 32, KT = KTOT / 64;
  constexpr int PA = BM / 8, PB = BN / 8, P = PA + PB, LMAX = (P + 3) / 4;
  constexpr int ABYTES = BM * 128, STB = P * 1024;
  constexpr int RS = BN * 2;
  static_assert(PA % 4 == 0 && KT >= NST && NST >= 3, "config");
  __shared__ __attribute__((aligned(16))) char smem[NST * STB + BM * 8 + BN * 4 + 256];
  float2* rowst = (float2*)(smem + NST * STB);
  float* colsum = (float*)(smem + NST * STB + BM * 8);

  constexpr int KNUM = EPI == EPI_QKV ? (RAW ? 0 : 1) : EPI == EPI_GEGLU ? 2 : KTOT == 640 ? 3 : 4;
  KLABEL("kbeg_", KNUM);
  const int tid = threadIdx.x, lane = tid & 63, wid = __builtin_amdgcn_readfirstlane(tid >> 6);
  if (wid < 4) STAMP(KID, 0);
  const int xcd = blockIdx.x & 7, idx = blockIdx.x >> 3;
  constexpr int CS = KNUM == 0 ? CS0 : KNUM == 1 ? CS1 : KNUM == 2 ? CS2 : KNUM == 3 ? CS3 : CS4, LRT = (RES ? 2 : 1) + CS;
  const int tm = ((xcd & ((8 >> CS) - 1)) << LRT) + (idx & ((1 << LRT) - 1)), tn = (xcd >> (3 - CS)) * (32 >> LRT) + (idx >> LRT);
  if (EPI == EPI_QKV && tn >= 15) return;
  const int m0 = tm * BM, n0 = tn * BN;
  const int fr = lane & 15, fg = lane >> 4;

  if (wid >= 4) {
    const int lw = wid - 4;
    const char* src[LMAX];
#pragma unroll
    for (int i = 0; i < LMAX; ++i) {
      const int p = lw + 4 * i;
      if (RAW) {
        if (i < PA / 4) {
          const int row = p * 8 + (lane >> 3), c = (lane & 7) ^ ((row >> 1) & 7);
          src[i] = g.A + ((size_t)(m0 + row) * KTOT + c * 8) * 2;
        } else {
          const int u = (p - PA) * 64 + lane, prow = u >> 4, c16 = u & 15;
          const int ch = (c16 >> 1) ^ (prow & 7);
          const int k = (prow & 32) + 8 * (((prow >> 3) & 1) * 2 + ((prow >> 2) & 1)) + 4 * ((prow >> 4) & 1) + (prow & 3);
          src[i] = g.B + ((size_t)k * (3 * DM) + n0 + ch * 16 + (c16 & 1) * 8) * 2;
        }
      } else {
        if (i < PA / 4) src[i] = g.A + ((size_t)((m0 >> 6) + (p >> 3)) * KT * 8 + (p & 7)) * 1024 + lane * 16;
        else src[i] = g.B + ((size_t)tn * KT * PB + (p - PA < PB ? p - PA : 0)) * 1024 + lane * 16;
      }
    }
    constexpr int STR_A = RAW ? 128 : 8192, STR_B = RAW ? 64 * 3 * DM * 2 : PB * 1024;
    auto body = [&](auto LC) {
      constexpr int L = decltype(LC)::v;
      auto issue = [&](int buf) {
        char* base = smem + buf * STB + lw * 1024;
#pragma unroll
        for (int i = 0; i < L; ++i) {
          glds16(src[i], base + i * 4096);
          src[i] += (i < PA / 4) ? STR_A : STR_B;
        }
      };
#define RAMP 2
      constexpr int R0 = RAMP < NST - 1 ? RAMP : NST - 1;
#pragma unroll
      for (int r = 0; r < R0; ++r) issue(r);
      int nxt = R0, ibuf = R0 % NST;
      constexpr int NMAIN = LNP ? KT - NST + 1 : KT;
      for (int i = 0; i < NMAIN; ++i) {
        wait_stages<L, NST - 1>(nxt - i - 1);
        raw_barrier();
        const int lim = min(KT, i + NST);
#pragma unroll
        for (int r = 0; r < 2; ++r)
          if (nxt < lim) { issue(ibuf); ++nxt; if (++ibuf == NST) ibuf = 0; }
      }
      if (LNP) {
        const int t = tid - 256;
        const float* cp = (const float*)(g.ws + (EPI == EPI_QKV ? WS_CQKV : WS_CGU)) + tn * 4 * BN + (t < BN ? t : 0);
        const char* sp = g.ws + WS_STAT + (size_t)(m0 + (t & 127)) * 64;
        unsigned c[4]; u32x4 st[4];
#pragma unroll
        for (int k = 0; k < 4; ++k) c[k] = gload4(cp + k * BN);
#pragma unroll
        for (int k = 0; k < 4; ++k) st[k] = gload16(sp + k * 16);
        static_for<NST - 1>([&](auto I) { wait_vm<(NST - 2 - decltype(I)::v) * L + 8>(); raw_barrier(); });
        wait_vm<0>();
        float cs = 0.f;
#pragma unroll
        for (int k = 0; k < 4; ++k) { asm volatile("" : "+v"(c[k])); cs += __uint_as_float(c[k]); }
#pragma unroll
        for (int k = 0; k < 4; ++k) asm volatile("" : "+v"(st[k]));
        const f32x4 sv = (__builtin_bit_cast(f32x4, st[0]) + __builtin_bit_cast(f32x4, st[1])) + (__builtin_bit_cast(f32x4, st[2]) + __builtin_bit_cast(f32x4, st[3]));
        const float mean = (sv[0] + sv[2]) * (1.0f / KTOT);
        const float var = fmaxf((sv[1] + sv[3]) * (1.0f / KTOT) - mean * mean, 0.f);
        if (t < BN) colsum[t] = cs;
        if (t >= 128) rowst[t - 128] = make_float2(mean, rsqrtf(var + LN_EPS));
        raw_barrier();
      }
    };
    if (P % 4 == 0 || lw < P % 4) body(IC<LMAX>{}); else body(IC<LMAX - 1>{});
    KLABEL("kend_", KNUM + 7);
    return;
  }

  const int wm = wid >> 1, wn = wid & 1;
  const char* dummy = smem + NST * STB + BM * 8 + BN * 4;
  constexpr int PFK = KNUM == 2 ? PFG_K : KNUM == 3 ? PFW_K : KNUM == 4 ? PFD_K : 7;
  auto pf_next = [&]() {
    constexpr int NEXT = KNUM == 0 ? 6 : KNUM == 1 ? 5 : KNUM == 2 ? 4 : KNUM == 3 ? 2 : 1;
    if (KNUM == 4 && g.out_raw) return;
    if (TC && wid == 0) touch_code<NEXT>(dummy, lane);
    const int sl = idx * 4 + wid;
    if (KNUM == 2 && PFG_WD > PFW_WD) touch_tiles<WS_PWD, 27 * 10240, 3 - CS4, 8, PFW_WD * 10240, (PFG_WD > PFW_WD ? PFG_WD : PFW_WD + 1) * 10240, 128>(g.ws, dummy, (xcd >> (3 - CS4)) * (8 >> CS4), sl, lane);
    if (KNUM == 3 && PFW_GU > PFA_GU) touch_tiles<WS_PGU, 10 * 28672, 4 - CS2, 16, PFA_GU * 28672, (PFW_GU > PFA_GU ? PFW_GU : PFA_GU + 1) * 28672, 128>(g.ws, dummy, (xcd >> (3 - CS2)) * (16 >> CS2), sl, lane);
    if (KNUM == 3 && PFW_WD) touch_tiles<WS_PWD, 27 * 10240, 3 - CS4, 8, 0, (PFW_WD ? PFW_WD : 1) * 10240, 128>(g.ws, dummy, (xcd >> (3 - CS4)) * (8 >> CS4), sl, lane);
    if (KNUM == 4 && PFD_Q) touch_tiles<WS_PQKV, 10 * 16384, 4 - CS1, 15, 0, (PFD_Q ? PFD_Q : 1) * 16384, 128>(g.ws, dummy, (xcd >> (3 - CS1)) * (16 >> CS1), sl, lane);
  };
  f32x4 acc[MT][NT];
#pragma unroll
  for (int i = 0; i < MT; ++i)
#pragma unroll
    for (int j = 0; j < NT; ++j) acc[i][j] = (f32x4){0.f, 0.f, 0.f, 0.f};

  if (RES) {
    const int row = m0 + wid * 16 + fr;
    u32x2 resv[NT];
#pragma unroll
    for (int j = 0; j < NT; ++j) {
      const int col = n0 + j * 16 + fg * 4;
      resv[j] = *(const u32x2*)(g.res + (g.res_raw ? (unsigned)(row * DM + col) * 2 : foff<10>(row, col)));
    }
    const int aoff = (wm * 4 + wn) * 1024 + lane * 16, boff = ABYTES + wn * 1024 + lane * 16;
    bf16x8 a0[2], b0[NT], a1[2], b1[NT];
    auto rd = [&](bf16x8(&a)[2], bf16x8(&b)[NT], const char* base) {
#pragma unroll
      for (int i = 0; i < 2; ++i) a[i] = *(const bf16x8*)(base + aoff + i * 2048);
#pragma unroll
      for (int j = 0; j < NT; ++j) b[j] = *(const bf16x8*)(base + boff + j * 2048);
      SB();
    };
    auto mm = [&](bf16x8(&a)[2], bf16x8(&b)[NT]) {
#pragma unroll
      for (int j = 0; j < NT; ++j)
#pragma unroll
        for (int i = 0; i < 2; ++i) acc[i][j] = __builtin_amdgcn_mfma_f32_16x16x32_bf16(b[j], a[i], acc[i][j], 0, 0, 0);
      SB();
    };
    STAMP(KID, 1);
    raw_barrier();
    STAMP(KID, 2); CSTAMP(KID, 2);
    rd(a0, b0, smem);
    int buf = 0;
#pragma unroll 1
    for (int kt = 0; kt + 2 < KT; kt += 2) {
      if (++buf == NST) buf = 0;
      if (kt == (PFK & ~1)) pf_next();
      raw_barrier();
      rd(a1, b1, smem + buf * STB);
      mm(a0, b0);
      if (++buf == NST) buf = 0;
      raw_barrier();
      rd(a0, b0, smem + buf * STB);
      mm(a1, b1);
    }
    if (KT % 2 == 0) {
      if (++buf == NST) buf = 0;
      raw_barrier();
      rd(a1, b1, smem + buf * STB);
      mm(a0, b0);
      mm(a1, b1);
    } else {
      mm(a0, b0);
    }
    STAMP(KID, 3); CSTAMP(KID, 3);
    char* xb = smem + ((KT % NST) * STB);
#pragma unroll
    for (int j = 0; j < NT; ++j) *(f32x4*)(xb + ((wid ^ 1) * NT + j) * 1024 + lane * 16) = wn ? acc[0][j] : acc[1][j];
    raw_barrier();
    STAMP(KID, 4);
    float s = 0.f, q = 0.f;
    u32x2 v[NT];
#pragma unroll
    for (int j = 0; j < NT; ++j) {
      const f32x4 o = *(const f32x4*)(xb + (wid * NT + j) * 1024 + lane * 16) + (wn ? acc[1][j] : acc[0][j]);
      const u32x2 r = resv[j];
      v[j] = (u32x2){cvtpk(o[0] + bflo(r[0]), o[1] + bfhi(r[0])), cvtpk(o[2] + bflo(r[1]), o[3] + bfhi(r[1]))};
      const float e0 = bflo(v[j][0]), e1 = bfhi(v[j][0]), e2 = bflo(v[j][1]), e3 = bfhi(v[j][1]);
      s += (e0 + e1) + (e2 + e3);
      q += (e0 * e0 + e1 * e1) + (e2 * e2 + e3 * e3);
    }
#pragma unroll
    for (int jp = 0; jp < NT; jp += 2) {
      const int j1 = jp + 1 < NT ? jp + 1 : jp;
      const u32x2 x = swap16(v[jp][0], v[j1][0]), y = swap16(v[jp][1], v[j1][1]);
      const int col = n0 + (jp + (fg & 1)) * 16 + (fg >> 1) * 8;
      if (jp + 1 < NT || !(fg & 1))
        st16<0>(g.out + (g.out_raw ? (unsigned)(row * DM + col) * 2 : foff<10>(row, col)), (u32x4){x[0], y[0], x[1], y[1]});
    }
    {
      const u32x2 t1 = swap16(__float_as_uint(s), __float_as_uint(q));
      const unsigned t2 = __float_as_uint(__uint_as_float(t1[0]) + __uint_as_float(t1[1]));
      const u32x2 t3 = swap32(t2, t2);
      if (lane < 32) *(float*)(g.ws + WS_STAT + (size_t)(row * 8 + tn) * 8 + fg * 4) = __uint_as_float(t3[0]) + __uint_as_float(t3[1]);
    }
    STAMP(KID, 5);
    wait_vm<0>();
    STAMP(KID, 6);
    KLABEL("kend_", KNUM);
    return;
  } else {
    if (EPI == EPI_QKV && tn >= 10) {
      const int vc0 = n0 - 2 * DM, head = (vc0 + DH - 1) / DH + (tid >> 4);
      if (head * DH < vc0 + BN) {
        const int tok = m0 + ((tid >> 1) & 7) * 16;
        char* p = g.ws + WS_KV + (size_t)((head >> 1) * 16 + (tok >> 7)) * KVP_CHUNK + KVP_V + (head & 1) * 8192 + ((tok >> 4) & 7) * 1024 + ((tid & 1) * 32 + DH) * 16;
        *(u32x4*)p = (u32x4){0x3f803f80u, 0x3f803f80u, 0x3f803f80u, 0x3f803f80u};
      }
    }
    int aoff[2], boff[RAW ? NT : 1];
    const int pr = 8 * (fg >> 1) + 4 * (fg & 1) + (fr >> 2);
#pragma unroll
    for (int ks = 0; ks < 2; ++ks)
      aoff[ks] = RAW ? (wm * 64 + fr) * 128 + (((ks * 4 + fg) ^ (fr >> 1)) * 16) : (wm * 8 + ks) * 1024 + lane * 16;
    if (RAW) {
#pragma unroll
      for (int j = 0; j < NT; ++j) boff[j] = ABYTES + pr * RS + ((wn * NT + j) ^ (pr & 7)) * 32 + (lane & 3) * 8;
    } else boff[0] = ABYTES + wn * NT * 2048 + lane * 16;
    f32x4 srow[2], sgr[2], scol[2];
#pragma unroll
    for (int i = 0; i < 2; ++i) srow[i] = sgr[i] = scol[i] = (f32x4){0.f, 0.f, 0.f, 0.f};
    const bf16x8 ones = (bf16x8){0x3f80, 0x3f80, 0x3f80, 0x3f80, 0x3f80, 0x3f80, 0x3f80, 0x3f80};

    const bool swp = !(EPI == EPI_QKV && tn >= 10);
    unsigned so[2][2];
    if (EPI == EPI_QKV) {
      const bool isq = tn < DM / BN;
#pragma unroll
      for (int jq = 0; jq < 2; ++jq) {
        if (swp) {
          const int G = ((isq ? n0 : n0 - DM) >> 3) + (wn * NT + jq * 2 + (fg & 1)) * 2 + (fg >> 1), pair = G / 5, gi = G % 5;
          const int mo = (((gi + (gi > 2)) & 1) * 32 + fr) * 16;
          if (isq) {
            so[jq][0] = (unsigned)WS_Q + (pair * 64 + (m0 >> 5) + wm * 2) * 2560 + (gi == 2 ? 2048 + fr * 16 : (gi > 2) * 1024 + mo);
            so[jq][1] = so[jq][0] + 2560;
          } else {
            so[jq][0] = (unsigned)WS_KV + (pair * 16 + (m0 >> 7)) * KVP_CHUNK + (gi == 2 ? KVP_TAIL + (wm * 64 + fr) * 16 : (gi > 2) * 4096 + wm * 2048 + mo);
            so[jq][1] = so[jq][0] + (gi == 2 ? 512 : 1024);
          }
        } else {
          const int cc = n0 - 2 * DM + (wn * NT + jq * 2 + (lane >> 5)) * 16 + fr, head = cc / DH, d = cc % DH;
          so[jq][0] = (unsigned)WS_KV + ((head >> 1) * 16 + (m0 >> 7)) * KVP_CHUNK + KVP_V + (head & 1) * 8192 + wm * 4096 + ((fg & 1) * 32 + d) * 16;
          so[jq][1] = so[jq][0] + 2048;
        }
        asm volatile("" : "+v"(so[jq][0]), "+v"(so[jq][1]));
      }
    }
    auto mainloop = [&](auto SW, auto WN, auto WM) {
      constexpr bool SWP = decltype(SW)::v;
      constexpr int CWN = decltype(WN)::v, CWM = decltype(WM)::v;
      bf16x8 a0[MT], a1[MT], b0[NT], b1[NT];
      auto rdB = [&](const char* base, int j, int ks) {
        if (RAW) {
          const char* p = base + boff[RAW ? j : 0] + ks * 32 * RS;
          bf16x4 lo = tr_read(p), hi = tr_read(p + 16 * RS);
          return __builtin_shufflevector(lo, hi, 0, 1, 2, 3, 4, 5, 6, 7);
        } else return *(const bf16x8*)(base + boff[0] + j * 2048 + ks * 1024);
      };
      auto step = [&](bf16x8(&ac)[MT], bf16x8(&bc)[NT], bf16x8(&an)[MT], bf16x8(&bn)[NT], const char* nb, int nks, auto RD) {
        constexpr bool rd = decltype(RD)::v;
        constexpr int RPG = 3;
#pragma unroll
        for (int j = 0; j < NT; ++j) {
#pragma unroll
          for (int i = 0; i < MT; ++i) {
            acc[i][j] = SWP ? __builtin_amdgcn_mfma_f32_16x16x32_bf16(bc[j], ac[i], acc[i][j], 0, 0, 0)
                            : __builtin_amdgcn_mfma_f32_16x16x32_bf16(ac[i], bc[j], acc[i][j], 0, 0, 0);
            const int r = j * RPG + i;
            if (rd && i < RPG && r < MT + NT) {
              if (r == 0) bn[0] = rdB(nb, 0, nks);
              else if (r <= MT) an[r - 1] = *(const bf16x8*)(nb + aoff[nks] + (r - 1) * 2048);
              else bn[r - MT] = rdB(nb, r - MT, nks);
              SB();
            }
          }
          if (RAW) {
            if ((j >> 1) == CWM) scol[j & 1] = __builtin_amdgcn_mfma_f32_16x16x32_bf16(bc[j], ones, scol[j & 1], 0, 0, 0);
            else {
              srow[j & 1] = __builtin_amdgcn_mfma_f32_16x16x32_bf16(ones, ac[CWN * 2 + (j & 1)], srow[j & 1], 0, 0, 0);
              sgr[j & 1] = __builtin_amdgcn_mfma_f32_16x16x32_bf16(ac[CWN * 2 + (j & 1)], ac[CWN * 2 + (j & 1)], sgr[j & 1], 0, 0, 0);
            }
          }
          SB();
        }
      };
      STAMP(KID, 1);
      raw_barrier();
      STAMP(KID, 2); CSTAMP(KID, 2);
#pragma unroll
      for (int j = 0; j < NT; ++j) { b0[j] = rdB(smem, j, 0); if (j < MT) a0[j] = *(const bf16x8*)(smem + aoff[0] + j * 2048); }
      step(a0, b0, a1, b1, smem, 1, IC<1>{});
      int buf = 0;
#pragma unroll 1
      for (int kt = 1; kt < KT; ++kt) {
        if (++buf == NST) buf = 0;
        if (EPI == EPI_GEGLU && kt == PFK) pf_next();
        raw_barrier();
        step(a1, b1, a0, b0, smem + buf * STB, 0, IC<1>{});
        step(a0, b0, a1, b1, smem + buf * STB, 1, IC<1>{});
      }
      step(a1, b1, a0, b0, smem, 0, IC<0>{});
    };
    if (RAW) {
      if (swp) {
        if (wid == 0) mainloop(IC<1>{}, IC<0>{}, IC<0>{}); else if (wid == 1) mainloop(IC<1>{}, IC<1>{}, IC<0>{});
        else if (wid == 2) mainloop(IC<1>{}, IC<0>{}, IC<1>{}); else mainloop(IC<1>{}, IC<1>{}, IC<1>{});
      } else {
        if (wid == 0) mainloop(IC<0>{}, IC<0>{}, IC<0>{}); else if (wid == 1) mainloop(IC<0>{}, IC<1>{}, IC<0>{});
        else if (wid == 2) mainloop(IC<0>{}, IC<0>{}, IC<1>{}); else mainloop(IC<0>{}, IC<1>{}, IC<1>{});
      }
      STAMP(KID, 3); CSTAMP(KID, 3);
      pf_next();
#pragma unroll
      for (int i = 0; i < 2; ++i) {
        const float sq = (fr & 2) ? ((fr & 1) ? sgr[i][3] : sgr[i][2]) : ((fr & 1) ? sgr[i][1] : sgr[i][0]);
        const float mean = srow[i][0] * (1.0f / KTOT);
        const float var = fmaxf(sq * (1.0f / KTOT) - mean * mean, 0.f);
        if (fg == (fr >> 2)) rowst[wm * 64 + (wn * 2 + i) * 16 + fr] = make_float2(mean, rsqrtf(var + LN_EPS));
        if (fr == 0) *(f32x4*)(colsum + (wn * NT + wm * 2 + i) * 16 + fg * 4) = scol[i];
      }
      raw_barrier();
    } else {
      if (swp) mainloop(IC<1>{}, IC<0>{}, IC<0>{}); else mainloop(IC<0>{}, IC<0>{}, IC<0>{});
      STAMP(KID, 3); CSTAMP(KID, 3);
      if (EPI == EPI_QKV) pf_next();
      raw_barrier();
    }
    STAMP(KID, 4);

    if (EPI == EPI_GEGLU) {
      const int hbase = (tn >> 1) * 216 + (tn & 1) * 112;
      const int orow = m0 + wm * 64 + fg * 16 + fr;
      constexpr float C1 = -2.0f * 0.7978845608028654f * 1.4426950408889634f, C2 = C1 * 0.044715f;
      f32x2 nm[MT], k1[MT], k2[MT], iv[MT];
#pragma unroll
      for (int i = 0; i < MT; ++i) {
        const float2 ms = rowst[wm * 64 + i * 16 + fr];
        const float r2 = ms.y * ms.y, ir2 = __builtin_amdgcn_rcpf(r2);
        nm[i] = (f32x2){-ms.x, -ms.x}; k1[i] = (f32x2){C1 * ms.y, C1 * ms.y}; k2[i] = (f32x2){C2 * ms.y * r2, C2 * ms.y * r2}; iv[i] = (f32x2){ir2, ir2};
      }
      f32x2 cgs[NT], cus[NT];
#pragma unroll
      for (int j = 0; j < NT; ++j) { cgs[j] = *(const f32x2*)(colsum + (wn * NT + j) * 16 + fg * 2); cus[j] = *(const f32x2*)(colsum + (wn * NT + j) * 16 + fg * 2 + 8); }
      const unsigned ro = foff<27>(orow, 0);
#pragma unroll
      for (int j = 0; j < NT; ++j) {
        const f32x2 cg = cgs[j], cu = cus[j];
        unsigned d[MT];
#pragma unroll
        for (int i = 0; i < MT; ++i) {
          const f32x2 gg = __builtin_elementwise_fma(nm[i], cg, (f32x2){acc[i][j][0], acc[i][j][1]});
          const f32x2 uu = __builtin_elementwise_fma(nm[i], cu, (f32x2){acc[i][j][2], acc[i][j][3]});
          const f32x2 t = gg * __builtin_elementwise_fma(gg * gg, k2[i], k1[i]);
          const f32x2 e = (f32x2){__builtin_amdgcn_exp2f(t[0]), __builtin_amdgcn_exp2f(t[1])};
          const f32x2 q = __builtin_elementwise_fma(e, iv[i], iv[i]);
          const f32x2 h = (gg * uu) * (f32x2){__builtin_amdgcn_rcpf(q[0]), __builtin_amdgcn_rcpf(q[1])};
          d[i] = cvtpk(h[0], h[1]);
        }
        const u32x2 x = swap32(d[0], d[2]), y = swap32(d[1], d[3]);
        const u32x2 z = swap16(x[0], y[0]), w = swap16(x[1], y[1]);
        const int jb = wn * NT + j;
        if (jb < 13 || !(tn & 1)) stw<0, (ST_MODE >> 1) & 1>(g.out, ro + foff<27>(0, hbase + jb * 8), (u32x4){z[0], z[1], w[0], w[1]});
      }
    } else {
      if (swp) {
        const float sc = tn < DM / BN ? QSCALE : 1.0f;
        float2 ms[MT]; f32x4 cs[NT];
#pragma unroll
        for (int i = 0; i < MT; ++i) ms[i] = rowst[wm * 64 + i * 16 + fr];
#pragma unroll
        for (int j = 0; j < NT; ++j) cs[j] = *(const f32x4*)(colsum + (wn * NT + j) * 16 + fg * 4);
        static_for<8>([&](auto I) {
          constexpr int jq = decltype(I)::v >> 2, i = decltype(I)::v & 3;
          const float rs = ms[i].y * sc;
          const f32x4 o0 = rs * (acc[i][jq * 2] - ms[i].x * cs[jq * 2]), o1 = rs * (acc[i][jq * 2 + 1] - ms[i].x * cs[jq * 2 + 1]);
          const u32x2 x = swap16(cvtpk(o0[0], o0[1]), cvtpk(o1[0], o1[1])), y = swap16(cvtpk(o0[2], o0[3]), cvtpk(o1[2], o1[3]));
          stw<(i & 1) * 256>(g.ws, so[jq][i >> 1], (u32x4){x[0], y[0], x[1], y[1]});
        });
      } else {
        f32x4 ma[MT], mb[MT]; float cs[NT];
#pragma unroll
        for (int i = 0; i < MT; ++i) { ma[i] = *(const f32x4*)(rowst + wm * 64 + i * 16 + fg * 4); mb[i] = *(const f32x4*)(rowst + wm * 64 + i * 16 + fg * 4 + 2); }
#pragma unroll
        for (int j = 0; j < NT; ++j) cs[j] = colsum[(wn * NT + j) * 16 + fr];
        static_for<8>([&](auto I) {
          constexpr int jq = decltype(I)::v >> 2, i = decltype(I)::v & 3;
          const float c0 = cs[jq * 2], c1 = cs[jq * 2 + 1];
          const f32x4 &p0 = acc[i][jq * 2], &p1 = acc[i][jq * 2 + 1], &a = ma[i], &b = mb[i];
          const u32x2 x = swap32(cvtpk(a[1] * (p0[0] - a[0] * c0), a[3] * (p0[1] - a[2] * c0)), cvtpk(a[1] * (p1[0] - a[0] * c1), a[3] * (p1[1] - a[2] * c1)));
          const u32x2 y = swap32(cvtpk(b[1] * (p0[2] - b[0] * c0), b[3] * (p0[3] - b[2] * c0)), cvtpk(b[1] * (p1[2] - b[0] * c1), b[3] * (p1[3] - b[2] * c1)));
          stw<(i & 1) * 1024>(g.ws, so[jq][i >> 1], (u32x4){x[0], y[0], x[1], y[1]});
        });
      }
    }
    STAMP(KID, 5);
    wait_vm<0>();
    STAMP(KID, 6);
    KLABEL("kend_", KNUM);
  }
}

constexpr int ATT_NBUF = 4, ATT_HALF = 14336, ATT_STB = 2 * ATT_HALF, ATT_V = 6144;
struct AttnArgs { char* ws; const u16* wqkv; const u16* wo; const u16* wg; const u16* wu; const u16* wd;
};

#define EX(x) __builtin_amdgcn_exp2f(x)
#define E4(P, I, S, J) P[I] = cvtpk(EX(S[J]), EX(S[J + 1])); P[I + 1] = cvtpk(EX(S[J + 2]), EX(S[J + 3]));
#define MF16(a, b, c) __builtin_amdgcn_mfma_f32_32x32x16_bf16(a, b, c, 0, 0, 0)
#define MF8(a, b, c) __builtin_amdgcn_mfma_f32_32x32x8bf16_1k(a, b, c, 0, 0, 0)
#define BC8(x) __builtin_bit_cast(bf16x8, x)

DI float rowmax16(const f32x16& s) {
  float m = s[0];
#pragma unroll
  for (int i = 1; i < 16; ++i) m = fmaxf(m, s[i]);
  return fmaxf(m, __shfl_xor(m, 32));
}
DI bool nonfinite12(const f32x16& o, int lane) {
  bool bad = false;
#pragma unroll
  for (int i = 0; i < 9; ++i) bad |= !(fabsf(o[i]) < 1.0e38f);
#pragma unroll
  for (int i = 9; i < 12; ++i) bad |= (lane < 32) && !(fabsf(o[i]) < 1.0e38f);
  return bad;
}

struct PackJob { const u16* src; int ld, kk0, kmax, s2; char* dst; float* cs; };
DI PackJob pack_job(const AttnArgs& a, int J, int wid, int lane) {
  const int prow = wid * 4 + (lane >> 4), c16 = lane & 15, ch = (c16 >> 1) ^ (prow & 7);
  const int k = 8 * (((prow >> 3) & 1) * 2 + ((prow >> 2) & 1)) + 4 * ((prow >> 4) & 1) + (prow & 3);
  const int fr = lane & 15, fg = lane >> 4;
  PackJob r;
  if (J >= 243) J -= 243;
  r.kmax = 19;
  if (J < 108) {
    const int cg = J >> 2, kq = J & 3, b = cg * 8 + wid, rr = b % 27, tn = (b / 27) * 2 + (rr >= 14), jb = rr - (rr >= 14) * 14;
    r.src = ((c16 & 1) ? a.wu : a.wg) + (size_t)k * DF + cg * 64 + ch * 8; r.ld = DF; r.kk0 = kq * 5; r.s2 = 28;
    r.dst = a.ws + WS_PGU + (size_t)(tn * 280 + jb * 2) * 1024 + (fg * 16 + ((fr & 7) >> 1) * 4 + (fr >> 3) * 2 + (fr & 1)) * 16;
    r.cs = (float*)(a.ws + WS_CGU) + (tn * 4 + kq) * 224 + jb * 16 + fg * 4;
  } else if (J < 168) {
    const int tn = (J - 108) >> 2, kq = (J - 108) & 3;
    r.src = a.wqkv + (size_t)k * (3 * DM) + tn * 128 + ch * 16 + (c16 & 1) * 8; r.ld = 3 * DM; r.kk0 = kq * 5; r.s2 = 16;
    r.dst = a.ws + WS_PQKV + (size_t)(tn * 160 + wid * 2) * 1024 + lane * 16;
    r.cs = (float*)(a.ws + WS_CQKV) + (tn * 4 + kq) * 128 + wid * 16 + fg * 4;
  } else {
    const bool d = J >= 188;
    const int cg = d ? (J - 188) / 11 : (J - 168) >> 2, kq = d ? (J - 188) % 11 : (J - 168) & 3, jb = cg * 8 + wid;
    r.src = (d ? a.wd : a.wo) + (size_t)k * DM + cg * 128 + ch * 16 + (c16 & 1) * 8; r.ld = DM; r.kk0 = kq * 5; r.s2 = 10;
    if (d) r.kmax = 53;
    r.dst = a.ws + (d ? WS_PWD : WS_PWO) + (size_t)((jb / 5) * (d ? 270 : 100) + (jb % 5) * 2) * 1024 + lane * 16;
    r.cs = nullptr;
  }
  return r;
}

template <bool PACK>
__global__ __launch_bounds__(512) void attn_kernel(AttnArgs a) {
  __shared__ __attribute__((aligned(16))) char smem[ATT_NBUF * ATT_STB + (PACK ? 5 * 8192 : 0) + 256];
  KLABEL("kbeg_", 5 + PACK);
  const int tid = threadIdx.x, lane = tid & 63, wid = __builtin_amdgcn_readfirstlane(tid >> 6);
  char* ws = a.ws;
  STAMP(KID, 0);
  const int bid = blockIdx.x, xcd = bid & 7, idx = bid >> 3;
  const int head = xcd * 4 + (idx >> 3), qb = idx & 7;
  const int r32 = lane & 31, hh = lane >> 5, half = wid >> 2;
  const int q0 = qb * 256 + (wid & 3) * 64 + r32;
  const char* kvbase = ws + WS_KV + (size_t)(head >> 1) * 16 * KVP_CHUNK + lane * 16;
  int so[4];
#pragma unroll
  for (int i = 0; i < 4; ++i) {
    int pc = wid + i * 8; if (pc > 27) pc = 27;
    const int hf = pc >= 14, q = pc - hf * 14;
    so[i] = hf * 8 * KVP_CHUNK + (q < 4 ? (head & 1) * 4096 + q * 1024 : q < 6 ? 4096 + q * 1024 : KVP_V + (head & 1) * 8192 + (q - 6) * 1024);
  }
  auto stage = [&](int s, int buf) {
#pragma unroll
    for (int i = 0; i < 4; ++i) {
      int pc = wid + i * 8; if (pc > 27) pc = 27;
      glds16(kvbase + (size_t)s * KVP_CHUNK + so[i], smem + buf * ATT_STB + pc * 1024);
    }
  };
  u32x4 qr1[2]; u32x2 qr2[2];
  {
    const char* Q = ws + WS_Q + (size_t)((head >> 1) * 64 + (q0 >> 5)) * 2560;
#pragma unroll
    for (int i = 0; i < 2; ++i) {
      qr1[i] = gload16(Q + i * 2560 + (head & 1) * 1024 + lane * 16);
      qr2[i] = gload8(Q + i * 2560 + 2048 + r32 * 16 + (head & 1) * 8);
    }
  }
  stage(0, 0); stage(1, 1); stage(2, 2);
  PackJob pj; const char* pp; bf16x4 plo, phi; f32x4 pcs = (f32x4){0.f, 0.f, 0.f, 0.f};
  if (PACK) {
    pj = pack_job(a, bid, wid, lane);
    const int pr = 8 * (lane >> 5) + 4 * ((lane >> 4) & 1) + ((lane & 15) >> 2);
    pp = smem + ATT_NBUF * ATT_STB + pr * 256 + (wid ^ (pr & 7)) * 32 + (lane & 3) * 8;
  }
  const char* dummy = smem + ATT_NBUF * ATT_STB + (PACK ? 5 * 8192 : 0);
  const char* kl = smem + half * ATT_HALF + lane * 16;
  const char* k8 = smem + half * ATT_HALF + 4096 + r32 * 16 + (head & 1) * 8;
  f32x16 oa, ob, nma, nmb, sa, sb;
#pragma unroll
  for (int i = 0; i < 16; ++i) { oa[i] = 0.f; ob[i] = 0.f; }
  wait_vm<8>();
#pragma unroll
  for (int i = 0; i < 2; ++i) asm volatile("" : "+v"(qr1[i]), "+v"(qr2[i]));
  const bf16x8 qa1 = BC8(qr1[0]), qb1 = BC8(qr1[1]);
  const bf16x4 qa2 = __builtin_bit_cast(bf16x4, hh ? (u32x2){0u, 0u} : qr2[0]), qb2 = __builtin_bit_cast(bf16x4, hh ? (u32x2){0u, 0u} : qr2[1]);
  raw_barrier();
  if (PACK) {
#pragma unroll
    for (int b = 0; b < 5; ++b) glds16(pj.src + (size_t)min(pj.kk0 + b, pj.kmax) * 32 * pj.ld, smem + ATT_NBUF * ATT_STB + b * 8192 + wid * 1024);
  }
  bf16x8 k1 = *(const bf16x8*)kl; bf16x4 k2 = *(const bf16x4*)k8;
  {
    sa = MF16(k1, qa1, oa); sa = MF8(k2, qa2, sa);
    sb = MF16(k1, qb1, ob); sb = MF8(k2, qb2, sb);
    k1 = *(const bf16x8*)(kl + 1024); k2 = *(const bf16x4*)(k8 + 512);
    const float ma = rowmax16(sa), mb = rowmax16(sb);
#pragma unroll
    for (int i = 0; i < 16; ++i) { nma[i] = -ma; nmb[i] = -mb; sa[i] -= ma; sb[i] -= mb; }
  }
  constexpr int NWO = PFA_WO ? 800 << (3 - CS3) : 0, NGU = (PFA_GU * 224) << (4 - CS2), NR = (NWO + NGU + 16383) / 16384, PC = PACK ? PFA_C0 : PFA_C;
  constexpr int NL0 = PC > 9 ? PC : 9, NL1 = PC + NR < 13 ? PC + NR : 13, NLATE = NL1 > NL0 ? NL1 - NL0 : 0;
  static_assert(PC + NR <= 16, "rounds");
  STAMP(KID, 2); CSTAMP(KID, 2);
#pragma unroll 1
  for (int c = 0; c < 8; ++c) {
    const int bo = (c & 3) * ATT_STB, bn = ((c + 1) & 3) * ATT_STB;
#pragma unroll
    for (int t = 0; t < 4; ++t) {
      if (t == 2 && c < 7) {
        if (!PACK) { if (c == 6) wait_vm<NLATE>(); else wait_vm<4>(); }
        else if (c < 2) wait_vm<9>(); else if (c == 2) wait_vm<4>(); else if (c == 3) wait_vm<6>(); else if (c == 4) wait_vm<8>();
        else if (c == 5) wait_vm<7>(); else wait_vm<1 + NLATE>();
        raw_barrier();
        if (c < 5) stage(c + 3, (c + 3) & 3);
      }
      if (!(t & 1)) {
        const int r = c * 2 + (t >> 1) - PC;
        if (r >= 0 && r < NR) {
          const int id = min((r * 256 + idx * 8 + wid) * 64 + lane, NWO + NGU - 1);
          gtouch(ws + (id < NWO ? tile_line<WS_PWO, 10 * 10240, 3 - CS3, 8, 0>((xcd >> (3 - CS3)) * (8 >> CS3), id)
                                : tile_line<WS_PGU, 10 * 28672, 4 - CS2, 16, 0>((xcd >> (3 - CS2)) * (16 >> CS2), id - NWO)), dummy);
        }
        if (TC && c == 6 && t == 2 && wid == 0) touch_code<3>(dummy, lane);
      }
      if (PACK) {
        const int pb = c * 2 + (t >> 1) - 5;
        if (pb >= 0 && pb < 5) {
          if (!(t & 1)) { plo = tr_read(pp + pb * 8192); phi = tr_read(pp + pb * 8192 + 4096); }
          else {
            const bf16x8 f = __builtin_shufflevector(plo, phi, 0, 1, 2, 3, 4, 5, 6, 7);
            const int kk = min(pj.kk0 + pb, pj.kmax);
            st16<3>(pj.dst + ((kk >> 1) * pj.s2 + (kk & 1)) * 1024, __builtin_bit_cast(u32x4, f));
            pcs = __builtin_amdgcn_mfma_f32_16x16x32_bf16(f, (bf16x8){0x3f80, 0x3f80, 0x3f80, 0x3f80, 0x3f80, 0x3f80, 0x3f80, 0x3f80}, pcs, 0, 0, 0);
          }
        }
      }
      const char* vp = kl + bo + ATT_V + t * 2048;
      const bf16x8 v0 = *(const bf16x8*)vp, v1 = *(const bf16x8*)(vp + 1024);
      const int kb = (t < 2) ? bo : bn, kt = (t + 2) & 3;
      u32x4 p0, p1, p2, p3; f32x16 na, nb;
      SB();
      na = MF16(k1, qa1, nma); SB();
      E4(p0, 0, sa, 0) SB();
      na = MF8(k2, qa2, na); SB();
      E4(p0, 2, sa, 4) SB();
      oa = MF16(v0, BC8(p0), oa); SB();
      E4(p1, 0, sa, 8) SB();
      nb = MF16(k1, qb1, nmb);
      k1 = *(const bf16x8*)(kl + kb + kt * 1024); SB();
      E4(p1, 2, sa, 12) SB();
      oa = MF16(v1, BC8(p1), oa); SB();
      E4(p2, 0, sb, 0) SB();
      nb = MF8(k2, qb2, nb);
      k2 = *(const bf16x4*)(k8 + kb + kt * 512); SB();
      SB();
      E4(p2, 2, sb, 4) SB();
      ob = MF16(v0, BC8(p2), ob); SB();
      E4(p3, 0, sb, 8) E4(p3, 2, sb, 12) SB();
      ob = MF16(v1, BC8(p3), ob); SB();
      sa = na; sb = nb;
    }
  }
  if (PACK && pj.cs != nullptr && (lane & 15) == 0) *(f32x4*)pj.cs = pcs;
  float ma = -nma[0], mb = -nmb[0];
  STAMP(KID, 3); CSTAMP(KID, 3);
  if (__syncthreads_or(nonfinite12(oa, lane) || nonfinite12(ob, lane))) {
    stage(0, 0); stage(1, 1); stage(2, 2);
#pragma unroll
    for (int i = 0; i < 16; ++i) { oa[i] = 0.f; ob[i] = 0.f; }
    ma = -3.0e38f; mb = -3.0e38f;
#pragma unroll 1
    for (int c = 0; c < 8; ++c) {
      if (c < 6) wait_vm<8>(); else if (c == 6) wait_vm<4>(); else wait_vm<0>();
      raw_barrier();
      if (c + 3 < 8) stage(c + 3, (c + 3) & 3);
      const int bo = (c & 3) * ATT_STB;
#pragma unroll 1
      for (int t = 0; t < 4; ++t) {
        k1 = *(const bf16x8*)(kl + bo + t * 1024); k2 = *(const bf16x4*)(k8 + bo + t * 512);
        const char* vp = kl + bo + ATT_V + t * 2048;
        const bf16x8 v0 = *(const bf16x8*)vp, v1 = *(const bf16x8*)(vp + 1024);
        auto one = [&](const bf16x8& q1, const bf16x4& q2, f32x16& o, float& m) {
          f32x16 s;
#pragma unroll
          for (int i = 0; i < 16; ++i) s[i] = 0.f;
          s = MF16(k1, q1, s); s = MF8(k2, q2, s);
          const float mn = fmaxf(m, rowmax16(s)), f = EX(m - mn);
          m = mn;
          u32x4 p0, p1;
#pragma unroll
          for (int i = 0; i < 16; ++i) { s[i] -= mn; o[i] *= f; }
          E4(p0, 0, s, 0) E4(p0, 2, s, 4) E4(p1, 0, s, 8) E4(p1, 2, s, 12)
          o = MF16(v0, BC8(p0), o); o = MF16(v1, BC8(p1), o);
        };
        one(qa1, qa2, oa, ma); one(qb1, qb2, ob, mb);
      }
    }
    __syncthreads();
  }
  STAMP(KID, 4);
  float* xo = (float*)smem + (wid ^ 4) * 13 * 64 + lane;
  const float* xi = (const float*)smem + wid * 13 * 64 + lane;
#pragma unroll
  for (int i = 0; i < 12; ++i) xo[i * 64] = half ? oa[i] : ob[i];
  xo[12 * 64] = half ? ma : mb;
  __syncthreads();
  {
    const float m1 = half ? mb : ma, m2 = xi[12 * 64], M = fmaxf(m1, m2);
    const float f1 = EX(m1 - M), f2 = EX(m2 - M);
    float o[12];
#pragma unroll
    for (int i = 0; i < 12; ++i) o[i] = (half ? ob[i] : oa[i]) * f1 + xi[i * 64] * f2;
    const float l = __shfl(o[8], r32 + 32);
    const float inv = 1.0f / l;
    const int q = q0 + half * 32, c0 = head * DH + hh * 4;
    char* O = ws + WS_O;
    st8<4>(O + foff<10>(q, c0), (u32x2){cvtpk(o[0] * inv, o[1] * inv), cvtpk(o[2] * inv, o[3] * inv)});
    st8<4>(O + foff<10>(q, c0 + 8), (u32x2){cvtpk(o[4] * inv, o[5] * inv), cvtpk(o[6] * inv, o[7] * inv)});
    if (hh == 0) st8<4>(O + foff<10>(q, c0 + 16), (u32x2){cvtpk(o[8] * inv, o[9] * inv), cvtpk(o[10] * inv, o[11] * inv)});
  }
  STAMP(KID, 5);
  wait_vm<0>();
  STAMP(KID, 6);
  KLABEL("kend_", 5 + PACK);
}

extern "C" void kernel_launch(void* const* d_in, const int* in_sizes, int n_in, void* d_out, int out_size, void* d_ws,
                              size_t ws_size, hipStream_t stream) {
  const char* x0 = (const char*)d_in[0];
  char* ws = (char*)d_ws;
  for (int layer = 0; layer < 2; ++layer) {
    AttnArgs at{ws, (const u16*)d_in[1], (const u16*)d_in[2], (const u16*)d_in[4], (const u16*)d_in[5], (const u16*)d_in[6] KIDARG(layer * 5 + 1)};
    const char* xin = layer ? ws + WS_X2 : x0;
    char* xmid = ws + (layer ? WS_X3 : WS_X1);
    char* xout = layer ? (char*)d_out : ws + WS_X2;
    if (layer == 0) {
      GemmArgs a{xin, (const char*)d_in[1], nullptr, nullptr, ws, 0, 0 KIDARG(0)};
      gemm_kernel<128, 128, 640, 4, EPI_QKV, true><<<256, 512, 0, stream>>>(a);
      attn_kernel<true><<<256, 512, 0, stream>>>(at);
    } else {
      GemmArgs a{xin, ws + WS_PQKV, nullptr, nullptr, ws, 0, 0 KIDARG(5)};
      gemm_kernel<128, 128, 640, 4, EPI_QKV, false><<<256, 512, 0, stream>>>(a);
      attn_kernel<false><<<256, 512, 0, stream>>>(at);
    }
    GemmArgs b{ws + WS_O, ws + WS_PWO, xin, xmid, ws, layer == 0, 0 KIDARG(layer * 5 + 2)};
    gemm_kernel<64, 80, 640, 8, EPI_RES, false><<<256, 512, 0, stream>>>(b);
    GemmArgs c{xmid, ws + WS_PGU, nullptr, ws + WS_H, ws, 0, 0 KIDARG(layer * 5 + 3)};
    gemm_kernel<128, 224, 640, 3, EPI_GEGLU, false><<<256, 512, 0, stream>>>(c);
    GemmArgs d{ws + WS_H, ws + WS_PWD, xmid, xout, ws, 0, layer == 1 KIDARG(layer * 5 + 4)};
    gemm_kernel<64, 80, 1728, 8, EPI_RES, false><<<256, 512, 0, stream>>>(d);
  }
}
```

```cpp
#include <hip/hip_runtime.h>
#include <hip/hip_bf16.h>
#include <stdint.h>

typedef __attribute__((ext_vector_type(8))) short bf16x8;
typedef __attribute__((ext_vector_type(4))) short bf16x4;
typedef __attribute__((ext_vector_type(4))) float f32x4;
typedef __attribute__((ext_vector_type(16))) float f32x16;
typedef __attribute__((ext_vector_type(4))) unsigned u32x4;
typedef __attribute__((ext_vector_type(2))) unsigned u32x2;
typedef unsigned short u16;

#define DI __device__ __forceinline__

constexpr int SEQ = 2048, DM = 640, NH = 32, DH = 20, DF = 1728;
constexpr float LN_EPS = 1e-5f;
constexpr float QSCALE = 0.22360679774997896f * 1.4426950408889634f;

constexpr size_t WS_X1   = 0;
constexpr size_t WS_X2   = WS_X1 + (size_t)SEQ * DM * 2;
constexpr size_t WS_X3   = WS_X2 + (size_t)SEQ * DM * 2;
constexpr size_t WS_Q    = WS_X3 + (size_t)SEQ * DM * 2;
constexpr size_t WS_O    = WS_Q + (size_t)SEQ * DM * 2;
constexpr size_t WS_H    = WS_O + (size_t)SEQ * DM * 2;
constexpr int    KVP_CHUNK = 26624, KVP_TAIL = 8192, KVP_V = 10240;
constexpr size_t WS_KV   = WS_H + (size_t)SEQ * DF * 2;
constexpr size_t WS_PWO  = WS_KV + (size_t)16 * 16 * KVP_CHUNK;
constexpr size_t WS_PWD  = WS_PWO + (size_t)DM * DM * 2;
constexpr size_t WS_PGU  = WS_PWD + (size_t)DF * DM * 2;
constexpr size_t WS_PQKV = WS_PGU + (size_t)16 * 10 * 28 * 1024;
constexpr size_t WS_CGU  = WS_PQKV + (size_t)DM * 3 * DM * 2;
constexpr size_t WS_CQKV = WS_CGU + (size_t)16 * 4 * 224 * 4;
constexpr size_t WS_STAT = WS_CQKV + (size_t)15 * 4 * 128 * 4;
constexpr size_t WS_END  = WS_STAT + (size_t)SEQ * 64;

typedef __attribute__((ext_vector_type(2))) __bf16 bf16p;
typedef __attribute__((ext_vector_type(2))) float f32x2;
DI unsigned cvtpk(float lo, float hi) { return __builtin_bit_cast(unsigned, __builtin_convertvector((f32x2){lo, hi}, bf16p)); }
DI float dot2ones(unsigned a, float s) {
  return __builtin_amdgcn_fdot2_f32_bf16(__builtin_bit_cast(bf16p, a), __builtin_bit_cast(bf16p, 0x3f803f80u), s, false);
}
DI float bflo(unsigned v) { return __uint_as_float(v << 16); }
DI float bfhi(unsigned v) { return __uint_as_float(v & 0xffff0000u); }
#define KIDARG(k)
#define STAMP(kid, i)
#define CSTAMP(kid, i)
template <int N> DI void wait_vm() { asm volatile("s_waitcnt vmcnt(%0)" ::"i"(N) : "memory"); }
DI void raw_barrier() {
  asm volatile("" ::: "memory");
  __builtin_amdgcn_s_waitcnt(0xC07F);
  __builtin_amdgcn_s_barrier();
  asm volatile("" ::: "memory");
}
DI u32x2 swap16(unsigned a, unsigned b) { return __builtin_amdgcn_permlane16_swap(a, b, false, false); }
DI u32x2 swap32(unsigned a, unsigned b) { return __builtin_amdgcn_permlane32_swap(a, b, false, false); }
DI bf16x4 tr_read(const char* p) {
  typedef short v4i16_t __attribute__((ext_vector_type(4)));
  return __builtin_bit_cast(bf16x4, __builtin_amdgcn_ds_read_tr16_b64_v4i16((__attribute__((address_space(3))) v4i16_t*)(p)));
}
DI void glds16(const void* g, const char* l) {
  unsigned sv; const unsigned lb = __builtin_amdgcn_readfirstlane((unsigned)(uintptr_t)l);
  asm volatile("s_mov_b32 %0, m0\n\ts_mov_b32 m0, %2\n\ts_nop 0\n\tglobal_load_lds_dwordx4 %1, off\n\ts_mov_b32 m0, %0"
               : "=&s"(sv) : "v"(g), "s"(lb) : "memory");
}
DI void gtouch(const void* g, const char* l) {
  unsigned sv; const unsigned lb = __builtin_amdgcn_readfirstlane((unsigned)(uintptr_t)l);
  asm volatile("s_mov_b32 %0, m0\n\ts_mov_b32 m0, %2\n\ts_nop 0\n\tglobal_load_lds_dword %1, off\n\ts_mov_b32 m0, %0"
               : "=&s"(sv) : "v"(g), "s"(lb) : "memory");
}
#define KLABEL(pfx, id) asm volatile(pfx "%c0:" ::"i"(id))
template <int ID, bool END> DI unsigned long long kaddr() {
  unsigned long long a;
  if (END) asm volatile("s_getpc_b64 s[20:21]\n\ts_add_u32 s20, s20, kend_%c1@rel32@lo+4\n\ts_addc_u32 s21, s21, kend_%c1@rel32@hi+12\n\ts_mov_b64 %0, s[20:21]" : "=s"(a) : "i"(ID) : "s20", "s21", "scc");
  else asm volatile("s_getpc_b64 s[20:21]\n\ts_add_u32 s20, s20, kbeg_%c1@rel32@lo+4\n\ts_addc_u32 s21, s21, kbeg_%c1@rel32@hi+12\n\ts_mov_b64 %0, s[20:21]" : "=s"(a) : "i"(ID) : "s20", "s21", "scc");
  return a;
}
template <int ID> DI void touch_code(const char* dummy, int lane) {
  const unsigned long long lo = kaddr<ID, false>() & ~255ull;
  unsigned long long hi = kaddr<ID, true>();
  if (ID < 5) { const unsigned long long e = kaddr<(ID < 5 ? ID + 7 : ID), true>(); hi = e > hi ? e : hi; }
  for (unsigned o = lane * 128; o < (unsigned)(hi - lo); o += 8192) gtouch((const char*)lo + o, dummy);
}
#define TC 1
template <size_t BASE, int TSTR, int LGT, int NT, int B0> DI unsigned tile_line(int t0, int id) {
  return (unsigned)BASE + B0 + (unsigned)(min(t0 + (id & ((1 << LGT) - 1)), NT - 1) * TSTR + (id >> LGT) * 128);
}
template <size_t BASE, int TSTR, int LGT, int NT, int B0, int B1, int NSL> DI void touch_tiles(const char* ws, const char* dummy, int t0, int slot, int lane) {
  constexpr int N = ((B1 - B0) / 128) << LGT;
#pragma unroll
  for (int r = 0; r < (N + NSL * 64 - 1) / (NSL * 64); ++r)
    gtouch(ws + tile_line<BASE, TSTR, LGT, NT, B0>(t0, min((r * NSL + slot) * 64 + lane, N - 1)), dummy);
}
#define CS0 0
#define CS1 0
#define CS2 0
#define CS3 0
#define CS4 0
#define PFW_WD 0
#define PFA_WO 0
#define PFA_GU 0
#define PFA_C 8
#define PFA_C0 11
#define PFW_GU 0
#define PFW_K 4
#define PFG_WD 0
#define PFG_K 8
#define PFD_Q 0
#define PFD_K 20
DI u32x2 gload8(const void* g) {
  u32x2 r; asm volatile("global_load_dwordx2 %0, %1, off" : "=v"(r) : "v"(g) : "memory"); return r;
}
DI unsigned gload4(const void* g) {
  unsigned r; asm volatile("global_load_dword %0, %1, off" : "=v"(r) : "v"(g) : "memory"); return r;
}
DI u32x4 gload16(const void* g) {
  u32x4 r; asm volatile("global_load_dwordx4 %0, %1, off" : "=v"(r) : "v"(g) : "memory"); return r;
}
#define ST_MODE 14
template <int CLS> DI void st16(void* p, u32x4 v) {
  if ((ST_MODE >> CLS) & 1) asm volatile("global_store_dwordx4 %0, %1, off sc1\n\ts_nop 1" ::"v"(p), "v"(v));
  else *(u32x4*)p = v;
}
template <int IMM, bool WT = true> DI void stw(const void* base, unsigned off, u32x4 v) {
  if (WT) asm volatile("global_store_dwordx4 %0, %1, %2 offset:%3 sc1\n\ts_nop 1" ::"v"(off), "v"(v), "s"(base), "i"(IMM));
  else asm volatile("global_store_dwordx4 %0, %1, %2 offset:%3\n\ts_nop 1" ::"v"(off), "v"(v), "s"(base), "i"(IMM));
}
template <int CLS> DI void st8(void* p, u32x2 v) {
  if ((ST_MODE >> CLS) & 1) asm volatile("global_store_dwordx2 %0, %1, off sc1" ::"v"(p), "v"(v) : "memory");
  else *(u32x2*)p = v;
}
template <int I> struct IC { static constexpr int v = I; };
template <int L, int NMAX> DI void wait_stages(int n) {
  if constexpr (NMAX == 0) wait_vm<0>();
  else { if (n >= NMAX) wait_vm<NMAX * L>(); else wait_stages<L, NMAX - 1>(n); }
}
template <int N, int I = 0, class F> DI void static_for(F&& f) {
  if constexpr (I < N) { f(IC<I>{}); static_for<N, I + 1>(f); }
}
#define SB() __builtin_amdgcn_sched_barrier(0)
template <int NS> DI unsigned foff(int row, int col) {
  return (unsigned)((((row >> 6) * NS + (col >> 6)) * 8 + ((row >> 4) & 3) * 2 + ((col >> 5) & 1)) * 1024 +
                    (((col >> 3) & 3) * 16 + (row & 15)) * 16 + (col & 7) * 2);
}

enum { EPI_QKV = 0, EPI_RES = 1, EPI_GEGLU = 2 };

struct GemmArgs {
  const char* A; const char* B;
  const char* res;
  char* out;
  char* ws;
  int res_raw, out_raw;
};

template <int BM, int BN, int KTOT, int NST, int EPI, bool RAW>
__global__ __launch_bounds__(512) void gemm_kernel(GemmArgs g) {
  constexpr bool RES = (EPI == EPI_RES), LNP = !RES && !RAW;
  constexpr int MT = RES ? 2 : 4, NT = RES ? 5 : BN / 32, KT = KTOT / 64;
  constexpr int PA = BM / 8, PB = BN / 8, P = PA + PB, LMAX = (P + 3) / 4;
  constexpr int ABYTES = BM * 128, STB = P * 1024;
  constexpr int RS = BN * 2;
  static_assert(PA % 4 == 0 && KT >= NST && NST >= 3, "config");
  __shared__ __attribute__((aligned(16))) char smem[NST * STB + BM * 8 + BN * 4 + 256];
  float2* rowst = (float2*)(smem + NST * STB);
  float* colsum = (float*)(smem + NST * STB + BM * 8);

  constexpr int KNUM = EPI == EPI_QKV ? (RAW ? 0 : 1) : EPI == EPI_GEGLU ? 2 : KTOT == 640 ? 3 : 4;
  KLABEL("kbeg_", KNUM);
  const int tid = threadIdx.x, lane = tid & 63, wid = __builtin_amdgcn_readfirstlane(tid >> 6);
  if (wid < 4) STAMP(KID, 0);
  const int xcd = blockIdx.x & 7, idx = blockIdx.x >> 3;
  constexpr int CS = KNUM == 0 ? CS0 : KNUM == 1 ? CS1 : KNUM == 2 ? CS2 : KNUM == 3 ? CS3 : CS4, LRT = (RES ? 2 : 1) + CS;
  const int tm = ((xcd & ((8 >> CS) - 1)) << LRT) + (idx & ((1 << LRT) - 1)), tn = (xcd >> (3 - CS)) * (32 >> LRT) + (idx >> LRT);
  if (EPI == EPI_QKV && tn >= 15) return;
  const int m0 = tm * BM, n0 = tn * BN;
  const int fr = lane & 15, fg = lane >> 4;

  if (wid >= 4) {
    const int lw = wid - 4;
    const char* src[LMAX];
#pragma unroll
    for (int i = 0; i < LMAX; ++i) {
      const int p = lw + 4 * i;
      if (RAW) {
        if (i < PA / 4) {
          const int row = p * 8 + (lane >> 3), c = (lane & 7) ^ ((row >> 1) & 7);
          src[i] = g.A + ((size_t)(m0 + row) * KTOT + c * 8) * 2;
        } else {
          const int u = (p - PA) * 64 + lane, prow = u >> 4, c16 = u & 15;
          const int ch = (c16 >> 1) ^ (prow & 7);
          const int k = (prow & 32) + 8 * (((prow >> 3) & 1) * 2 + ((prow >> 2) & 1)) + 4 * ((prow >> 4) & 1) + (prow & 3);
          src[i] = g.B + ((size_t)k * (3 * DM) + n0 + ch * 16 + (c16 & 1) * 8) * 2;
        }
      } else {
        if (i < PA / 4) src[i] = g.A + ((size_t)((m0 >> 6) + (p >> 3)) * KT * 8 + (p & 7)) * 1024 + lane * 16;
        else src[i] = g.B + ((size_t)tn * KT * PB + (p - PA < PB ? p - PA : 0)) * 1024 + lane * 16;
      }
    }
    constexpr int STR_A = RAW ? 128 : 8192, STR_B = RAW ? 64 * 3 * DM * 2 : PB * 1024;
    auto body = [&](auto LC) {
      constexpr int L = decltype(LC)::v;
      auto issue = [&](int buf) {
        char* base = smem + buf * STB + lw * 1024;
#pragma unroll
        for (int i = 0; i < L; ++i) {
          glds16(src[i], base + i * 4096);
          src[i] += (i < PA / 4) ? STR_A : STR_B;
        }
      };
#define RAMP 2
      constexpr int R0 = RAMP < NST - 1 ? RAMP : NST - 1;
#pragma unroll
      for (int r = 0; r < R0; ++r) issue(r);
      int nxt = R0, ibuf = R0 % NST;
      constexpr int NMAIN = LNP ? KT - NST + 1 : KT;
      for (int i = 0; i < NMAIN; ++i) {
        wait_stages<L, NST - 1>(nxt - i - 1);
        raw_barrier();
        const int lim = min(KT, i + NST);
#pragma unroll
        for (int r = 0; r < 2; ++r)
          if (nxt < lim) { issue(ibuf); ++nxt; if (++ibuf == NST) ibuf = 0; }
      }
      if (LNP) {
        const int t = tid - 256;
        const float* cp = (const float*)(g.ws + (EPI == EPI_QKV ? WS_CQKV : WS_CGU)) + tn * 4 * BN + (t < BN ? t : 0);
        const char* sp = g.ws + WS_STAT + (size_t)(m0 + (t & 127)) * 64;
        unsigned c[4]; u32x4 st[4];
#pragma unroll
        for (int k = 0; k < 4; ++k) c[k] = gload4(cp + k * BN);
#pragma unroll
        for (int k = 0; k < 4; ++k) st[k] = gload16(sp + k * 16);
        static_for<NST - 1>([&](auto I) { wait_vm<(NST - 2 - decltype(I)::v) * L + 8>(); raw_barrier(); });
        wait_vm<0>();
        float cs = 0.f;
#pragma unroll
        for (int k = 0; k < 4; ++k) { asm volatile("" : "+v"(c[k])); cs += __uint_as_float(c[k]); }
#pragma unroll
        for (int k = 0; k < 4; ++k) asm volatile("" : "+v"(st[k]));
        const f32x4 sv = (__builtin_bit_cast(f32x4, st[0]) + __builtin_bit_cast(f32x4, st[1])) + (__builtin_bit_cast(f32x4, st[2]) + __builtin_bit_cast(f32x4, st[3]));
        const float mean = (sv[0] + sv[2]) * (1.0f / KTOT);
        const float var = fmaxf((sv[1] + sv[3]) * (1.0f / KTOT) - mean * mean, 0.f);
        if (t < BN) colsum[t] = cs;
        if (t >= 128) rowst[t - 128] = make_float2(mean, rsqrtf(var + LN_EPS));
        raw_barrier();
      }
    };
    if (P % 4 == 0 || lw < P % 4) body(IC<LMAX>{}); else body(IC<LMAX - 1>{});
    KLABEL("kend_", KNUM + 7);
    return;
  }

  const int wm = wid >> 1, wn = wid & 1;
  const char* dummy = smem + NST * STB + BM * 8 + BN * 4;
  constexpr int PFK = KNUM == 2 ? PFG_K : KNUM == 3 ? PFW_K : KNUM == 4 ? PFD_K : 7;
  auto pf_next = [&]() {
    constexpr int NEXT = KNUM == 0 ? 6 : KNUM == 1 ? 5 : KNUM == 2 ? 4 : KNUM == 3 ? 2 : 1;
    if (KNUM == 4 && g.out_raw) return;
    if (TC && wid == 0) touch_code<NEXT>(dummy, lane);
    const int sl = idx * 4 + wid;
    if (KNUM == 2 && PFG_WD > PFW_WD) touch_tiles<WS_PWD, 27 * 10240, 3 - CS4, 8, PFW_WD * 10240, (PFG_WD > PFW_WD ? PFG_WD : PFW_WD + 1) * 10240, 128>(g.ws, dummy, (xcd >> (3 - CS4)) * (8 >> CS4), sl, lane);
    if (KNUM == 3 && PFW_GU > PFA_GU) touch_tiles<WS_PGU, 10 * 28672, 4 - CS2, 16, PFA_GU * 28672, (PFW_GU > PFA_GU ? PFW_GU : PFA_GU + 1) * 28672, 128>(g.ws, dummy, (xcd >> (3 - CS2)) * (16 >> CS2), sl, lane);
    if (KNUM == 3 && PFW_WD) touch_tiles<WS_PWD, 27 * 10240, 3 - CS4, 8, 0, (PFW_WD ? PFW_WD : 1) * 10240, 128>(g.ws, dummy, (xcd >> (3 - CS4)) * (8 >> CS4), sl, lane);
    if (KNUM == 4 && PFD_Q) touch_tiles<WS_PQKV, 10 * 16384, 4 - CS1, 15, 0, (PFD_Q ? PFD_Q : 1) * 16384, 128>(g.ws, dummy, (xcd >> (3 - CS1)) * (16 >> CS1), sl, lane);
  };
  f32x4 acc[MT][NT];
#pragma unroll
  for (int i = 0; i < MT; ++i)
#pragma unroll
    for (int j = 0; j < NT; ++j) acc[i][j] = (f32x4){0.f, 0.f, 0.f, 0.f};

  if (RES) {
    const int row = m0 + wid * 16 + fr;
    u32x2 resv[NT];
#pragma unroll
    for (int j = 0; j < NT; ++j) {
      const int col = n0 + j * 16 + fg * 4;
      resv[j] = *(const u32x2*)(g.res + (g.res_raw ? (unsigned)(row * DM + col) * 2 : foff<10>(row, col)));
    }
    const int aoff = (wm * 4 + wn) * 1024 + lane * 16, boff = ABYTES + wn * 1024 + lane * 16;
    bf16x8 a0[2], b0[NT], a1[2], b1[NT];
    auto rd = [&](bf16x8(&a)[2], bf16x8(&b)[NT], const char* base) {
#pragma unroll
      for (int i = 0; i < 2; ++i) a[i] = *(const bf16x8*)(base + aoff + i * 2048);
#pragma unroll
      for (int j = 0; j < NT; ++j) b[j] = *(const bf16x8*)(base + boff + j * 2048);
      SB();
    };
    auto mm = [&](bf16x8(&a)[2], bf16x8(&b)[NT]) {
#pragma unroll
      for (int j = 0; j < NT; ++j)
#pragma unroll
        for (int i = 0; i < 2; ++i) acc[i][j] = __builtin_amdgcn_mfma_f32_16x16x32_bf16(b[j], a[i], acc[i][j], 0, 0, 0);
      SB();
    };
    STAMP(KID, 1);
    raw_barrier();
    STAMP(KID, 2); CSTAMP(KID, 2);
    rd(a0, b0, smem);
    int buf = 0;
#pragma unroll 1
    for (int kt = 0; kt + 2 < KT; kt += 2) {
      if (++buf == NST) buf = 0;
      if (kt == (PFK & ~1)) pf_next();
      raw_barrier();
      rd(a1, b1, smem + buf * STB);
      mm(a0, b0);
      if (++buf == NST) buf = 0;
      raw_barrier();
      rd(a0, b0, smem + buf * STB);
      mm(a1, b1);
    }
    if (KT % 2 == 0) {
      if (++buf == NST) buf = 0;
      raw_barrier();
      rd(a1, b1, smem + buf * STB);
      mm(a0, b0);
      mm(a1, b1);
    } else {
      mm(a0, b0);
    }
    STAMP(KID, 3); CSTAMP(KID, 3);
    char* xb = smem + ((KT % NST) * STB);
#pragma unroll
    for (int j = 0; j < NT; ++j) *(f32x4*)(xb + ((wid ^ 1) * NT + j) * 1024 + lane * 16) = wn ? acc[0][j] : acc[1][j];
    raw_barrier();
    STAMP(KID, 4);
    float s = 0.f, q = 0.f;
    u32x2 v[NT];
#pragma unroll
    for (int j = 0; j < NT; ++j) {
      const f32x4 o = *(const f32x4*)(xb + (wid * NT + j) * 1024 + lane * 16) + (wn ? acc[1][j] : acc[0][j]);
      const u32x2 r = resv[j];
      v[j] = (u32x2){cvtpk(o[0] + bflo(r[0]), o[1] + bfhi(r[0])), cvtpk(o[2] + bflo(r[1]), o[3] + bfhi(r[1]))};
      const float e0 = bflo(v[j][0]), e1 = bfhi(v[j][0]), e2 = bflo(v[j][1]), e3 = bfhi(v[j][1]);
      s += (e0 + e1) + (e2 + e3);
      q += (e0 * e0 + e1 * e1) + (e2 * e2 + e3 * e3);
    }
#pragma unroll
    for (int jp = 0; jp < NT; jp += 2) {
      const int j1 = jp + 1 < NT ? jp + 1 : jp;
      const u32x2 x = swap16(v[jp][0], v[j1][0]), y = swap16(v[jp][1], v[j1][1]);
      const int col = n0 + (jp + (fg & 1)) * 16 + (fg >> 1) * 8;
      if (jp + 1 < NT || !(fg & 1))
        st16<0>(g.out + (g.out_raw ? (unsigned)(row * DM + col) * 2 : foff<10>(row, col)), (u32x4){x[0], y[0], x[1], y[1]});
    }
    {
      const u32x2 t1 = swap16(__float_as_uint(s), __float_as_uint(q));
      const unsigned t2 = __float_as_uint(__uint_as_float(t1[0]) + __uint_as_float(t1[1]));
      const u32x2 t3 = swap32(t2, t2);
      if (lane < 32) *(float*)(g.ws + WS_STAT + (size_t)(row * 8 + tn) * 8 + fg * 4) = __uint_as_float(t3[0]) + __uint_as_float(t3[1]);
    }
    STAMP(KID, 5);
    wait_vm<0>();
    STAMP(KID, 6);
    KLABEL("kend_", KNUM);
    return;
  } else {
    if (EPI == EPI_QKV && tn >= 10) {
      const int vc0 = n0 - 2 * DM, head = (vc0 + DH - 1) / DH + (tid >> 4);
      if (head * DH < vc0 + BN) {
        const int tok = m0 + ((tid >> 1) & 7) * 16;
        char* p = g.ws + WS_KV + (size_t)((head >> 1) * 16 + (tok >> 7)) * KVP_CHUNK + KVP_V + (head & 1) * 8192 + ((tok >> 4) & 7) * 1024 + ((tid & 1) * 32 + DH) * 16;
        *(u32x4*)p = (u32x4){0x3f803f80u, 0x3f803f80u, 0x3f803f80u, 0x3f803f80u};
      }
    }
    int aoff[2], boff[RAW ? NT : 1];
    const int pr = 8 * (fg >> 1) + 4 * (fg & 1) + (fr >> 2);
#pragma unroll
    for (int ks = 0; ks < 2; ++ks)
      aoff[ks] = RAW ? (wm * 64 + fr) * 128 + (((ks * 4 + fg) ^ (fr >> 1)) * 16) : (wm * 8 + ks) * 1024 + lane * 16;
    if (RAW) {
#pragma unroll
      for (int j = 0; j < NT; ++j) boff[j] = ABYTES + pr * RS + ((wn * NT + j) ^ (pr & 7)) * 32 + (lane & 3) * 8;
    } else boff[0] = ABYTES + wn * NT * 2048 + lane * 16;
    f32x4 srow[2], sgr[2], scol[2];
#pragma unroll
    for (int i = 0; i < 2; ++i) srow[i] = sgr[i] = scol[i] = (f32x4){0.f, 0.f, 0.f, 0.f};
    const bf16x8 ones = (bf16x8){0x3f80, 0x3f80, 0x3f80, 0x3f80, 0x3f80, 0x3f80, 0x3f80, 0x3f80};

    const bool swp = !(EPI == EPI_QKV && tn >= 10);
    unsigned so[2][2];
    if (EPI == EPI_QKV) {
      const bool isq = tn < DM / BN;
#pragma unroll
      for (int jq = 0; jq < 2; ++jq) {
        if (swp) {
          const int G = ((isq ? n0 : n0 - DM) >> 3) + (wn * NT + jq * 2 + (fg & 1)) * 2 + (fg >> 1), pair = G / 5, gi = G % 5;
          const int mo = (((gi + (gi > 2)) & 1) * 32 + fr) * 16;
          if (isq) {
            so[jq][0] = (unsigned)WS_Q + (pair * 64 + (m0 >> 5) + wm * 2) * 2560 + (gi == 2 ? 2048 + fr * 16 : (gi > 2) * 1024 + mo);
            so[jq][1] = so[jq][0] + 2560;
          } else {
            so[jq][0] = (unsigned)WS_KV + (pair * 16 + (m0 >> 7)) * KVP_CHUNK + (gi == 2 ? KVP_TAIL + (wm * 64 + fr) * 16 : (gi > 2) * 4096 + wm * 2048 + mo);
            so[jq][1] = so[jq][0] + (gi == 2 ? 512 : 1024);
          }
        } else {
          const int cc = n0 - 2 * DM + (wn * NT + jq * 2 + (lane >> 5)) * 16 + fr, head = cc / DH, d = cc % DH;
          so[jq][0] = (unsigned)WS_KV + ((head >> 1) * 16 + (m0 >> 7)) * KVP_CHUNK + KVP_V + (head & 1) * 8192 + wm * 4096 + ((fg & 1) * 32 + d) * 16;
          so[jq][1] = so[jq][0] + 2048;
        }
        asm volatile("" : "+v"(so[jq][0]), "+v"(so[jq][1]));
      }
    }
    auto mainloop = [&](auto SW, auto WN, auto WM) {
      constexpr bool SWP = decltype(SW)::v;
      constexpr int CWN = decltype(WN)::v, CWM = decltype(WM)::v;
      bf16x8 a0[MT], a1[MT], b0[NT], b1[NT];
      auto rdB = [&](const char* base, int j, int ks) {
        if (RAW) {
          const char* p = base + boff[RAW ? j : 0] + ks * 32 * RS;
          bf16x4 lo = tr_read(p), hi = tr_read(p + 16 * RS);
          return __builtin_shufflevector(lo, hi, 0, 1, 2, 3, 4, 5, 6, 7);
        } else return *(const bf16x8*)(base + boff[0] + j * 2048 + ks * 1024);
      };
      auto step = [&](bf16x8(&ac)[MT], bf16x8(&bc)[NT], bf16x8(&an)[MT], bf16x8(&bn)[NT], const char* nb, int nks, auto RD) {
        constexpr bool rd = decltype(RD)::v;
        constexpr int RPG = 3;
#pragma unroll
        for (int j = 0; j < NT; ++j) {
#pragma unroll
          for (int i = 0; i < MT; ++i) {
            acc[i][j] = SWP ? __builtin_amdgcn_mfma_f32_16x16x32_bf16(bc[j], ac[i], acc[i][j], 0, 0, 0)
                            : __builtin_amdgcn_mfma_f32_16x16x32_bf16(ac[i], bc[j], acc[i][j], 0, 0, 0);
            const int r = j * RPG + i;
            if (rd && i < RPG && r < MT + NT) {
              if (r == 0) bn[0] = rdB(nb, 0, nks);
              else if (r <= MT) an[r - 1] = *(const bf16x8*)(nb + aoff[nks] + (r - 1) * 2048);
              else bn[r - MT] = rdB(nb, r - MT, nks);
              SB();
            }
          }
          if (RAW) {
            if ((j >> 1) == CWM) scol[j & 1] = __builtin_amdgcn_mfma_f32_16x16x32_bf16(bc[j], ones, scol[j & 1], 0, 0, 0);
            else {
              srow[j & 1] = __builtin_amdgcn_mfma_f32_16x16x32_bf16(ones, ac[CWN * 2 + (j & 1)], srow[j & 1], 0, 0, 0);
              sgr[j & 1] = __builtin_amdgcn_mfma_f32_16x16x32_bf16(ac[CWN * 2 + (j & 1)], ac[CWN * 2 + (j & 1)], sgr[j & 1], 0, 0, 0);
            }
          }
          SB();
        }
      };
      STAMP(KID, 1);
      raw_barrier();
      STAMP(KID, 2); CSTAMP(KID, 2);
#pragma unroll
      for (int j = 0; j < NT; ++j) { b0[j] = rdB(smem, j, 0); if (j < MT) a0[j] = *(const bf16x8*)(smem + aoff[0] + j * 2048); }
      step(a0, b0, a1, b1, smem, 1, IC<1>{});
      int buf = 0;
#pragma unroll 1
      for (int kt = 1; kt < KT; ++kt) {
        if (++buf == NST) buf = 0;
        if (EPI == EPI_GEGLU && kt == PFK) pf_next();
        raw_barrier();
        step(a1, b1, a0, b0, smem + buf * STB, 0, IC<1>{});
        step(a0, b0, a1, b1, smem + buf * STB, 1, IC<1>{});
      }
      step(a1, b1, a0, b0, smem, 0, IC<0>{});
    };
    if (RAW) {
      if (swp) {
        if (wid == 0) mainloop(IC<1>{}, IC<0>{}, IC<0>{}); else if (wid == 1) mainloop(IC<1>{}, IC<1>{}, IC<0>{});
        else if (wid == 2) mainloop(IC<1>{}, IC<0>{}, IC<1>{}); else mainloop(IC<1>{}, IC<1>{}, IC<1>{});
      } else {
        if (wid == 0) mainloop(IC<0>{}, IC<0>{}, IC<0>{}); else if (wid == 1) mainloop(IC<0>{}, IC<1>{}, IC<0>{});
        else if (wid == 2) mainloop(IC<0>{}, IC<0>{}, IC<1>{}); else mainloop(IC<0>{}, IC<1>{}, IC<1>{});
      }
      STAMP(KID, 3); CSTAMP(KID, 3);
      pf_next();
#pragma unroll
      for (int i = 0; i < 2; ++i) {
        const float sq = (fr & 2) ? ((fr & 1) ? sgr[i][3] : sgr[i][2]) : ((fr & 1) ? sgr[i][1] : sgr[i][0]);
        const float mean = srow[i][0] * (1.0f / KTOT);
        const float var = fmaxf(sq * (1.0f / KTOT) - mean * mean, 0.f);
        if (fg == (fr >> 2)) rowst[wm * 64 + (wn * 2 + i) * 16 + fr] = make_float2(mean, rsqrtf(var + LN_EPS));
        if (fr == 0) *(f32x4*)(colsum + (wn * NT + wm * 2 + i) * 16 + fg * 4) = scol[i];
      }
      raw_barrier();
    } else {
      if (swp) mainloop(IC<1>{}, IC<0>{}, IC<0>{}); else mainloop(IC<0>{}, IC<0>{}, IC<0>{});
      STAMP(KID, 3); CSTAMP(KID, 3);
      if (EPI == EPI_QKV) pf_next();
      raw_barrier();
    }
    STAMP(KID, 4);

    if (EPI == EPI_GEGLU) {
      const int hbase = (tn >> 1) * 216 + (tn & 1) * 112;
      const int orow = m0 + wm * 64 + fg * 16 + fr;
      constexpr float C1 = -2.0f * 0.7978845608028654f * 1.4426950408889634f, C2 = C1 * 0.044715f;
      f32x2 nm[MT], k1[MT], k2[MT], iv[MT];
#pragma unroll
      for (int i = 0; i < MT; ++i) {
        const float2 ms = rowst[wm * 64 + i * 16 + fr];
        const float r2 = ms.y * ms.y, ir2 = __builtin_amdgcn_rcpf(r2);
        nm[i] = (f32x2){-ms.x, -ms.x}; k1[i] = (f32x2){C1 * ms.y, C1 * ms.y}; k2[i] = (f32x2){C2 * ms.y * r2, C2 * ms.y * r2}; iv[i] = (f32x2){ir2, ir2};
      }
      f32x2 cgs[NT], cus[NT];
#pragma unroll
      for (int j = 0; j < NT; ++j) { cgs[j] = *(const f32x2*)(colsum + (wn * NT + j) * 16 + fg * 2); cus[j] = *(const f32x2*)(colsum + (wn * NT + j) * 16 + fg * 2 + 8); }
      const unsigned ro = foff<27>(orow, 0);
#pragma unroll
      for (int j = 0; j < NT; ++j) {
        const f32x2 cg = cgs[j], cu = cus[j];
        unsigned d[MT];
#pragma unroll
        for (int i = 0; i < MT; ++i) {
          const f32x2 gg = __builtin_elementwise_fma(nm[i], cg, (f32x2){acc[i][j][0], acc[i][j][1]});
          const f32x2 uu = __builtin_elementwise_fma(nm[i], cu, (f32x2){acc[i][j][2], acc[i][j][3]});
          const f32x2 t = gg * __builtin_elementwise_fma(gg * gg, k2[i], k1[i]);
          const f32x2 e = (f32x2){__builtin_amdgcn_exp2f(t[0]), __builtin_amdgcn_exp2f(t[1])};
          const f32x2 q = __builtin_elementwise_fma(e, iv[i], iv[i]);
          const f32x2 h = (gg * uu) * (f32x2){__builtin_amdgcn_rcpf(q[0]), __builtin_amdgcn_rcpf(q[1])};
          d[i] = cvtpk(h[0], h[1]);
        }
        const u32x2 x = swap32(d[0], d[2]), y = swap32(d[1], d[3]);
        const u32x2 z = swap16(x[0], y[0]), w = swap16(x[1], y[1]);
        const int jb = wn * NT + j;
        if (jb < 13 || !(tn & 1)) stw<0, (ST_MODE >> 1) & 1>(g.out, ro + foff<27>(0, hbase + jb * 8), (u32x4){z[0], z[1], w[0], w[1]});
      }
    } else {
      if (swp) {
        const float sc = tn < DM / BN ? QSCALE : 1.0f;
        float2 ms[MT]; f32x4 cs[NT];
#pragma unroll
        for (int i = 0; i < MT; ++i) ms[i] = rowst[wm * 64 + i * 16 + fr];
#pragma unroll
        for (int j = 0; j < NT; ++j) cs[j] = *(const f32x4*)(colsum + (wn * NT + j) * 16 + fg * 4);
        static_for<8>([&](auto I) {
          constexpr int jq = decltype(I)::v >> 2, i = decltype(I)::v & 3;
          const float rs = ms[i].y * sc;
          const f32x4 o0 = rs * (acc[i][jq * 2] - ms[i].x * cs[jq * 2]), o1 = rs * (acc[i][jq * 2 + 1] - ms[i].x * cs[jq * 2 + 1]);
          const u32x2 x = swap16(cvtpk(o0[0], o0[1]), cvtpk(o1[0], o1[1])), y = swap16(cvtpk(o0[2], o0[3]), cvtpk(o1[2], o1[3]));
          stw<(i & 1) * 256>(g.ws, so[jq][i >> 1], (u32x4){x[0], y[0], x[1], y[1]});
        });
      } else {
        f32x4 ma[MT], mb[MT]; float cs[NT];
#pragma unroll
        for (int i = 0; i < MT; ++i) { ma[i] = *(const f32x4*)(rowst + wm * 64 + i * 16 + fg * 4); mb[i] = *(const f32x4*)(rowst + wm * 64 + i * 16 + fg * 4 + 2); }
#pragma unroll
        for (int j = 0; j < NT; ++j) cs[j] = colsum[(wn * NT + j) * 16 + fr];
        static_for<8>([&](auto I) {
          constexpr int jq = decltype(I)::v >> 2, i = decltype(I)::v & 3;
          const float c0 = cs[jq * 2], c1 = cs[jq * 2 + 1];
          const f32x4 &p0 = acc[i][jq * 2], &p1 = acc[i][jq * 2 + 1], &a = ma[i], &b = mb[i];
          const u32x2 x = swap32(cvtpk(a[1] * (p0[0] - a[0] * c0), a[3] * (p0[1] - a[2] * c0)), cvtpk(a[1] * (p1[0] - a[0] * c1), a[3] * (p1[1] - a[2] * c1)));
          const u32x2 y = swap32(cvtpk(b[1] * (p0[2] - b[0] * c0), b[3] * (p0[3] - b[2] * c0)), cvtpk(b[1] * (p1[2] - b[0] * c1), b[3] * (p1[3] - b[2] * c1)));
          stw<(i & 1) * 1024>(g.ws, so[jq][i >> 1], (u32x4){x[0], y[0], x[1], y[1]});
        });
      }
    }
    STAMP(KID, 5);
    wait_vm<0>();
    STAMP(KID, 6);
    KLABEL("kend_", KNUM);
  }
}

constexpr int ATT_NBUF = 4, ATT_HALF = 14336, ATT_STB = 2 * ATT_HALF, ATT_V = 6144;
struct AttnArgs { char* ws; const u16* wqkv; const u16* wo; const u16* wg; const u16* wu; const u16* wd;
};

#define EX(x) __builtin_amdgcn_exp2f(x)
#define E4(P, I, S, J) P[I] = cvtpk(EX(S[J]), EX(S[J + 1])); P[I + 1] = cvtpk(EX(S[J + 2]), EX(S[J + 3]));
#define MF16(a, b, c) __builtin_amdgcn_mfma_f32_32x32x16_bf16(a, b, c, 0, 0, 0)
#define MF8(a, b, c) __builtin_amdgcn_mfma_f32_32x32x8bf16_1k(a, b, c, 0, 0, 0)
#define BC8(x) __builtin_bit_cast(bf16x8, x)

DI float rowmax16(const f32x16& s) {
  float m = s[0];
#pragma unroll
  for (int i = 1; i < 16; ++i) m = fmaxf(m, s[i]);
  return fmaxf(m, __shfl_xor(m, 32));
}
DI bool nonfinite12(const f32x16& o, int lane) {
  bool bad = false;
#pragma unroll
  for (int i = 0; i < 9; ++i) bad |= !(fabsf(o[i]) < 1.0e38f);
#pragma unroll
  for (int i = 9; i < 12; ++i) bad |= (lane < 32) && !(fabsf(o[i]) < 1.0e38f);
  return bad;
}

struct PackJob { const u16* src; int ld, kk0, kmax, s2; char* dst; float* cs; };
DI PackJob pack_job(const AttnArgs& a, int J, int wid, int lane) {
  const int prow = wid * 4 + (lane >> 4), c16 = lane & 15, ch = (c16 >> 1) ^ (prow & 7);
  const int k = 8 * (((prow >> 3) & 1) * 2 + ((prow >> 2) & 1)) + 4 * ((prow >> 4) & 1) + (prow & 3);
  const int fr = lane & 15, fg = lane >> 4;
  PackJob r;
  if (J >= 243) J -= 243;
  r.kmax = 19;
  if (J < 108) {
    const int cg = J >> 2, kq = J & 3, b = cg * 8 + wid, rr = b % 27, tn = (b / 27) * 2 + (rr >= 14), jb = rr - (rr >= 14) * 14;
    r.src = ((c16 & 1) ? a.wu : a.wg) + (size_t)k * DF + cg * 64 + ch * 8; r.ld = DF; r.kk0 = kq * 5; r.s2 = 28;
    r.dst = a.ws + WS_PGU + (size_t)(tn * 280 + jb * 2) * 1024 + (fg * 16 + ((fr & 7) >> 1) * 4 + (fr >> 3) * 2 + (fr & 1)) * 16;
    r.cs = (float*)(a.ws + WS_CGU) + (tn * 4 + kq) * 224 + jb * 16 + fg * 4;
  } else if (J < 168) {
    const int tn = (J - 108) >> 2, kq = (J - 108) & 3;
    r.src = a.wqkv + (size_t)k * (3 * DM) + tn * 128 + ch * 16 + (c16 & 1) * 8; r.ld = 3 * DM; r.kk0 = kq * 5; r.s2 = 16;
    r.dst = a.ws + WS_PQKV + (size_t)(tn * 160 + wid * 2) * 1024 + lane * 16;
    r.cs = (float*)(a.ws + WS_CQKV) + (tn * 4 + kq) * 128 + wid * 16 + fg * 4;
  } else {
    const bool d = J >= 188;
    const int cg = d ? (J - 188) / 11 : (J - 168) >> 2, kq = d ? (J - 188) % 11 : (J - 168) & 3, jb = cg * 8 + wid;
    r.src = (d ? a.wd : a.wo) + (size_t)k * DM + cg * 128 + ch * 16 + (c16 & 1) * 8; r.ld = DM; r.kk0 = kq * 5; r.s2 = 10;
    if (d) r.kmax = 53;
    r.dst = a.ws + (d ? WS_PWD : WS_PWO) + (size_t)((jb / 5) * (d ? 270 : 100) + (jb % 5) * 2) * 1024 + lane * 16;
    r.cs = nullptr;
  }
  return r;
}

template <bool PACK>
__global__ __launch_bounds__(512) void attn_kernel(AttnArgs a) {
  __shared__ __attribute__((aligned(16))) char smem[ATT_NBUF * ATT_STB + (PACK ? 5 * 8192 : 0) + 256];
  KLABEL("kbeg_", 5 + PACK);
  const int tid = threadIdx.x, lane = tid & 63, wid = __builtin_amdgcn_readfirstlane(tid >> 6);
  char* ws = a.ws;
  STAMP(KID, 0);
  const int bid = blockIdx.x, xcd = bid & 7, idx = bid >> 3;
  const int head = xcd * 4 + (idx >> 3), qb = idx & 7;
  const int r32 = lane & 31, hh = lane >> 5, half = wid >> 2;
  const int q0 = qb * 256 + (wid & 3) * 64 + r32;
  const char* kvbase = ws + WS_KV + (size_t)(head >> 1) * 16 * KVP_CHUNK + lane * 16;
  int so[4];
#pragma unroll
  for (int i = 0; i < 4; ++i) {
    int pc = wid + i * 8; if (pc > 27) pc = 27;
    const int hf = pc >= 14, q = pc - hf * 14;
    so[i] = hf * 8 * KVP_CHUNK + (q < 4 ? (head & 1) * 4096 + q * 1024 : q < 6 ? 4096 + q * 1024 : KVP_V + (head & 1) * 8192 + (q - 6) * 1024);
  }
  auto stage = [&](int s, int buf) {
#pragma unroll
    for (int i = 0; i < 4; ++i) {
      int pc = wid + i * 8; if (pc > 27) pc = 27;
      glds16(kvbase + (size_t)s * KVP_CHUNK + so[i], smem + buf * ATT_STB + pc * 1024);
    }
  };
  u32x4 qr1[2]; u32x2 qr2[2];
  {
    const char* Q = ws + WS_Q + (size_t)((head >> 1) * 64 + (q0 >> 5)) * 2560;
#pragma unroll
    for (int i = 0; i < 2; ++i) {
      qr1[i] = gload16(Q + i * 2560 + (head & 1) * 1024 + lane * 16);
      qr2[i] = gload8(Q + i * 2560 + 2048 + r32 * 16 + (head & 1) * 8);
    }
  }
  stage(0, 0); stage(1, 1); stage(2, 2);
  PackJob pj; const char* pp; bf16x4 plo, phi; f32x4 pcs = (f32x4){0.f, 0.f, 0.f, 0.f};
  if (PACK) {
    pj = pack_job(a, bid, wid, lane);
    const int pr = 8 * (lane >> 5) + 4 * ((lane >> 4) & 1) + ((lane & 15) >> 2);
    pp = smem + ATT_NBUF * ATT_STB + pr * 256 + (wid ^ (pr & 7)) * 32 + (lane & 3) * 8;
  }
  const char* dummy = smem + ATT_NBUF * ATT_STB + (PACK ? 5 * 8192 : 0);
  const char* kl = smem + half * ATT_HALF + lane * 16;
  const char* k8 = smem + half * ATT_HALF + 4096 + r32 * 16 + (head & 1) * 8;
  f32x16 oa, ob, nma, nmb, sa, sb;
#pragma unroll
  for (int i = 0; i < 16; ++i) { oa[i] = 0.f; ob[i] = 0.f; }
  wait_vm<8>();
#pragma unroll
  for (int i = 0; i < 2; ++i) asm volatile("" : "+v"(qr1[i]), "+v"(qr2[i]));
  const bf16x8 qa1 = BC8(qr1[0]), qb1 = BC8(qr1[1]);
  const bf16x4 qa2 = __builtin_bit_cast(bf16x4, hh ? (u32x2){0u, 0u} : qr2[0]), qb2 = __builtin_bit_cast(bf16x4, hh ? (u32x2){0u, 0u} : qr2[1]);
  raw_barrier();
  if (PACK) {
#pragma unroll
    for (int b = 0; b < 5; ++b) glds16(pj.src + (size_t)min(pj.kk0 + b, pj.kmax) * 32 * pj.ld, smem + ATT_NBUF * ATT_STB + b * 8192 + wid * 1024);
  }
  bf16x8 k1 = *(const bf16x8*)kl; bf16x4 k2 = *(const bf16x4*)k8;
  {
    sa = MF16(k1, qa1, oa); sa = MF8(k2, qa2, sa);
    sb = MF16(k1, qb1, ob); sb = MF8(k2, qb2, sb);
    k1 = *(const bf16x8*)(kl + 1024); k2 = *(const bf16x4*)(k8 + 512);
    const float ma = rowmax16(sa), mb = rowmax16(sb);
#pragma unroll
    for (int i = 0; i < 16; ++i) { nma[i] = -ma; nmb[i] = -mb; sa[i] -= ma; sb[i] -= mb; }
  }
  constexpr int NWO = PFA_WO ? 800 << (3 - CS3) : 0, NGU = (PFA_GU * 224) << (4 - CS2), NR = (NWO + NGU + 16383) / 16384, PC = PACK ? PFA_C0 : PFA_C;
  constexpr int NL0 = PC > 9 ? PC : 9, NL1 = PC + NR < 13 ? PC + NR : 13, NLATE = NL1 > NL0 ? NL1 - NL0 : 0;
  static_assert(PC + NR <= 16, "rounds");
  STAMP(KID, 2); CSTAMP(KID, 2);
#pragma unroll 1
  for (int c = 0; c < 8; ++c) {
    const int bo = (c & 3) * ATT_STB, bn = ((c + 1) & 3) * ATT_STB;
#pragma unroll
    for (int t = 0; t < 4; ++t) {
      if (t == 2 && c < 7) {
        if (!PACK) { if (c == 6) wait_vm<NLATE>(); else wait_vm<4>(); }
        else if (c < 2) wait_vm<9>(); else if (c == 2) wait_vm<4>(); else if (c == 3) wait_vm<6>(); else if (c == 4) wait_vm<8>();
        else if (c == 5) wait_vm<7>(); else wait_vm<1 + NLATE>();
        raw_barrier();
        if (c < 5) stage(c + 3, (c + 3) & 3);
      }
      if (!(t & 1)) {
        const int r = c * 2 + (t >> 1) - PC;
        if (r >= 0 && r < NR) {
          const int id = min((r * 256 + idx * 8 + wid) * 64 + lane, NWO + NGU - 1);
          gtouch(ws + (id < NWO ? tile_line<WS_PWO, 10 * 10240, 3 - CS3, 8, 0>((xcd >> (3 - CS3)) * (8 >> CS3), id)
                                : tile_line<WS_PGU, 10 * 28672, 4 - CS2, 16, 0>((xcd >> (3 - CS2)) * (16 >> CS2), id - NWO)), dummy);
        }
        if (TC && c == 6 && t == 2 && wid == 0) touch_code<3>(dummy, lane);
      }
      if (PACK) {
        const int pb = c * 2 + (t >> 1) - 5;
        if (pb >= 0 && pb < 5) {
          if (!(t & 1)) { plo = tr_read(pp + pb * 8192); phi = tr_read(pp + pb * 8192 + 4096); }
          else {
            const bf16x8 f = __builtin_shufflevector(plo, phi, 0, 1, 2, 3, 4, 5, 6, 7);
            const int kk = min(pj.kk0 + pb, pj.kmax);
            st16<3>(pj.dst + ((kk >> 1) * pj.s2 + (kk & 1)) * 1024, __builtin_bit_cast(u32x4, f));
            pcs = __builtin_amdgcn_mfma_f32_16x16x32_bf16(f, (bf16x8){0x3f80, 0x3f80, 0x3f80, 0x3f80, 0x3f80, 0x3f80, 0x3f80, 0x3f80}, pcs, 0, 0, 0);
          }
        }
      }
      const char* vp = kl + bo + ATT_V + t * 2048;
      const bf16x8 v0 = *(const bf16x8*)vp, v1 = *(const bf16x8*)(vp + 1024);
      const int kb = (t < 2) ? bo : bn, kt = (t + 2) & 3;
      u32x4 p0, p1, p2, p3; f32x16 na, nb;
      SB();
      na = MF16(k1, qa1, nma); SB();
      E4(p0, 0, sa, 0) SB();
      na = MF8(k2, qa2, na); SB();
      E4(p0, 2, sa, 4) SB();
      oa = MF16(v0, BC8(p0), oa); SB();
      E4(p1, 0, sa, 8) SB();
      nb = MF16(k1, qb1, nmb);
      k1 = *(const bf16x8*)(kl + kb + kt * 1024); SB();
      E4(p1, 2, sa, 12) SB();
      oa = MF16(v1, BC8(p1), oa); SB();
      E4(p2, 0, sb, 0) SB();
      nb = MF8(k2, qb2, nb);
      k2 = *(const bf16x4*)(k8 + kb + kt * 512); SB();
      SB();
      E4(p2, 2, sb, 4) SB();
      ob = MF16(v0, BC8(p2), ob); SB();
      E4(p3, 0, sb, 8) E4(p3, 2, sb, 12) SB();
      ob = MF16(v1, BC8(p3), ob); SB();
      sa = na; sb = nb;
    }
  }
  if (PACK && pj.cs != nullptr && (lane & 15) == 0) *(f32x4*)pj.cs = pcs;
  float ma = -nma[0], mb = -nmb[0];
  STAMP(KID, 3); CSTAMP(KID, 3);
  if (__syncthreads_or(nonfinite12(oa, lane) || nonfinite12(ob, lane))) {
    stage(0, 0); stage(1, 1); stage(2, 2);
#pragma unroll
    for (int i = 0; i < 16; ++i) { oa[i] = 0.f; ob[i] = 0.f; }
    ma = -3.0e38f; mb = -3.0e38f;
#pragma unroll 1
    for (int c = 0; c < 8; ++c) {
      if (c < 6) wait_vm<8>(); else if (c == 6) wait_vm<4>(); else wait_vm<0>();
      raw_barrier();
      if (c + 3 < 8) stage(c + 3, (c + 3) & 3);
      const int bo = (c & 3) * ATT_STB;
#pragma unroll 1
      for (int t = 0; t < 4; ++t) {
        k1 = *(const bf16x8*)(kl + bo + t * 1024); k2 = *(const bf16x4*)(k8 + bo + t * 512);
        const char* vp = kl + bo + ATT_V + t * 2048;
        const bf16x8 v0 = *(const bf16x8*)vp, v1 = *(const bf16x8*)(vp + 1024);
        auto one = [&](const bf16x8& q1, const bf16x4& q2, f32x16& o, float& m) {
          f32x16 s;
#pragma unroll
          for (int i = 0; i < 16; ++i) s[i] = 0.f;
          s = MF16(k1, q1, s); s = MF8(k2, q2, s);
          const float mn = fmaxf(m, rowmax16(s)), f = EX(m - mn);
          m = mn;
          u32x4 p0, p1;
#pragma unroll
          for (int i = 0; i < 16; ++i) { s[i] -= mn; o[i] *= f; }
          E4(p0, 0, s, 0) E4(p0, 2, s, 4) E4(p1, 0, s, 8) E4(p1, 2, s, 12)
          o = MF16(v0, BC8(p0), o); o = MF16(v1, BC8(p1), o);
        };
        one(qa1, qa2, oa, ma); one(qb1, qb2, ob, mb);
      }
    }
    __syncthreads();
  }
  STAMP(KID, 4);
  float* xo = (float*)smem + (wid ^ 4) * 13 * 64 + lane;
  const float* xi = (const float*)smem + wid * 13 * 64 + lane;
#pragma unroll
  for (int i = 0; i < 12; ++i) xo[i * 64] = half ? oa[i] : ob[i];
  xo[12 * 64] = half ? ma : mb;
  __syncthreads();
  {
    const float m1 = half ? mb : ma, m2 = xi[12 * 64], M = fmaxf(m1, m2);
    const float f1 = EX(m1 - M), f2 = EX(m2 - M);
    float o[12];
#pragma unroll
    for (int i = 0; i < 12; ++i) o[i] = (half ? ob[i] : oa[i]) * f1 + xi[i * 64] * f2;
    const float l = __shfl(o[8], r32 + 32);
    const float inv = 1.0f / l;
    const int q = q0 + half * 32, c0 = head * DH + hh * 4;
    char* O = ws + WS_O;
    st8<4>(O + foff<10>(q, c0), (u32x2){cvtpk(o[0] * inv, o[1] * inv), cvtpk(o[2] * inv, o[3] * inv)});
    st8<4>(O + foff<10>(q, c0 + 8), (u32x2){cvtpk(o[4] * inv, o[5] * inv), cvtpk(o[6] * inv, o[7] * inv)});
    if (hh == 0) st8<4>(O + foff<10>(q, c0 + 16), (u32x2){cvtpk(o[8] * inv, o[9] * inv), cvtpk(o[10] * inv, o[11] * inv)});
  }
  STAMP(KID, 5);
  wait_vm<0>();
  STAMP(KID, 6);
  KLABEL("kend_", 5 + PACK);
}

#define EXPT 1
struct ExArgs { const char* p; char* ws; };
template <int SH, int OFF> __global__ __launch_bounds__(64) void ex_pre(ExArgs a) {
  const int b = blockIdx.x, g = (b + SH) & 7, idx = b >> 3;
  if (idx >= 16) return;
  const unsigned v = *(const unsigned*)(a.p + OFF + g * 131072 + (idx * 64 + threadIdx.x) * 128);
  if (v == 0x12345678u) *(unsigned*)(a.ws + (200u << 20)) = v;
}
__global__ __launch_bounds__(64) void ex_chase(ExArgs a) {
  const int b = blockIdx.x, g = b & 7, idx = b >> 3;
  if (idx != 0 || threadIdx.x != 0) return;
  unsigned o = 0;
  for (int i = 0; i < 128; ++i) {
    const unsigned v = *(const volatile unsigned*)(a.p + g * 131072 + o);
    o = ((i + 1) * 37 % 1024) * 128 + (v == 0x12345679u ? 4 : 0);
  }
  if (o == 3) *(unsigned*)(a.ws + (200u << 20)) = o;
}
extern "C" void kernel_launch(void* const* d_in, const int* in_sizes, int n_in, void* d_out, int out_size, void* d_ws,
                              size_t ws_size, hipStream_t stream) {
  const char* x0 = (const char*)d_in[0];
  char* ws = (char*)d_ws;
  for (int layer = 0; layer < 2; ++layer) {
    AttnArgs at{ws, (const u16*)d_in[1], (const u16*)d_in[2], (const u16*)d_in[4], (const u16*)d_in[5], (const u16*)d_in[6] KIDARG(layer * 5 + 1)};
    const char* xin = layer ? ws + WS_X2 : x0;
    char* xmid = ws + (layer ? WS_X3 : WS_X1);
    char* xout = layer ? (char*)d_out : ws + WS_X2;
    if (layer == 0) {
      GemmArgs a{xin, (const char*)d_in[1], nullptr, nullptr, ws, 0, 0 KIDARG(0)};
      gemm_kernel<128, 128, 640, 4, EPI_QKV, true><<<256, 512, 0, stream>>>(a);
      attn_kernel<true><<<256, 512, 0, stream>>>(at);
    } else {
      GemmArgs a{xin, ws + WS_PQKV, nullptr, nullptr, ws, 0, 0 KIDARG(5)};
      gemm_kernel<128, 128, 640, 4, EPI_QKV, false><<<256, 512, 0, stream>>>(a);
      attn_kernel<false><<<256, 512, 0, stream>>>(at);
    }
    GemmArgs b{ws + WS_O, ws + WS_PWO, xin, xmid, ws, layer == 0, 0 KIDARG(layer * 5 + 2)};
    gemm_kernel<64, 80, 640, 8, EPI_RES, false><<<256, 512, 0, stream>>>(b);
    GemmArgs c{xmid, ws + WS_PGU, nullptr, ws + WS_H, ws, 0, 0 KIDARG(layer * 5 + 3)};
    gemm_kernel<128, 224, 640, 3, EPI_GEGLU, false><<<256, 512, 0, stream>>>(c);
    GemmArgs d{ws + WS_H, ws + WS_PWD, xmid, xout, ws, 0, layer == 1 KIDARG(layer * 5 + 4)};
    gemm_kernel<64, 80, 1728, 8, EPI_RES, false><<<256, 512, 0, stream>>>(d);
  }
  ExArgs e{(const char*)d_in[3], ws};
  if (EXPT == 1) ex_pre<0, 0><<<256, 64, 0, stream>>>(e);
  if (EXPT == 2) ex_pre<1, 0><<<256, 64, 0, stream>>>(e);
  if (EXPT == 3) ex_pre<0, 1048576><<<256, 64, 0, stream>>>(e);
  if (EXPT) ex_chase<<<256, 64, 0, stream>>>(e);
}
```

```cpp
#include <hip/hip_runtime.h>
#include <hip/hip_bf16.h>
#include <stdint.h>

typedef __attribute__((ext_vector_type(8))) short bf16x8;
typedef __attribute__((ext_vector_type(4))) short bf16x4;
typedef __attribute__((ext_vector_type(4))) float f32x4;
typedef __attribute__((ext_vector_type(16))) float f32x16;
typedef __attribute__((ext_vector_type(4))) unsigned u32x4;
typedef __attribute__((ext_vector_type(2))) unsigned u32x2;
typedef unsigned short u16;

#define DI __device__ __forceinline__

constexpr int SEQ = 2048, DM = 640, NH = 32, DH = 20, DF = 1728;
constexpr float LN_EPS = 1e-5f;
constexpr float QSCALE = 0.22360679774997896f * 1.4426950408889634f;

constexpr size_t WS_X1   = 0;
constexpr size_t WS_X2   = WS_X1 + (size_t)SEQ * DM * 2;
constexpr size_t WS_X3   = WS_X2 + (size_t)SEQ * DM * 2;
constexpr size_t WS_Q    = WS_X3 + (size_t)SEQ * DM * 2;
constexpr size_t WS_O    = WS_Q + (size_t)SEQ * DM * 2;
constexpr size_t WS_H    = WS_O + (size_t)SEQ * DM * 2;
constexpr int    KVP_CHUNK = 26624, KVP_TAIL = 8192, KVP_V = 10240;
constexpr size_t WS_KV   = WS_H + (size_t)SEQ * DF * 2;
constexpr size_t WS_PWO  = WS_KV + (size_t)16 * 16 * KVP_CHUNK;
constexpr size_t WS_PWD  = WS_PWO + (size_t)DM * DM * 2;
constexpr size_t WS_PGU  = WS_PWD + (size_t)DF * DM * 2;
constexpr size_t WS_PQKV = WS_PGU + (size_t)16 * 10 * 28 * 1024;
constexpr size_t WS_CGU  = WS_PQKV + (size_t)DM * 3 * DM * 2;
constexpr size_t WS_CQKV = WS_CGU + (size_t)16 * 4 * 224 * 4;
constexpr size_t WS_STAT = WS_CQKV + (size_t)15 * 4 * 128 * 4;
constexpr size_t WS_STAT2 = WS_STAT + (size_t)SEQ * 64;
constexpr size_t WS_CNT  = WS_STAT2 + (size_t)SEQ * 64;
constexpr size_t WS_END  = WS_CNT + 6 * 16 * 256;

typedef __attribute__((ext_vector_type(2))) __bf16 bf16p;
typedef __attribute__((ext_vector_type(2))) float f32x2;
DI unsigned cvtpk(float lo, float hi) { return __builtin_bit_cast(unsigned, __builtin_convertvector((f32x2){lo, hi}, bf16p)); }
DI float dot2ones(unsigned a, float s) {
  return __builtin_amdgcn_fdot2_f32_bf16(__builtin_bit_cast(bf16p, a), __builtin_bit_cast(bf16p, 0x3f803f80u), s, false);
}
DI float bflo(unsigned v) { return __uint_as_float(v << 16); }
DI float bfhi(unsigned v) { return __uint_as_float(v & 0xffff0000u); }
#define KIDARG(k)
#define STAMP(kid, i)
#define CSTAMP(kid, i)
template <int N> DI void wait_vm() { asm volatile("s_waitcnt vmcnt(%0)" ::"i"(N) : "memory"); }
DI void raw_barrier() {
  asm volatile("" ::: "memory");
  __builtin_amdgcn_s_waitcnt(0xC07F);
  __builtin_amdgcn_s_barrier();
  asm volatile("" ::: "memory");
}
DI u32x2 swap16(unsigned a, unsigned b) { return __builtin_amdgcn_permlane16_swap(a, b, false, false); }
DI u32x2 swap32(unsigned a, unsigned b) { return __builtin_amdgcn_permlane32_swap(a, b, false, false); }
DI bf16x4 tr_read(const char* p) {
  typedef short v4i16_t __attribute__((ext_vector_type(4)));
  return __builtin_bit_cast(bf16x4, __builtin_amdgcn_ds_read_tr16_b64_v4i16((__attribute__((address_space(3))) v4i16_t*)(p)));
}
template <bool SC = false> DI void glds16(const void* g, const char* l) {
  unsigned sv; const unsigned lb = __builtin_amdgcn_readfirstlane((unsigned)(uintptr_t)l);
  if (SC) asm volatile("s_mov_b32 %0, m0\n\ts_mov_b32 m0, %2\n\ts_nop 0\n\tglobal_load_lds_dwordx4 %1, off sc1\n\ts_mov_b32 m0, %0"
               : "=&s"(sv) : "v"(g), "s"(lb) : "memory");
  else asm volatile("s_mov_b32 %0, m0\n\ts_mov_b32 m0, %2\n\ts_nop 0\n\tglobal_load_lds_dwordx4 %1, off\n\ts_mov_b32 m0, %0"
               : "=&s"(sv) : "v"(g), "s"(lb) : "memory");
}
DI void gtouch(const void* g, const char* l) {
  unsigned sv; const unsigned lb = __builtin_amdgcn_readfirstlane((unsigned)(uintptr_t)l);
  asm volatile("s_mov_b32 %0, m0\n\ts_mov_b32 m0, %2\n\ts_nop 0\n\tglobal_load_lds_dword %1, off\n\ts_mov_b32 m0, %0"
               : "=&s"(sv) : "v"(g), "s"(lb) : "memory");
}
#define KLABEL(pfx, id) asm volatile(pfx "%c0:" ::"i"(id))
template <int ID, bool END> DI unsigned long long kaddr() {
  unsigned long long a;
  if (END) asm volatile("s_getpc_b64 s[20:21]\n\ts_add_u32 s20, s20, kend_%c1@rel32@lo+4\n\ts_addc_u32 s21, s21, kend_%c1@rel32@hi+12\n\ts_mov_b64 %0, s[20:21]" : "=s"(a) : "i"(ID) : "s20", "s21", "scc");
  else asm volatile("s_getpc_b64 s[20:21]\n\ts_add_u32 s20, s20, kbeg_%c1@rel32@lo+4\n\ts_addc_u32 s21, s21, kbeg_%c1@rel32@hi+12\n\ts_mov_b64 %0, s[20:21]" : "=s"(a) : "i"(ID) : "s20", "s21", "scc");
  return a;
}
template <int ID> DI void touch_code(const char* dummy, int lane) {
  const unsigned long long lo = kaddr<ID, false>() & ~255ull;
  unsigned long long hi = kaddr<ID, true>();
  if (ID < 5) { const unsigned long long e = kaddr<(ID < 5 ? ID + 7 : ID), true>(); hi = e > hi ? e : hi; }
  for (unsigned o = lane * 128; o < (unsigned)(hi - lo); o += 8192) gtouch((const char*)lo + o, dummy);
}
#define TC 1
DI u32x2 gload8(const void* g) {
  u32x2 r; asm volatile("global_load_dwordx2 %0, %1, off" : "=v"(r) : "v"(g) : "memory"); return r;
}
DI unsigned gload4(const void* g) {
  unsigned r; asm volatile("global_load_dword %0, %1, off" : "=v"(r) : "v"(g) : "memory"); return r;
}
DI u32x4 gload16(const void* g) {
  u32x4 r; asm volatile("global_load_dwordx4 %0, %1, off" : "=v"(r) : "v"(g) : "memory"); return r;
}
DI u32x4 gload16sc(const void* g) {
  u32x4 r; asm volatile("global_load_dwordx4 %0, %1, off sc1" : "=v"(r) : "v"(g) : "memory"); return r;
}
DI void st4sc(void* p, float v) { asm volatile("global_store_dword %0, %1, off sc1" ::"v"(p), "v"(v) : "memory"); }
DI void st16sc(void* p, u32x4 v) { asm volatile("global_store_dwordx4 %0, %1, off sc1\n\ts_nop 1" ::"v"(p), "v"(v)); }
#define ST_MODE 14
template <int CLS> DI void st16(void* p, u32x4 v) {
  if ((ST_MODE >> CLS) & 1) asm volatile("global_store_dwordx4 %0, %1, off sc1\n\ts_nop 1" ::"v"(p), "v"(v));
  else *(u32x4*)p = v;
}
template <int IMM, bool WT = true> DI void stw(const void* base, unsigned off, u32x4 v) {
  if (WT) asm volatile("global_store_dwordx4 %0, %1, %2 offset:%3 sc1\n\ts_nop 1" ::"v"(off), "v"(v), "s"(base), "i"(IMM));
  else asm volatile("global_store_dwordx4 %0, %1, %2 offset:%3\n\ts_nop 1" ::"v"(off), "v"(v), "s"(base), "i"(IMM));
}
template <int CLS> DI void st8(void* p, u32x2 v) {
  if ((ST_MODE >> CLS) & 1) asm volatile("global_store_dwordx2 %0, %1, off sc1" ::"v"(p), "v"(v) : "memory");
  else *(u32x2*)p = v;
}
template <int I> struct IC { static constexpr int v = I; };
template <int L, int NMAX> DI void wait_stages(int n) {
  if constexpr (NMAX == 0) wait_vm<0>();
  else { if (n >= NMAX) wait_vm<NMAX * L>(); else wait_stages<L, NMAX - 1>(n); }
}
template <int N, int I = 0, class F> DI void static_for(F&& f) {
  if constexpr (I < N) { f(IC<I>{}); static_for<N, I + 1>(f); }
}
#define SB() __builtin_amdgcn_sched_barrier(0)
template <int NS> DI unsigned foff(int row, int col) {
  return (unsigned)((((row >> 6) * NS + (col >> 6)) * 8 + ((row >> 4) & 3) * 2 + ((col >> 5) & 1)) * 1024 +
                    (((col >> 3) & 3) * 16 + (row & 15)) * 16 + (col & 7) * 2);
}

enum { EPI_QKV = 0, EPI_RES = 1, EPI_GEGLU = 2 };
constexpr int L_TOTAL = 163840, L_SMALL = L_TOTAL - 2304, L_XB = L_SMALL - 20480, L_XHI = L_XB + 8192;
DI int xb_off(int w, int j) { return j < 3 ? L_XHI + (w * 3 + j) * 1024 : L_XB + (w * 2 + j - 3) * 1024; }

template <int BM_, int BN_, int KTOT_, int NST_, int EPI_, bool RAW_> struct Cfg {
  static constexpr int BM = BM_, BN = BN_, KTOT = KTOT_, NST = NST_, EPI = EPI_;
  static constexpr bool RAW = RAW_, RES = (EPI_ == EPI_RES), LNP = !RES && !RAW_;
  static constexpr int MT = RES ? 2 : 4, NT = RES ? 5 : BN_ / 32, KT = KTOT_ / 64;
  static constexpr int PA = BM_ / 8, PB = BN_ / 8, P = PA + PB, LMAX = (P + 3) / 4;
  static constexpr int ABYTES = BM_ * 128, STB = P * 1024;
  static constexpr int RS = BN_ * 2;
};
typedef Cfg<128, 128, 640, 4, EPI_QKV, true> CQ0;
typedef Cfg<128, 128, 640, 4, EPI_QKV, false> CQ1;
typedef Cfg<64, 80, 640, 8, EPI_RES, false> CWO;
typedef Cfg<128, 224, 640, 3, EPI_GEGLU, false> CGU;
typedef Cfg<64, 80, 1728, 8, EPI_RES, false> CDN;

template <class C> DI void tile_of(int bid, int& tm, int& tn) {
  const int xcd = bid & 7, idx = bid >> 3;
  if (C::RES) { tm = xcd * 4 + (idx & 1) * 2 + ((idx >> 1) & 1); tn = idx >> 2; }
  else { tm = xcd * 2 + (idx & 1); tn = idx >> 1; }
}
DI int opaque(int x) { asm volatile("" : "+s"(x)); return x; }
DI int opaque_v(int x) { asm volatile("" : "+v"(x)); return x; }

template <int KT, int N, int L, int LA, int E> constexpr int ld_allowed(int i) {
  const int full = i - 1 < 0 ? 0 : (i - 1 > KT - N ? KT - N : i - 1);
  return (i < N ? (N - 1 - i) * LA + full * L : (N + full - 1 - i) * L) + (i > KT - N ? E : 0);
}

template <class C, bool SEAM> DI void loader_phase(char* smem, const char* A, const char* B, const char* ws, size_t stat, int bid, int tid) {
  constexpr int KT = C::KT, NST = C::NST, PA = C::PA, PB = C::PB, P = C::P, LMAX = C::LMAX, STB = C::STB, BN = C::BN;
  constexpr bool RAW = C::RAW, LNP = C::LNP;
  const int lane = tid & 63, lw = __builtin_amdgcn_readfirstlane(tid >> 6) - 4;
  int tm, tn; tile_of<C>(bid, tm, tn);
  const int m0 = tm * C::BM, n0 = tn * BN;
  const char* src[LMAX];
#pragma unroll
  for (int i = 0; i < LMAX; ++i) {
    const int p = lw + 4 * i;
    if (RAW) {
      if (i < PA / 4) {
        const int row = p * 8 + (lane >> 3), c = (lane & 7) ^ ((row >> 1) & 7);
        src[i] = A + ((size_t)(m0 + row) * C::KTOT + c * 8) * 2;
      } else {
        const int u = (p - PA) * 64 + lane, prow = u >> 4, c16 = u & 15;
        const int ch = (c16 >> 1) ^ (prow & 7);
        const int k = (prow & 32) + 8 * (((prow >> 3) & 1) * 2 + ((prow >> 2) & 1)) + 4 * ((prow >> 4) & 1) + (prow & 3);
        src[i] = B + ((size_t)k * (3 * DM) + n0 + ch * 16 + (c16 & 1) * 8) * 2;
      }
    } else {
      if (i < PA / 4) src[i] = A + ((size_t)((m0 >> 6) + (p >> 3)) * KT * 8 + (p & 7)) * 1024 + lane * 16;
      else src[i] = B + ((size_t)tn * KT * PB + (p - PA < PB ? p - PA : 0)) * 1024 + lane * 16;
    }
  }
  constexpr int STR_A = RAW ? 128 : 8192, STR_B = RAW ? 64 * 3 * DM * 2 : PB * 1024;
  auto body = [&](auto LC) {
    constexpr int L = decltype(LC)::v, LA = PA / 4;
    auto issue = [&](int buf, auto I0, auto I1) {
      char* base = smem + buf * STB + lw * 1024;
#pragma unroll
      for (int i = decltype(I0)::v; i < decltype(I1)::v; ++i) {
        if (i < LA) glds16<SEAM>(src[i], base + i * 4096); else glds16(src[i], base + i * 4096);
        src[i] += (i < LA) ? STR_A : STR_B;
      }
    };
    const int t = tid - 256;
    unsigned c[4]; u32x4 st[4];
    auto ln_loads = [&]() {
      const float* cp = (const float*)(ws + (C::EPI == EPI_QKV ? WS_CQKV : WS_CGU)) + tn * 4 * BN + (t < BN ? t : 0);
      const char* sp = ws + stat + (size_t)(m0 + (t & 127)) * 64;
#pragma unroll
      for (int k = 0; k < 4; ++k) c[k] = gload4(cp + k * BN);
#pragma unroll
      for (int k = 0; k < 4; ++k) st[k] = gload16sc(sp + k * 16);
    };
    if constexpr (SEAM) {
      constexpr int E = LNP ? 8 : 0;
#pragma unroll
      for (int s = 0; s < NST; ++s) issue(s, IC<LA>{}, IC<L>{});
      raw_barrier();
      raw_barrier();
#pragma unroll
      for (int s = 0; s < NST; ++s) issue(s, IC<0>{}, IC<LA>{});
      auto iter = [&](auto I) {
        constexpr int i = decltype(I)::v;
        wait_vm<ld_allowed<KT, NST, L, LA, E>(i)>();
        raw_barrier();
        if (i >= 1 && i <= KT - NST) issue((i - 1) % NST, IC<0>{}, IC<L>{});
        if (LNP && i == KT - NST) ln_loads();
      };
      if constexpr (KT >= 3 * NST) {
        static_for<NST>(iter);
        int ibuf = NST - 1;
#pragma unroll 1
        for (int i = NST; i < KT - NST; ++i) {
          wait_vm<(NST - 2) * L>();
          raw_barrier();
          issue(ibuf, IC<0>{}, IC<L>{});
          if (++ibuf == NST) ibuf = 0;
        }
        static_for<KT, KT - NST>(iter);
      } else static_for<KT>(iter);
    } else {
      issue(0, IC<0>{}, IC<L>{}); issue(1, IC<0>{}, IC<L>{});
      int nxt = 2, ibuf = 2;
      constexpr int NMAIN = LNP ? KT - NST + 1 : KT;
      for (int i = 0; i < NMAIN; ++i) {
        wait_stages<L, NST - 1>(nxt - i - 1);
        raw_barrier();
        const int lim = min(KT, i + NST);
#pragma unroll
        for (int r = 0; r < 2; ++r)
          if (nxt < lim) { issue(ibuf, IC<0>{}, IC<L>{}); ++nxt; if (++ibuf == NST) ibuf = 0; }
      }
      if (LNP) {
        ln_loads();
        static_for<NST - 1>([&](auto I) { wait_vm<(NST - 2 - decltype(I)::v) * L + 8>(); raw_barrier(); });
      }
    }
    if (LNP) {
      float2* rowst = (float2*)(smem + L_SMALL);
      float* colsum = (float*)(smem + L_SMALL + 1024);
      wait_vm<0>();
      float cs = 0.f;
#pragma unroll
      for (int k = 0; k < 4; ++k) { asm volatile("" : "+v"(c[k])); cs += __uint_as_float(c[k]); }
#pragma unroll
      for (int k = 0; k < 4; ++k) asm volatile("" : "+v"(st[k]));
      const f32x4 sv = (__builtin_bit_cast(f32x4, st[0]) + __builtin_bit_cast(f32x4, st[1])) + (__builtin_bit_cast(f32x4, st[2]) + __builtin_bit_cast(f32x4, st[3]));
      const float mean = (sv[0] + sv[2]) * (1.0f / C::KTOT);
      const float var = fmaxf((sv[1] + sv[3]) * (1.0f / C::KTOT) - mean * mean, 0.f);
      if (t < BN) colsum[t] = cs;
      if (t >= 128) rowst[t - 128] = make_float2(mean, rsqrtf(var + LN_EPS));
    }
    raw_barrier();
  };
  if (P % 4 == 0 || lw < P % 4) body(IC<LMAX>{}); else body(IC<LMAX - 1>{});
}

DI void seam(char* ws, int id, int bid, int tid) {
  unsigned* cnt = (unsigned*)(ws + WS_CNT + (size_t)(id * 16 + (bid & 7) * 2 + ((bid >> 3) & 1)) * 256);
  wait_vm<0>();
  raw_barrier();
  if (tid == 0) __hip_atomic_fetch_add(cnt, 1u, __ATOMIC_RELAXED, __HIP_MEMORY_SCOPE_AGENT);
  if (tid == 64) {
    for (int s = 0; s < (1 << 18); ++s) {
      if (__hip_atomic_load(cnt, __ATOMIC_RELAXED, __HIP_MEMORY_SCOPE_AGENT) >= 16u) break;
      __builtin_amdgcn_s_sleep(1);
    }
  }
  raw_barrier();
}

template <class C, int MODE> DI void compute_res(char* smem, const char* res, bool res_raw, char* out, bool out_raw, char* ws, size_t stat, int bid, int tid, int KID) {
  constexpr int KT = C::KT, NST = C::NST, NT = C::NT, STB = C::STB, ABYTES = C::ABYTES;
  const int lane = tid & 63, wid = __builtin_amdgcn_readfirstlane(tid >> 6), fr = lane & 15, fg = lane >> 4, wm = wid >> 1, wn = wid & 1;
  int tm, tn; tile_of<C>(bid, tm, tn);
  const int m0 = tm * C::BM, n0 = tn * C::BN;
  f32x4 acc[2][NT];
#pragma unroll
  for (int i = 0; i < 2; ++i)
#pragma unroll
    for (int j = 0; j < NT; ++j) acc[i][j] = (f32x4){0.f, 0.f, 0.f, 0.f};
  const int row = m0 + wid * 16 + fr;
  u32x2 resv[NT];
#pragma unroll
  for (int j = 0; j < NT; ++j) {
    const int col = n0 + j * 16 + fg * 4;
    if (MODE == 0) resv[j] = *(const u32x2*)(res + (res_raw ? (unsigned)(row * DM + col) * 2 : foff<10>(row, col)));
    else resv[j] = *(const u32x2*)(smem + L_XHI + wid * 3072 + j * 512 + lane * 8);
  }
  const int aoff = (wm * 4 + wn) * 1024 + lane * 16, boff = ABYTES + wn * 1024 + lane * 16;
  bf16x8 a0[2], b0[NT], a1[2], b1[NT];
  auto rd = [&](bf16x8(&a)[2], bf16x8(&b)[NT], const char* base) {
#pragma unroll
    for (int i = 0; i < 2; ++i) a[i] = *(const bf16x8*)(base + aoff + i * 2048);
#pragma unroll
    for (int j = 0; j < NT; ++j) b[j] = *(const bf16x8*)(base + boff + j * 2048);
    SB();
  };
  auto mm = [&](bf16x8(&a)[2], bf16x8(&b)[NT]) {
#pragma unroll
    for (int j = 0; j < NT; ++j)
#pragma unroll
      for (int i = 0; i < 2; ++i) acc[i][j] = __builtin_amdgcn_mfma_f32_16x16x32_bf16(b[j], a[i], acc[i][j], 0, 0, 0);
    SB();
  };
  STAMP(KID, 1);
  raw_barrier();
  STAMP(KID, 2); CSTAMP(KID, 2);
  rd(a0, b0, smem);
  int buf = 0;
#pragma unroll 1
  for (int kt = 0; kt + 2 < KT; kt += 2) {
    if (++buf == NST) buf = 0;
    raw_barrier();
    rd(a1, b1, smem + buf * STB);
    mm(a0, b0);
    if (++buf == NST) buf = 0;
    raw_barrier();
    rd(a0, b0, smem + buf * STB);
    mm(a1, b1);
  }
  if (KT % 2 == 0) {
    if (++buf == NST) buf = 0;
    raw_barrier();
    rd(a1, b1, smem + buf * STB);
    mm(a0, b0);
    mm(a1, b1);
  } else {
    mm(a0, b0);
  }
  STAMP(KID, 3); CSTAMP(KID, 3);
#pragma unroll
  for (int j = 0; j < NT; ++j) *(f32x4*)(smem + xb_off(wid ^ 1, j) + lane * 16) = wn ? acc[0][j] : acc[1][j];
  raw_barrier();
  STAMP(KID, 4);
  float s = 0.f, q = 0.f;
  u32x2 v[NT];
#pragma unroll
  for (int j = 0; j < NT; ++j) {
    const f32x4 o = *(const f32x4*)(smem + xb_off(wid, j) + lane * 16) + (wn ? acc[1][j] : acc[0][j]);
    const u32x2 r = resv[j];
    v[j] = (u32x2){cvtpk(o[0] + bflo(r[0]), o[1] + bfhi(r[0])), cvtpk(o[2] + bflo(r[1]), o[3] + bfhi(r[1]))};
    const float e0 = bflo(v[j][0]), e1 = bfhi(v[j][0]), e2 = bflo(v[j][1]), e3 = bfhi(v[j][1]);
    s += (e0 + e1) + (e2 + e3);
    q += (e0 * e0 + e1 * e1) + (e2 * e2 + e3 * e3);
  }
#pragma unroll
  for (int jp = 0; jp < NT; jp += 2) {
    const int j1 = jp + 1 < NT ? jp + 1 : jp;
    const u32x2 x = swap16(v[jp][0], v[j1][0]), y = swap16(v[jp][1], v[j1][1]);
    const int col = n0 + (jp + (fg & 1)) * 16 + (fg >> 1) * 8;
    if (jp + 1 < NT || !(fg & 1))
      st16sc(out + (out_raw ? (unsigned)(row * DM + col) * 2 : foff<10>(row, col)), (u32x4){x[0], y[0], x[1], y[1]});
  }
  {
    const u32x2 t1 = swap16(__float_as_uint(s), __float_as_uint(q));
    const unsigned t2 = __float_as_uint(__uint_as_float(t1[0]) + __uint_as_float(t1[1]));
    const u32x2 t3 = swap32(t2, t2);
    if (lane < 32) st4sc(ws + stat + (size_t)(row * 8 + tn) * 8 + fg * 4, __uint_as_float(t3[0]) + __uint_as_float(t3[1]));
  }
  if (MODE == 0) {
#pragma unroll
    for (int j = 0; j < NT; ++j) *(u32x2*)(smem + L_XHI + wid * 3072 + j * 512 + lane * 8) = v[j];
  }
  STAMP(KID, 5);
}

template <class C> DI void compute_ln(char* smem, char* out, char* ws, int bid, int tid, int KID) {
  constexpr int KT = C::KT, NST = C::NST, NT = C::NT, MT = C::MT, STB = C::STB, ABYTES = C::ABYTES, RS = C::RS, BN = C::BN, EPI = C::EPI, KTOT = C::KTOT;
  constexpr bool RAW = C::RAW;
  const int lane = tid & 63, wid = __builtin_amdgcn_readfirstlane(tid >> 6), fr = lane & 15, fg = lane >> 4, wm = wid >> 1, wn = wid & 1;
  int tm, tn; tile_of<C>(bid, tm, tn);
  const int m0 = tm * C::BM, n0 = tn * BN;
  float2* rowst = (float2*)(smem + L_SMALL);
  float* colsum = (float*)(smem + L_SMALL + 1024);
  const char* dummy = smem + L_SMALL + 2048;
  f32x4 acc[MT][NT];
#pragma unroll
  for (int i = 0; i < MT; ++i)
#pragma unroll
    for (int j = 0; j < NT; ++j) acc[i][j] = (f32x4){0.f, 0.f, 0.f, 0.f};
  if (EPI == EPI_QKV && tn >= 10) {
    const int vc0 = n0 - 2 * DM, head = (vc0 + DH - 1) / DH + (tid >> 4);
    if (head * DH < vc0 + BN) {
      const int tok = m0 + ((tid >> 1) & 7) * 16;
      char* p = ws + WS_KV + (size_t)((head >> 1) * 16 + (tok >> 7)) * KVP_CHUNK + KVP_V + (head & 1) * 8192 + ((tok >> 4) & 7) * 1024 + ((tid & 1) * 32 + DH) * 16;
      *(u32x4*)p = (u32x4){0x3f803f80u, 0x3f803f80u, 0x3f803f80u, 0x3f803f80u};
    }
  }
  int aoff[2], boff[RAW ? NT : 1];
  const int pr = 8 * (fg >> 1) + 4 * (fg & 1) + (fr >> 2);
#pragma unroll
  for (int ks = 0; ks < 2; ++ks)
    aoff[ks] = RAW ? (wm * 64 + fr) * 128 + (((ks * 4 + fg) ^ (fr >> 1)) * 16) : (wm * 8 + ks) * 1024 + lane * 16;
  if (RAW) {
#pragma unroll
    for (int j = 0; j < NT; ++j) boff[j] = ABYTES + pr * RS + ((wn * NT + j) ^ (pr & 7)) * 32 + (lane & 3) * 8;
  } else boff[0] = ABYTES + wn * NT * 2048 + lane * 16;
  f32x4 srow[2], sgr[2], scol[2];
#pragma unroll
  for (int i = 0; i < 2; ++i) srow[i] = sgr[i] = scol[i] = (f32x4){0.f, 0.f, 0.f, 0.f};
  const bf16x8 ones = (bf16x8){0x3f80, 0x3f80, 0x3f80, 0x3f80, 0x3f80, 0x3f80, 0x3f80, 0x3f80};

  const bool swp = !(EPI == EPI_QKV && tn >= 10);
  unsigned so[2][2];
  if (EPI == EPI_QKV) {
    const bool isq = tn < DM / BN;
#pragma unroll
    for (int jq = 0; jq < 2; ++jq) {
      if (swp) {
        const int G = ((isq ? n0 : n0 - DM) >> 3) + (wn * NT + jq * 2 + (fg & 1)) * 2 + (fg >> 1), pair = G / 5, gi = G % 5;
        const int mo = (((gi + (gi > 2)) & 1) * 32 + fr) * 16;
        if (isq) {
          so[jq][0] = (unsigned)WS_Q + (pair * 64 + (m0 >> 5) + wm * 2) * 2560 + (gi == 2 ? 2048 + fr * 16 : (gi > 2) * 1024 + mo);
          so[jq][1] = so[jq][0] + 2560;
        } else {
          so[jq][0] = (unsigned)WS_KV + (pair * 16 + (m0 >> 7)) * KVP_CHUNK + (gi == 2 ? KVP_TAIL + (wm * 64 + fr) * 16 : (gi > 2) * 4096 + wm * 2048 + mo);
          so[jq][1] = so[jq][0] + (gi == 2 ? 512 : 1024);
        }
      } else {
        const int cc = n0 - 2 * DM + (wn * NT + jq * 2 + (lane >> 5)) * 16 + fr, head = cc / DH, d = cc % DH;
        so[jq][0] = (unsigned)WS_KV + ((head >> 1) * 16 + (m0 >> 7)) * KVP_CHUNK + KVP_V + (head & 1) * 8192 + wm * 4096 + ((fg & 1) * 32 + d) * 16;
        so[jq][1] = so[jq][0] + 2048;
      }
      asm volatile("" : "+v"(so[jq][0]), "+v"(so[jq][1]));
    }
  }
  auto mainloop = [&](auto SW, auto WN, auto WM) {
    constexpr bool SWP = decltype(SW)::v;
    constexpr int CWN = decltype(WN)::v, CWM = decltype(WM)::v;
    bf16x8 a0[MT], a1[MT], b0[NT], b1[NT];
    auto rdB = [&](const char* base, int j, int ks) {
      if (RAW) {
        const char* p = base + boff[RAW ? j : 0] + ks * 32 * RS;
        bf16x4 lo = tr_read(p), hi = tr_read(p + 16 * RS);
        return __builtin_shufflevector(lo, hi, 0, 1, 2, 3, 4, 5, 6, 7);
      } else return *(const bf16x8*)(base + boff[0] + j * 2048 + ks * 1024);
    };
    auto step = [&](bf16x8(&ac)[MT], bf16x8(&bc)[NT], bf16x8(&an)[MT], bf16x8(&bn)[NT], const char* nb, int nks, auto RD) {
      constexpr bool rd = decltype(RD)::v;
      constexpr int RPG = 3;
#pragma unroll
      for (int j = 0; j < NT; ++j) {
#pragma unroll
        for (int i = 0; i < MT; ++i) {
          acc[i][j] = SWP ? __builtin_amdgcn_mfma_f32_16x16x32_bf16(bc[j], ac[i], acc[i][j], 0, 0, 0)
                          : __builtin_amdgcn_mfma_f32_16x16x32_bf16(ac[i], bc[j], acc[i][j], 0, 0, 0);
          const int r = j * RPG + i;
          if (rd && i < RPG && r < MT + NT) {
            if (r == 0) bn[0] = rdB(nb, 0, nks);
            else if (r <= MT) an[r - 1] = *(const bf16x8*)(nb + aoff[nks] + (r - 1) * 2048);
            else bn[r - MT] = rdB(nb, r - MT, nks);
            SB();
          }
        }
        if (RAW) {
          if ((j >> 1) == CWM) scol[j & 1] = __builtin_amdgcn_mfma_f32_16x16x32_bf16(bc[j], ones, scol[j & 1], 0, 0, 0);
          else {
            srow[j & 1] = __builtin_amdgcn_mfma_f32_16x16x32_bf16(ones, ac[CWN * 2 + (j & 1)], srow[j & 1], 0, 0, 0);
            sgr[j & 1] = __builtin_amdgcn_mfma_f32_16x16x32_bf16(ac[CWN * 2 + (j & 1)], ac[CWN * 2 + (j & 1)], sgr[j & 1], 0, 0, 0);
          }
        }
        SB();
      }
    };
    STAMP(KID, 1);
    raw_barrier();
    STAMP(KID, 2); CSTAMP(KID, 2);
#pragma unroll
    for (int j = 0; j < NT; ++j) { b0[j] = rdB(smem, j, 0); if (j < MT) a0[j] = *(const bf16x8*)(smem + aoff[0] + j * 2048); }
    step(a0, b0, a1, b1, smem, 1, IC<1>{});
    int buf = 0;
#pragma unroll 1
    for (int kt = 1; kt < KT; ++kt) {
      if (++buf == NST) buf = 0;
      raw_barrier();
      step(a1, b1, a0, b0, smem + buf * STB, 0, IC<1>{});
      step(a0, b0, a1, b1, smem + buf * STB, 1, IC<1>{});
    }
    step(a1, b1, a0, b0, smem, 0, IC<0>{});
  };
  if (RAW) {
    if (swp) {
      if (wid == 0) mainloop(IC<1>{}, IC<0>{}, IC<0>{}); else if (wid == 1) mainloop(IC<1>{}, IC<1>{}, IC<0>{});
      else if (wid == 2) mainloop(IC<1>{}, IC<0>{}, IC<1>{}); else mainloop(IC<1>{}, IC<1>{}, IC<1>{});
    } else {
      if (wid == 0) mainloop(IC<0>{}, IC<0>{}, IC<0>{}); else if (wid == 1) mainloop(IC<0>{}, IC<1>{}, IC<0>{});
      else if (wid == 2) mainloop(IC<0>{}, IC<0>{}, IC<1>{}); else mainloop(IC<0>{}, IC<1>{}, IC<1>{});
    }
    STAMP(KID, 3); CSTAMP(KID, 3);
    if (TC && wid == 0) touch_code<6>(dummy, lane);
#pragma unroll
    for (int i = 0; i < 2; ++i) {
      const float sq = (fr & 2) ? ((fr & 1) ? sgr[i][3] : sgr[i][2]) : ((fr & 1) ? sgr[i][1] : sgr[i][0]);
      const float mean = srow[i][0] * (1.0f / KTOT);
      const float var = fmaxf(sq * (1.0f / KTOT) - mean * mean, 0.f);
      if (fg == (fr >> 2)) rowst[wm * 64 + (wn * 2 + i) * 16 + fr] = make_float2(mean, rsqrtf(var + LN_EPS));
      if (fr == 0) *(f32x4*)(colsum + (wn * NT + wm * 2 + i) * 16 + fg * 4) = scol[i];
    }
    raw_barrier();
  } else {
    if (swp) mainloop(IC<1>{}, IC<0>{}, IC<0>{}); else mainloop(IC<0>{}, IC<0>{}, IC<0>{});
    STAMP(KID, 3); CSTAMP(KID, 3);
    if (TC && EPI == EPI_QKV && wid == 0) touch_code<5>(dummy, lane);
    raw_barrier();
  }
  STAMP(KID, 4);

  if constexpr (EPI == EPI_GEGLU) {
    const int hbase = (tn >> 1) * 216 + (tn & 1) * 112;
    const int orow = m0 + wm * 64 + fg * 16 + fr;
    constexpr float C1 = -2.0f * 0.7978845608028654f * 1.4426950408889634f, C2 = C1 * 0.044715f;
    f32x2 nm[MT], k1[MT], k2[MT], iv[MT];
#pragma unroll
    for (int i = 0; i < MT; ++i) {
      const float2 ms = rowst[wm * 64 + i * 16 + fr];
      const float r2 = ms.y * ms.y, ir2 = __builtin_amdgcn_rcpf(r2);
      nm[i] = (f32x2){-ms.x, -ms.x}; k1[i] = (f32x2){C1 * ms.y, C1 * ms.y}; k2[i] = (f32x2){C2 * ms.y * r2, C2 * ms.y * r2}; iv[i] = (f32x2){ir2, ir2};
    }
    f32x2 cgs[NT], cus[NT];
#pragma unroll
    for (int j = 0; j < NT; ++j) { cgs[j] = *(const f32x2*)(colsum + (wn * NT + j) * 16 + fg * 2); cus[j] = *(const f32x2*)(colsum + (wn * NT + j) * 16 + fg * 2 + 8); }
    const unsigned ro = foff<27>(orow, 0);
#pragma unroll
    for (int j = 0; j < NT; ++j) {
      const f32x2 cg = cgs[j], cu = cus[j];
      unsigned d[MT];
#pragma unroll
      for (int i = 0; i < MT; ++i) {
        const f32x2 gg = __builtin_elementwise_fma(nm[i], cg, (f32x2){acc[i][j][0], acc[i][j][1]});
        const f32x2 uu = __builtin_elementwise_fma(nm[i], cu, (f32x2){acc[i][j][2], acc[i][j][3]});
        const f32x2 t = gg * __builtin_elementwise_fma(gg * gg, k2[i], k1[i]);
        const f32x2 e = (f32x2){__builtin_amdgcn_exp2f(t[0]), __builtin_amdgcn_exp2f(t[1])};
        const f32x2 q = __builtin_elementwise_fma(e, iv[i], iv[i]);
        const f32x2 h = (gg * uu) * (f32x2){__builtin_amdgcn_rcpf(q[0]), __builtin_amdgcn_rcpf(q[1])};
        d[i] = cvtpk(h[0], h[1]);
      }
      const u32x2 x = swap32(d[0], d[2]), y = swap32(d[1], d[3]);
      const u32x2 z = swap16(x[0], y[0]), w = swap16(x[1], y[1]);
      const int jb = wn * NT + j;
      if (jb < 13 || !(tn & 1)) stw<0>(out, ro + foff<27>(0, hbase + jb * 8), (u32x4){z[0], z[1], w[0], w[1]});
    }
  } else {
    if (swp) {
      const float sc = tn < DM / BN ? QSCALE : 1.0f;
      float2 ms[MT]; f32x4 cs[NT];
#pragma unroll
      for (int i = 0; i < MT; ++i) ms[i] = rowst[wm * 64 + i * 16 + fr];
#pragma unroll
      for (int j = 0; j < NT; ++j) cs[j] = *(const f32x4*)(colsum + (wn * NT + j) * 16 + fg * 4);
      static_for<8>([&](auto I) {
        constexpr int jq = decltype(I)::v >> 2, i = decltype(I)::v & 3;
        const float rs = ms[i].y * sc;
        const f32x4 o0 = rs * (acc[i][jq * 2] - ms[i].x * cs[jq * 2]), o1 = rs * (acc[i][jq * 2 + 1] - ms[i].x * cs[jq * 2 + 1]);
        const u32x2 x = swap16(cvtpk(o0[0], o0[1]), cvtpk(o1[0], o1[1])), y = swap16(cvtpk(o0[2], o0[3]), cvtpk(o1[2], o1[3]));
        stw<(i & 1) * 256>(ws, so[jq][i >> 1], (u32x4){x[0], y[0], x[1], y[1]});
      });
    } else {
      f32x4 ma[MT], mb[MT]; float cs[NT];
#pragma unroll
      for (int i = 0; i < MT; ++i) { ma[i] = *(const f32x4*)(rowst + wm * 64 + i * 16 + fg * 4); mb[i] = *(const f32x4*)(rowst + wm * 64 + i * 16 + fg * 4 + 2); }
#pragma unroll
      for (int j = 0; j < NT; ++j) cs[j] = colsum[(wn * NT + j) * 16 + fr];
      static_for<8>([&](auto I) {
        constexpr int jq = decltype(I)::v >> 2, i = decltype(I)::v & 3;
        const float c0 = cs[jq * 2], c1 = cs[jq * 2 + 1];
        const f32x4 &p0 = acc[i][jq * 2], &p1 = acc[i][jq * 2 + 1], &a = ma[i], &b = mb[i];
        const u32x2 x = swap32(cvtpk(a[1] * (p0[0] - a[0] * c0), a[3] * (p0[1] - a[2] * c0)), cvtpk(a[1] * (p1[0] - a[0] * c1), a[3] * (p1[1] - a[2] * c1)));
        const u32x2 y = swap32(cvtpk(b[1] * (p0[2] - b[0] * c0), b[3] * (p0[3] - b[2] * c0)), cvtpk(b[1] * (p1[2] - b[0] * c1), b[3] * (p1[3] - b[2] * c1)));
        stw<(i & 1) * 1024>(ws, so[jq][i >> 1], (u32x4){x[0], y[0], x[1], y[1]});
      });
    }
  }
  STAMP(KID, 5);
}

struct Q0Args { const char* x; const char* wqkv; char* ws; };
__global__ __launch_bounds__(512) void qkv0_kernel(Q0Args g) {
  __shared__ __attribute__((aligned(16))) char smem[L_TOTAL];
  KLABEL("kbeg_", 0);
  const int tid = threadIdx.x, bid = blockIdx.x;
  if (tid < 256) STAMP(KID, 0);
  if (bid >= 240) {
    if (bid == 240 && tid < 96) *(unsigned*)(g.ws + WS_CNT + tid * 256) = 0u;
    return;
  }
  if (tid >= 256) {
    loader_phase<CQ0, false>(smem, g.x, g.wqkv, g.ws, WS_STAT, bid, tid);
    KLABEL("kend_", 7);
    return;
  }
  compute_ln<CQ0>(smem, nullptr, g.ws, bid, tid, 0);
  STAMP(KID, 6);
  KLABEL("kend_", 0);
}

struct ChainArgs { const char* xin; char* xmid; char* xout; char* ws; long layer; };
__global__ __launch_bounds__(512) void chain_kernel(ChainArgs g) {
  __shared__ __attribute__((aligned(16))) char smem[L_TOTAL];
  KLABEL("kbeg_", 1);
  const int tid = threadIdx.x, bid = blockIdx.x, layer = (int)g.layer;
  char* ws = g.ws;
  if (tid >= 256) {
    loader_phase<CWO, false>(smem, ws + WS_O, ws + WS_PWO, ws, WS_STAT, opaque(bid), opaque_v(tid));
    loader_phase<CGU, true>(smem, g.xmid, ws + WS_PGU, ws, WS_STAT, opaque(bid), opaque_v(tid));
    loader_phase<CDN, true>(smem, ws + WS_H, ws + WS_PWD, ws, WS_STAT, opaque(bid), opaque_v(tid));
    if (layer == 0) {
      if (bid < 240) loader_phase<CQ1, true>(smem, g.xout, ws + WS_PQKV, ws, WS_STAT2, opaque(bid), opaque_v(tid));
      else { raw_barrier(); raw_barrier(); }
    }
    KLABEL("kend_", 8);
    return;
  }
  STAMP(K0 + 2, 0);
  compute_res<CWO, 0>(smem, g.xin, layer == 0, g.xmid, false, ws, WS_STAT, opaque(bid), opaque_v(tid), layer * 5 + 2);
  STAMP(K0 + 2, 6); STAMP(K0 + 3, 0);
  seam(ws, layer * 3, opaque(bid), opaque_v(tid));
  compute_ln<CGU>(smem, ws + WS_H, ws, opaque(bid), opaque_v(tid), layer * 5 + 3);
  STAMP(K0 + 3, 6); STAMP(K0 + 4, 0);
  seam(ws, layer * 3 + 1, opaque(bid), opaque_v(tid));
  compute_res<CDN, 1>(smem, nullptr, false, g.xout, layer == 1, ws, WS_STAT2, opaque(bid), opaque_v(tid), layer * 5 + 4);
  if (layer == 0) {
    STAMP(K0 + 4, 6); STAMP(K0 + 5, 0);
    seam(ws, 2, opaque(bid), opaque_v(tid));
    if (bid < 240) compute_ln<CQ1>(smem, nullptr, ws, opaque(bid), opaque_v(tid), 5);
  }
  KLABEL("kend_", 1);
}

constexpr int ATT_NBUF = 4, ATT_HALF = 14336, ATT_STB = 2 * ATT_HALF, ATT_V = 6144;
struct AttnArgs { char* ws; const u16* wqkv; const u16* wo; const u16* wg; const u16* wu; const u16* wd;
};

#define EX(x) __builtin_amdgcn_exp2f(x)
#define E4(P, I, S, J) P[I] = cvtpk(EX(S[J]), EX(S[J + 1])); P[I + 1] = cvtpk(EX(S[J + 2]), EX(S[J + 3]));
#define MF16(a, b, c) __builtin_amdgcn_mfma_f32_32x32x16_bf16(a, b, c, 0, 0, 0)
#define MF8(a, b, c) __builtin_amdgcn_mfma_f32_32x32x8bf16_1k(a, b, c, 0, 0, 0)
#define BC8(x) __builtin_bit_cast(bf16x8, x)

DI float rowmax16(const f32x16& s) {
  float m = s[0];
#pragma unroll
  for (int i = 1; i < 16; ++i) m = fmaxf(m, s[i]);
  return fmaxf(m, __shfl_xor(m, 32));
}
DI bool nonfinite12(const f32x16& o, int lane) {
  bool bad = false;
#pragma unroll
  for (int i = 0; i < 9; ++i) bad |= !(fabsf(o[i]) < 1.0e38f);
#pragma unroll
  for (int i = 9; i < 12; ++i) bad |= (lane < 32) && !(fabsf(o[i]) < 1.0e38f);
  return bad;
}

struct PackJob { const u16* src; int ld, kk0, kmax, s2; char* dst; float* cs; };
DI PackJob pack_job(const AttnArgs& a, int J, int wid, int lane) {
  const int prow = wid * 4 + (lane >> 4), c16 = lane & 15, ch = (c16 >> 1) ^ (prow & 7);
  const int k = 8 * (((prow >> 3) & 1) * 2 + ((prow >> 2) & 1)) + 4 * ((prow >> 4) & 1) + (prow & 3);
  const int fr = lane & 15, fg = lane >> 4;
  PackJob r;
  if (J >= 243) J -= 243;
  r.kmax = 19;
  if (J < 108) {
    const int cg = J >> 2, kq = J & 3, b = cg * 8 + wid, rr = b % 27, tn = (b / 27) * 2 + (rr >= 14), jb = rr - (rr >= 14) * 14;
    r.src = ((c16 & 1) ? a.wu : a.wg) + (size_t)k * DF + cg * 64 + ch * 8; r.ld = DF; r.kk0 = kq * 5; r.s2 = 28;
    r.dst = a.ws + WS_PGU + (size_t)(tn * 280 + jb * 2) * 1024 + (fg * 16 + ((fr & 7) >> 1) * 4 + (fr >> 3) * 2 + (fr & 1)) * 16;
    r.cs = (float*)(a.ws + WS_CGU) + (tn * 4 + kq) * 224 + jb * 16 + fg * 4;
  } else if (J < 168) {
    const int tn = (J - 108) >> 2, kq = (J - 108) & 3;
    r.src = a.wqkv + (size_t)k * (3 * DM) + tn * 128 + ch * 16 + (c16 & 1) * 8; r.ld = 3 * DM; r.kk0 = kq * 5; r.s2 = 16;
    r.dst = a.ws + WS_PQKV + (size_t)(tn * 160 + wid * 2) * 1024 + lane * 16;
    r.cs = (float*)(a.ws + WS_CQKV) + (tn * 4 + kq) * 128 + wid * 16 + fg * 4;
  } else {
    const bool d = J >= 188;
    const int cg = d ? (J - 188) / 11 : (J - 168) >> 2, kq = d ? (J - 188) % 11 : (J - 168) & 3, jb = cg * 8 + wid;
    r.src = (d ? a.wd : a.wo) + (size_t)k * DM + cg * 128 + ch * 16 + (c16 & 1) * 8; r.ld = DM; r.kk0 = kq * 5; r.s2 = 10;
    if (d) r.kmax = 53;
    r.dst = a.ws + (d ? WS_PWD : WS_PWO) + (size_t)((jb / 5) * (d ? 270 : 100) + (jb % 5) * 2) * 1024 + lane * 16;
    r.cs = nullptr;
  }
  return r;
}

template <bool PACK>
__global__ __launch_bounds__(512) void attn_kernel(AttnArgs a) {
  __shared__ __attribute__((aligned(16))) char smem[ATT_NBUF * ATT_STB + (PACK ? 5 * 8192 : 0) + 256];
  KLABEL("kbeg_", 5 + PACK);
  const int tid = threadIdx.x, lane = tid & 63, wid = __builtin_amdgcn_readfirstlane(tid >> 6);
  char* ws = a.ws;
  STAMP(KID, 0);
  const int bid = blockIdx.x, xcd = bid & 7, idx = bid >> 3;
  const int head = xcd * 4 + (idx >> 3), qb = idx & 7;
  const int r32 = lane & 31, hh = lane >> 5, half = wid >> 2;
  const int q0 = qb * 256 + (wid & 3) * 64 + r32;
  const char* kvbase = ws + WS_KV + (size_t)(head >> 1) * 16 * KVP_CHUNK + lane * 16;
  int so[4];
#pragma unroll
  for (int i = 0; i < 4; ++i) {
    int pc = wid + i * 8; if (pc > 27) pc = 27;
    const int hf = pc >= 14, q = pc - hf * 14;
    so[i] = hf * 8 * KVP_CHUNK + (q < 4 ? (head & 1) * 4096 + q * 1024 : q < 6 ? 4096 + q * 1024 : KVP_V + (head & 1) * 8192 + (q - 6) * 1024);
  }
  auto stage = [&](int s, int buf) {
#pragma unroll
    for (int i = 0; i < 4; ++i) {
      int pc = wid + i * 8; if (pc > 27) pc = 27;
      glds16(kvbase + (size_t)s * KVP_CHUNK + so[i], smem + buf * ATT_STB + pc * 1024);
    }
  };
  u32x4 qr1[2]; u32x2 qr2[2];
  {
    const char* Q = ws + WS_Q + (size_t)((head >> 1) * 64 + (q0 >> 5)) * 2560;
#pragma unroll
    for (int i = 0; i < 2; ++i) {
      qr1[i] = gload16(Q + i * 2560 + (head & 1) * 1024 + lane * 16);
      qr2[i] = gload8(Q + i * 2560 + 2048 + r32 * 16 + (head & 1) * 8);
    }
  }
  stage(0, 0); stage(1, 1); stage(2, 2);
  PackJob pj; const char* pp; bf16x4 plo, phi; f32x4 pcs = (f32x4){0.f, 0.f, 0.f, 0.f};
  if (PACK) {
    pj = pack_job(a, bid, wid, lane);
    const int pr = 8 * (lane >> 5) + 4 * ((lane >> 4) & 1) + ((lane & 15) >> 2);
    pp = smem + ATT_NBUF * ATT_STB + pr * 256 + (wid ^ (pr & 7)) * 32 + (lane & 3) * 8;
  }
  const char* dummy = smem + ATT_NBUF * ATT_STB + (PACK ? 5 * 8192 : 0);
  const char* kl = smem + half * ATT_HALF + lane * 16;
  const char* k8 = smem + half * ATT_HALF + 4096 + r32 * 16 + (head & 1) * 8;
  f32x16 oa, ob, nma, nmb, sa, sb;
#pragma unroll
  for (int i = 0; i < 16; ++i) { oa[i] = 0.f; ob[i] = 0.f; }
  wait_vm<8>();
#pragma unroll
  for (int i = 0; i < 2; ++i) asm volatile("" : "+v"(qr1[i]), "+v"(qr2[i]));
  const bf16x8 qa1 = BC8(qr1[0]), qb1 = BC8(qr1[1]);
  const bf16x4 qa2 = __builtin_bit_cast(bf16x4, hh ? (u32x2){0u, 0u} : qr2[0]), qb2 = __builtin_bit_cast(bf16x4, hh ? (u32x2){0u, 0u} : qr2[1]);
  raw_barrier();
  if (PACK) {
#pragma unroll
    for (int b = 0; b < 5; ++b) glds16(pj.src + (size_t)min(pj.kk0 + b, pj.kmax) * 32 * pj.ld, smem + ATT_NBUF * ATT_STB + b * 8192 + wid * 1024);
  }
  bf16x8 k1 = *(const bf16x8*)kl; bf16x4 k2 = *(const bf16x4*)k8;
  {
    sa = MF16(k1, qa1, oa); sa = MF8(k2, qa2, sa);
    sb = MF16(k1, qb1, ob); sb = MF8(k2, qb2, sb);
    k1 = *(const bf16x8*)(kl + 1024); k2 = *(const bf16x4*)(k8 + 512);
    const float ma = rowmax16(sa), mb = rowmax16(sb);
#pragma unroll
    for (int i = 0; i < 16; ++i) { nma[i] = -ma; nmb[i] = -mb; sa[i] -= ma; sb[i] -= mb; }
  }
  constexpr int NLATE = 0;
  STAMP(KID, 2); CSTAMP(KID, 2);
#pragma unroll 1
  for (int c = 0; c < 8; ++c) {
    const int bo = (c & 3) * ATT_STB, bn = ((c + 1) & 3) * ATT_STB;
#pragma unroll
    for (int t = 0; t < 4; ++t) {
      if (t == 2 && c < 7) {
        if (!PACK) { if (c == 6) wait_vm<NLATE>(); else wait_vm<4>(); }
        else if (c < 2) wait_vm<9>(); else if (c == 2) wait_vm<4>(); else if (c == 3) wait_vm<6>(); else if (c == 4) wait_vm<8>();
        else if (c == 5) wait_vm<7>(); else wait_vm<1 + NLATE>();
        raw_barrier();
        if (c < 5) stage(c + 3, (c + 3) & 3);
      }
      if (TC && PACK && c == 6 && t == 2 && wid == 0) touch_code<1>(dummy, lane);
      if (PACK) {
        const int pb = c * 2 + (t >> 1) - 5;
        if (pb >= 0 && pb < 5) {
          if (!(t & 1)) { plo = tr_read(pp + pb * 8192); phi = tr_read(pp + pb * 8192 + 4096); }
          else {
            const bf16x8 f = __builtin_shufflevector(plo, phi, 0, 1, 2, 3, 4, 5, 6, 7);
            const int kk = min(pj.kk0 + pb, pj.kmax);
            st16<3>(pj.dst + ((kk >> 1) * pj.s2 + (kk & 1)) * 1024, __builtin_bit_cast(u32x4, f));
            pcs = __builtin_amdgcn_mfma_f32_16x16x32_bf16(f, (bf16x8){0x3f80, 0x3f80, 0x3f80, 0x3f80, 0x3f80, 0x3f80, 0x3f80, 0x3f80}, pcs, 0, 0, 0);
          }
        }
      }
      const char* vp = kl + bo + ATT_V + t * 2048;
      const bf16x8 v0 = *(const bf16x8*)vp, v1 = *(const bf16x8*)(vp + 1024);
      const int kb = (t < 2) ? bo : bn, kt = (t + 2) & 3;
      u32x4 p0, p1, p2, p3; f32x16 na, nb;
      SB();
      na = MF16(k1, qa1, nma); SB();
      E4(p0, 0, sa, 0) SB();
      na = MF8(k2, qa2, na); SB();
      E4(p0, 2, sa, 4) SB();
      oa = MF16(v0, BC8(p0), oa); SB();
      E4(p1, 0, sa, 8) SB();
      nb = MF16(k1, qb1, nmb);
      k1 = *(const bf16x8*)(kl + kb + kt * 1024); SB();
      E4(p1, 2, sa, 12) SB();
      oa = MF16(v1, BC8(p1), oa); SB();
      E4(p2, 0, sb, 0) SB();
      nb = MF8(k2, qb2, nb);
      k2 = *(const bf16x4*)(k8 + kb + kt * 512); SB();
      SB();
      E4(p2, 2, sb, 4) SB();
      ob = MF16(v0, BC8(p2), ob); SB();
      E4(p3, 0, sb, 8) E4(p3, 2, sb, 12) SB();
      ob = MF16(v1, BC8(p3), ob); SB();
      sa = na; sb = nb;
    }
  }
  if (PACK && pj.cs != nullptr && (lane & 15) == 0) *(f32x4*)pj.cs = pcs;
  float ma = -nma[0], mb = -nmb[0];
  STAMP(KID, 3); CSTAMP(KID, 3);
  if (__syncthreads_or(nonfinite12(oa, lane) || nonfinite12(ob, lane))) {
    stage(0, 0); stage(1, 1); stage(2, 2);
#pragma unroll
    for (int i = 0; i < 16; ++i) { oa[i] = 0.f; ob[i] = 0.f; }
    ma = -3.0e38f; mb = -3.0e38f;
#pragma unroll 1
    for (int c = 0; c < 8; ++c) {
      if (c < 6) wait_vm<8>(); else if (c == 6) wait_vm<4>(); else wait_vm<0>();
      raw_barrier();
      if (c + 3 < 8) stage(c + 3, (c + 3) & 3);
      const int bo = (c & 3) * ATT_STB;
#pragma unroll 1
      for (int t = 0; t < 4; ++t) {
        k1 = *(const bf16x8*)(kl + bo + t * 1024); k2 = *(const bf16x4*)(k8 + bo + t * 512);
        const char* vp = kl + bo + ATT_V + t * 2048;
        const bf16x8 v0 = *(const bf16x8*)vp, v1 = *(const bf16x8*)(vp + 1024);
        auto one = [&](const bf16x8& q1, const bf16x4& q2, f32x16& o, float& m) {
          f32x16 s;
#pragma unroll
          for (int i = 0; i < 16; ++i) s[i] = 0.f;
          s = MF16(k1, q1, s); s = MF8(k2, q2, s);
          const float mn = fmaxf(m, rowmax16(s)), f = EX(m - mn);
          m = mn;
          u32x4 p0, p1;
#pragma unroll
          for (int i = 0; i < 16; ++i) { s[i] -= mn; o[i] *= f; }
          E4(p0, 0, s, 0) E4(p0, 2, s, 4) E4(p1, 0, s, 8) E4(p1, 2, s, 12)
          o = MF16(v0, BC8(p0), o); o = MF16(v1, BC8(p1), o);
        };
        one(qa1, qa2, oa, ma); one(qb1, qb2, ob, mb);
      }
    }
    __syncthreads();
  }
  STAMP(KID, 4);
  float* xo = (float*)smem + (wid ^ 4) * 13 * 64 + lane;
  const float* xi = (const float*)smem + wid * 13 * 64 + lane;
#pragma unroll
  for (int i = 0; i < 12; ++i) xo[i * 64] = half ? oa[i] : ob[i];
  xo[12 * 64] = half ? ma : mb;
  __syncthreads();
  {
    const float m1 = half ? mb : ma, m2 = xi[12 * 64], M = fmaxf(m1, m2);
    const float f1 = EX(m1 - M), f2 = EX(m2 - M);
    float o[12];
#pragma unroll
    for (int i = 0; i < 12; ++i) o[i] = (half ? ob[i] : oa[i]) * f1 + xi[i * 64] * f2;
    const float l = __shfl(o[8], r32 + 32);
    const float inv = 1.0f / l;
    const int q = q0 + half * 32, c0 = head * DH + hh * 4;
    char* O = ws + WS_O;
    st8<4>(O + foff<10>(q, c0), (u32x2){cvtpk(o[0] * inv, o[1] * inv), cvtpk(o[2] * inv, o[3] * inv)});
    st8<4>(O + foff<10>(q, c0 + 8), (u32x2){cvtpk(o[4] * inv, o[5] * inv), cvtpk(o[6] * inv, o[7] * inv)});
    if (hh == 0) st8<4>(O + foff<10>(q, c0 + 16), (u32x2){cvtpk(o[8] * inv, o[9] * inv), cvtpk(o[10] * inv, o[11] * inv)});
  }
  STAMP(KID, 5);
  wait_vm<0>();
  STAMP(KID, 6);
  KLABEL("kend_", 5 + PACK);
}

extern "C" void kernel_launch(void* const* d_in, const int* in_sizes, int n_in, void* d_out, int out_size, void* d_ws,
                              size_t ws_size, hipStream_t stream) {
  const char* x0 = (const char*)d_in[0];
  char* ws = (char*)d_ws;
  Q0Args q0{x0, (const char*)d_in[1], ws};
  qkv0_kernel<<<256, 512, 0, stream>>>(q0);
  for (int layer = 0; layer < 2; ++layer) {
    AttnArgs at{ws, (const u16*)d_in[1], (const u16*)d_in[2], (const u16*)d_in[4], (const u16*)d_in[5], (const u16*)d_in[6] KIDARG(layer * 5 + 1)};
    if (layer == 0) attn_kernel<true><<<256, 512, 0, stream>>>(at); else attn_kernel<false><<<256, 512, 0, stream>>>(at);
    ChainArgs c{layer ? ws + WS_X2 : x0, ws + (layer ? WS_X3 : WS_X1), layer ? (char*)d_out : ws + WS_X2, ws, layer};
    chain_kernel<<<256, 512, 0, stream>>>(c);
  }
}
```

```cpp
#include <hip/hip_runtime.h>
#include <hip/hip_bf16.h>
#include <stdint.h>

typedef __attribute__((ext_vector_type(8))) short bf16x8;
typedef __attribute__((ext_vector_type(4))) short bf16x4;
typedef __attribute__((ext_vector_type(4))) float f32x4;
typedef __attribute__((ext_vector_type(16))) float f32x16;
typedef __attribute__((ext_vector_type(4))) unsigned u32x4;
typedef __attribute__((ext_vector_type(2))) unsigned u32x2;
typedef unsigned short u16;

#define DI __device__ __forceinline__

constexpr int SEQ = 2048, DM = 640, NH = 32, DH = 20, DF = 1728;
constexpr float LN_EPS = 1e-5f;
constexpr float QSCALE = 0.22360679774997896f * 1.4426950408889634f;

constexpr size_t WS_X1   = 0;
constexpr size_t WS_X2   = WS_X1 + (size_t)SEQ * DM * 2;
constexpr size_t WS_X3   = WS_X2 + (size_t)SEQ * DM * 2;
constexpr size_t WS_Q    = WS_X3 + (size_t)SEQ * DM * 2;
constexpr size_t WS_O    = WS_Q + (size_t)SEQ * DM * 2;
constexpr size_t WS_H    = WS_O + (size_t)SEQ * DM * 2;
constexpr int    KVP_CHUNK = 22528, KVP_KE = 6144, KVP_TAIL = 10240, KVP_KO = 12288, KVP_VO = 16384, VFR = 768;
constexpr size_t WS_KV   = WS_H + (size_t)SEQ * DF * 2;
constexpr size_t WS_PWO  = WS_KV + (size_t)16 * 16 * KVP_CHUNK;
constexpr size_t WS_PWD  = WS_PWO + (size_t)DM * DM * 2;
constexpr size_t WS_PGU  = WS_PWD + (size_t)DF * DM * 2;
constexpr size_t WS_PQKV = WS_PGU + (size_t)16 * 10 * 28 * 1024;
constexpr size_t WS_CGU  = WS_PQKV + (size_t)DM * 3 * DM * 2;
constexpr size_t WS_CQKV = WS_CGU + (size_t)16 * 4 * 224 * 4;
constexpr size_t WS_STAT = WS_CQKV + (size_t)15 * 4 * 128 * 4;
constexpr size_t WS_STAT2 = WS_STAT + (size_t)SEQ * 64;
constexpr size_t WS_CNT  = WS_STAT2 + (size_t)SEQ * 64;
constexpr size_t WS_END  = WS_CNT + 6 * 16 * 256;

typedef __attribute__((ext_vector_type(2))) __bf16 bf16p;
typedef __attribute__((ext_vector_type(2))) float f32x2;
DI unsigned cvtpk(float lo, float hi) { return __builtin_bit_cast(unsigned, __builtin_convertvector((f32x2){lo, hi}, bf16p)); }
DI float dot2ones(unsigned a, float s) {
  return __builtin_amdgcn_fdot2_f32_bf16(__builtin_bit_cast(bf16p, a), __builtin_bit_cast(bf16p, 0x3f803f80u), s, false);
}
DI float bflo(unsigned v) { return __uint_as_float(v << 16); }
DI float bfhi(unsigned v) { return __uint_as_float(v & 0xffff0000u); }
#define KIDARG(k)
#define STAMP(kid, i)
#define CSTAMP(kid, i)
template <int N> DI void wait_vm() { asm volatile("s_waitcnt vmcnt(%0)" ::"i"(N) : "memory"); }
DI void raw_barrier() {
  asm volatile("" ::: "memory");
  __builtin_amdgcn_s_waitcnt(0xC07F);
  __builtin_amdgcn_s_barrier();
  asm volatile("" ::: "memory");
}
DI u32x2 swap16(unsigned a, unsigned b) { return __builtin_amdgcn_permlane16_swap(a, b, false, false); }
DI u32x2 swap32(unsigned a, unsigned b) { return __builtin_amdgcn_permlane32_swap(a, b, false, false); }
DI bf16x4 tr_read(const char* p) {
  typedef short v4i16_t __attribute__((ext_vector_type(4)));
  return __builtin_bit_cast(bf16x4, __builtin_amdgcn_ds_read_tr16_b64_v4i16((__attribute__((address_space(3))) v4i16_t*)(p)));
}
template <bool SC = false> DI void glds16(const void* g, const char* l) {
  unsigned sv; const unsigned lb = __builtin_amdgcn_readfirstlane((unsigned)(uintptr_t)l);
  if (SC) asm volatile("s_mov_b32 %0, m0\n\ts_mov_b32 m0, %2\n\ts_nop 0\n\tglobal_load_lds_dwordx4 %1, off sc1\n\ts_mov_b32 m0, %0"
               : "=&s"(sv) : "v"(g), "s"(lb) : "memory");
  else asm volatile("s_mov_b32 %0, m0\n\ts_mov_b32 m0, %2\n\ts_nop 0\n\tglobal_load_lds_dwordx4 %1, off\n\ts_mov_b32 m0, %0"
               : "=&s"(sv) : "v"(g), "s"(lb) : "memory");
}
DI void gtouch(const void* g, const char* l) {
  unsigned sv; const unsigned lb = __builtin_amdgcn_readfirstlane((unsigned)(uintptr_t)l);
  asm volatile("s_mov_b32 %0, m0\n\ts_mov_b32 m0, %2\n\ts_nop 0\n\tglobal_load_lds_dword %1, off\n\ts_mov_b32 m0, %0"
               : "=&s"(sv) : "v"(g), "s"(lb) : "memory");
}
#define KLABEL(pfx, id) asm volatile(pfx "%c0:" ::"i"(id))
template <int ID, bool END> DI unsigned long long kaddr() {
  unsigned long long a;
  if (END) asm volatile("s_getpc_b64 s[20:21]\n\ts_add_u32 s20, s20, kend_%c1@rel32@lo+4\n\ts_addc_u32 s21, s21, kend_%c1@rel32@hi+12\n\ts_mov_b64 %0, s[20:21]" : "=s"(a) : "i"(ID) : "s20", "s21", "scc");
  else asm volatile("s_getpc_b64 s[20:21]\n\ts_add_u32 s20, s20, kbeg_%c1@rel32@lo+4\n\ts_addc_u32 s21, s21, kbeg_%c1@rel32@hi+12\n\ts_mov_b64 %0, s[20:21]" : "=s"(a) : "i"(ID) : "s20", "s21", "scc");
  return a;
}
template <int ID> DI void touch_code(const char* dummy, int lane) {
  const unsigned long long lo = kaddr<ID, false>() & ~255ull;
  unsigned long long hi = kaddr<ID, true>();
  if (ID < 5) { const unsigned long long e = kaddr<(ID < 5 ? ID + 7 : ID), true>(); hi = e > hi ? e : hi; }
  for (unsigned o = lane * 128; o < (unsigned)(hi - lo); o += 8192) gtouch((const char*)lo + o, dummy);
}
#define TC 1
DI u32x2 gload8(const void* g) {
  u32x2 r; asm volatile("global_load_dwordx2 %0, %1, off" : "=v"(r) : "v"(g) : "memory"); return r;
}
DI unsigned gload4(const void* g) {
  unsigned r; asm volatile("global_load_dword %0, %1, off" : "=v"(r) : "v"(g) : "memory"); return r;
}
DI u32x4 gload16(const void* g) {
  u32x4 r; asm volatile("global_load_dwordx4 %0, %1, off" : "=v"(r) : "v"(g) : "memory"); return r;
}
DI u32x4 gload16sc(const void* g) {
  u32x4 r; asm volatile("global_load_dwordx4 %0, %1, off sc1" : "=v"(r) : "v"(g) : "memory"); return r;
}
DI void st4sc(void* p, float v) { asm volatile("global_store_dword %0, %1, off sc1" ::"v"(p), "v"(v) : "memory"); }
DI void st16sc(void* p, u32x4 v) { asm volatile("global_store_dwordx4 %0, %1, off sc1\n\ts_nop 1" ::"v"(p), "v"(v)); }
#define ST_MODE 14
template <int CLS> DI void st16(void* p, u32x4 v) {
  if ((ST_MODE >> CLS) & 1) asm volatile("global_store_dwordx4 %0, %1, off sc1\n\ts_nop 1" ::"v"(p), "v"(v));
  else *(u32x4*)p = v;
}
template <int IMM, bool WT = true> DI void stw(const void* base, unsigned off, u32x4 v) {
  if (WT) asm volatile("global_store_dwordx4 %0, %1, %2 offset:%3 sc1\n\ts_nop 1" ::"v"(off), "v"(v), "s"(base), "i"(IMM));
  else asm volatile("global_store_dwordx4 %0, %1, %2 offset:%3\n\ts_nop 1" ::"v"(off), "v"(v), "s"(base), "i"(IMM));
}
template <int CLS> DI void st8(void* p, u32x2 v) {
  if ((ST_MODE >> CLS) & 1) asm volatile("global_store_dwordx2 %0, %1, off sc1" ::"v"(p), "v"(v) : "memory");
  else *(u32x2*)p = v;
}
template <int I> struct IC { static constexpr int v = I; };
template <int L, int NMAX> DI void wait_stages(int n) {
  if constexpr (NMAX == 0) wait_vm<0>();
  else { if (n >= NMAX) wait_vm<NMAX * L>(); else wait_stages<L, NMAX - 1>(n); }
}
template <int N, int I = 0, class F> DI void static_for(F&& f) {
  if constexpr (I < N) { f(IC<I>{}); static_for<N, I + 1>(f); }
}
#define SB() __builtin_amdgcn_sched_barrier(0)
template <int NS> DI unsigned foff(int row, int col) {
  return (unsigned)((((row >> 6) * NS + (col >> 6)) * 8 + ((row >> 4) & 3) * 2 + ((col >> 5) & 1)) * 1024 +
                    (((col >> 3) & 3) * 16 + (row & 15)) * 16 + (col & 7) * 2);
}

enum { EPI_QKV = 0, EPI_RES = 1, EPI_GEGLU = 2 };
constexpr int L_TOTAL = 163840, L_SMALL = L_TOTAL - 2304, L_XB = L_SMALL - 20480, L_XHI = L_XB + 8192;
DI int xb_off(int w, int j) { return j < 3 ? L_XHI + (w * 3 + j) * 1024 : L_XB + (w * 2 + j - 3) * 1024; }

template <int BM_, int BN_, int KTOT_, int NST_, int EPI_, bool RAW_> struct Cfg {
  static constexpr int BM = BM_, BN = BN_, KTOT = KTOT_, NST = NST_, EPI = EPI_;
  static constexpr bool RAW = RAW_, RES = (EPI_ == EPI_RES), LNP = !RES && !RAW_;
  static constexpr int MT = RES ? 2 : 4, NT = RES ? 5 : BN_ / 32, KT = KTOT_ / 64;
  static constexpr int PA = BM_ / 8, PB = BN_ / 8, P = PA + PB, LMAX = (P + 3) / 4;
  static constexpr int ABYTES = BM_ * 128, STB = P * 1024;
  static constexpr int RS = BN_ * 2;
};
typedef Cfg<128, 128, 640, 4, EPI_QKV, true> CQ0;
typedef Cfg<128, 128, 640, 4, EPI_QKV, false> CQ1;
typedef Cfg<64, 80, 640, 8, EPI_RES, false> CWO;
typedef Cfg<128, 224, 640, 3, EPI_GEGLU, false> CGU;
typedef Cfg<64, 80, 1728, 8, EPI_RES, false> CDN;

template <class C> DI void tile_of(int bid, int& tm, int& tn) {
  const int xcd = bid & 7, idx = bid >> 3;
  if (C::RES) { tm = xcd * 4 + (idx & 1) * 2 + ((idx >> 1) & 1); tn = idx >> 2; }
  else { tm = xcd * 2 + (idx & 1); tn = idx >> 1; }
}
DI const char* uni(const char* p) {
  const unsigned long long v = (unsigned long long)p;
  return (const char*)(((unsigned long long)(unsigned)__builtin_amdgcn_readfirstlane((unsigned)(v >> 32)) << 32) | (unsigned)__builtin_amdgcn_readfirstlane((unsigned)v));
}
DI int opaque(int x) { asm volatile("" : "+s"(x)); return x; }
DI int opaque_v(int x) { asm volatile("" : "+v"(x)); return x; }

template <int KT, int N, int L, int LA, int E> constexpr int ld_allowed(int i) {
  const int full = i - 1 < 0 ? 0 : (i - 1 > KT - N ? KT - N : i - 1);
  return (i < N ? (N - 1 - i) * LA + full * L : (N + full - 1 - i) * L) + (i > KT - N ? E : 0);
}

template <class C, bool SEAM> DI void loader_phase(char* smem, const char* A, const char* B, const char* ws, size_t stat, int bid, int tid) {
  constexpr int KT = C::KT, NST = C::NST, PA = C::PA, PB = C::PB, P = C::P, LMAX = C::LMAX, STB = C::STB, BN = C::BN;
  constexpr bool RAW = C::RAW, LNP = C::LNP;
  const int lane = tid & 63, lw = __builtin_amdgcn_readfirstlane(tid >> 6) - 4;
  int tm, tn; tile_of<C>(bid, tm, tn);
  const int m0 = tm * C::BM, n0 = tn * BN;
  const char* src[LMAX];
#pragma unroll
  for (int i = 0; i < LMAX; ++i) {
    const int p = lw + 4 * i;
    if (RAW) {
      if (i < PA / 4) {
        const int row = p * 8 + (lane >> 3), c = (lane & 7) ^ ((row >> 1) & 7);
        src[i] = A + ((size_t)(m0 + row) * C::KTOT + c * 8) * 2;
      } else {
        const int u = (p - PA) * 64 + lane, prow = u >> 4, c16 = u & 15;
        const int ch = (c16 >> 1) ^ (prow & 7);
        const int k = (prow & 32) + 8 * (((prow >> 3) & 1) * 2 + ((prow >> 2) & 1)) + 4 * ((prow >> 4) & 1) + (prow & 3);
        src[i] = B + ((size_t)k * (3 * DM) + n0 + ch * 16 + (c16 & 1) * 8) * 2;
      }
    } else {
      if (i < PA / 4) src[i] = A + ((size_t)((m0 >> 6) + (p >> 3)) * KT * 8 + (p & 7)) * 1024 + lane * 16;
      else src[i] = B + ((size_t)tn * KT * PB + (p - PA < PB ? p - PA : 0)) * 1024 + lane * 16;
    }
  }
  constexpr int STR_A = RAW ? 128 : 8192, STR_B = RAW ? 64 * 3 * DM * 2 : PB * 1024;
  auto body = [&](auto LC) {
    constexpr int L = decltype(LC)::v, LA = PA / 4;
    auto issue = [&](int buf, auto I0, auto I1) {
      char* base = smem + buf * STB + lw * 1024;
#pragma unroll
      for (int i = decltype(I0)::v; i < decltype(I1)::v; ++i) {
        if (i < LA) glds16<SEAM>(src[i], base + i * 4096); else glds16(src[i], base + i * 4096);
        src[i] += (i < LA) ? STR_A : STR_B;
      }
    };
    const int t = tid - 256;
    unsigned c[4]; u32x4 st[4];
    auto ln_loads = [&]() {
      const float* cp = (const float*)(ws + (C::EPI == EPI_QKV ? WS_CQKV : WS_CGU)) + tn * 4 * BN + (t < BN ? t : 0);
      const char* sp = ws + stat + (size_t)(m0 + (t & 127)) * 64;
#pragma unroll
      for (int k = 0; k < 4; ++k) c[k] = gload4(cp + k * BN);
#pragma unroll
      for (int k = 0; k < 4; ++k) st[k] = gload16sc(sp + k * 16);
    };
    if constexpr (SEAM) {
      constexpr int E = LNP ? 8 : 0;
#pragma unroll
      for (int s = 0; s < NST; ++s) issue(s, IC<LA>{}, IC<L>{});
      raw_barrier();
      raw_barrier();
#pragma unroll
      for (int s = 0; s < NST; ++s) issue(s, IC<0>{}, IC<LA>{});
      auto iter = [&](auto I) {
        constexpr int i = decltype(I)::v;
        wait_vm<ld_allowed<KT, NST, L, LA, E>(i)>();
        raw_barrier();
        if (i >= 1 && i <= KT - NST) issue((i - 1) % NST, IC<0>{}, IC<L>{});
        if (LNP && i == KT - NST) ln_loads();
      };
      if constexpr (KT >= 3 * NST) {
        static_for<NST>(iter);
        int ibuf = NST - 1;
#pragma unroll 1
        for (int i = NST; i < KT - NST; ++i) {
          wait_vm<(NST - 2) * L>();
          raw_barrier();
          issue(ibuf, IC<0>{}, IC<L>{});
          if (++ibuf == NST) ibuf = 0;
        }
        static_for<KT, KT - NST>(iter);
      } else static_for<KT>(iter);
    } else {
      issue(0, IC<0>{}, IC<L>{}); issue(1, IC<0>{}, IC<L>{});
      int nxt = 2, ibuf = 2;
      constexpr int NMAIN = LNP ? KT - NST + 1 : KT;
      for (int i = 0; i < NMAIN; ++i) {
        wait_stages<L, NST - 1>(nxt - i - 1);
        raw_barrier();
        const int lim = min(KT, i + NST);
#pragma unroll
        for (int r = 0; r < 2; ++r)
          if (nxt < lim) { issue(ibuf, IC<0>{}, IC<L>{}); ++nxt; if (++ibuf == NST) ibuf = 0; }
      }
      if (LNP) {
        ln_loads();
        static_for<NST - 1>([&](auto I) { wait_vm<(NST - 2 - decltype(I)::v) * L + 8>(); raw_barrier(); });
      }
    }
    if (LNP) {
      float2* rowst = (float2*)(smem + L_SMALL);
      float* colsum = (float*)(smem + L_SMALL + 1024);
      wait_vm<0>();
      float cs = 0.f;
#pragma unroll
      for (int k = 0; k < 4; ++k) { asm volatile("" : "+v"(c[k])); cs += __uint_as_float(c[k]); }
#pragma unroll
      for (int k = 0; k < 4; ++k) asm volatile("" : "+v"(st[k]));
      const f32x4 sv = (__builtin_bit_cast(f32x4, st[0]) + __builtin_bit_cast(f32x4, st[1])) + (__builtin_bit_cast(f32x4, st[2]) + __builtin_bit_cast(f32x4, st[3]));
      const float mean = (sv[0] + sv[2]) * (1.0f / C::KTOT);
      const float var = fmaxf((sv[1] + sv[3]) * (1.0f / C::KTOT) - mean * mean, 0.f);
      if (t < BN) colsum[t] = cs;
      if (t >= 128) rowst[t - 128] = make_float2(mean, rsqrtf(var + LN_EPS));
    }
    raw_barrier();
  };
  if (P % 4 == 0 || lw < P % 4) body(IC<LMAX>{}); else body(IC<LMAX - 1>{});
}

DI void seam(char* ws, int id, int bid, int tid) {
  unsigned* cnt = (unsigned*)(ws + WS_CNT + (size_t)(id * 16 + (bid & 7) * 2 + ((bid >> 3) & 1)) * 256);
  wait_vm<0>();
  raw_barrier();
  if (tid == 0) __hip_atomic_fetch_add(cnt, 1u, __ATOMIC_RELAXED, __HIP_MEMORY_SCOPE_AGENT);
  if (tid == 64) {
    for (int s = 0; s < (1 << 18); ++s) {
      if (__hip_atomic_load(cnt, __ATOMIC_RELAXED, __HIP_MEMORY_SCOPE_AGENT) >= 16u) break;
      __builtin_amdgcn_s_sleep(1);
    }
  }
  raw_barrier();
}

template <class C, int MODE> DI void compute_res(char* smem, const char* res, bool res_raw, char* out, bool out_raw, char* ws, size_t stat, int bid, int tid, int KID) {
  constexpr int KT = C::KT, NST = C::NST, NT = C::NT, STB = C::STB, ABYTES = C::ABYTES;
  const int lane = tid & 63, wid = __builtin_amdgcn_readfirstlane(tid >> 6), fr = lane & 15, fg = lane >> 4, wm = wid >> 1, wn = wid & 1;
  int tm, tn; tile_of<C>(bid, tm, tn);
  const int m0 = tm * C::BM, n0 = tn * C::BN;
  f32x4 acc[2][NT];
#pragma unroll
  for (int i = 0; i < 2; ++i)
#pragma unroll
    for (int j = 0; j < NT; ++j) acc[i][j] = (f32x4){0.f, 0.f, 0.f, 0.f};
  const int row = m0 + wid * 16 + fr;
  u32x2 resv[NT];
#pragma unroll
  for (int j = 0; j < NT; ++j) {
    const int col = n0 + j * 16 + fg * 4;
    if (MODE == 0) resv[j] = *(const u32x2*)(res + (res_raw ? (unsigned)(row * DM + col) * 2 : foff<10>(row, col)));
    else resv[j] = *(const u32x2*)(smem + L_XHI + wid * 3072 + j * 512 + lane * 8);
  }
  const int aoff = (wm * 4 + wn) * 1024 + lane * 16, boff = ABYTES + wn * 1024 + lane * 16;
  bf16x8 a0[2], b0[NT], a1[2], b1[NT];
  auto rd = [&](bf16x8(&a)[2], bf16x8(&b)[NT], const char* base) {
#pragma unroll
    for (int i = 0; i < 2; ++i) a[i] = *(const bf16x8*)(base + aoff + i * 2048);
#pragma unroll
    for (int j = 0; j < NT; ++j) b[j] = *(const bf16x8*)(base + boff + j * 2048);
    SB();
  };
  auto mm = [&](bf16x8(&a)[2], bf16x8(&b)[NT]) {
#pragma unroll
    for (int j = 0; j < NT; ++j)
#pragma unroll
      for (int i = 0; i < 2; ++i) acc[i][j] = __builtin_amdgcn_mfma_f32_16x16x32_bf16(b[j], a[i], acc[i][j], 0, 0, 0);
    SB();
  };
  STAMP(KID, 1);
  raw_barrier();
  STAMP(KID, 2); CSTAMP(KID, 2);
  rd(a0, b0, smem);
  int buf = 0;
#pragma unroll 1
  for (int kt = 0; kt + 2 < KT; kt += 2) {
    if (++buf == NST) buf = 0;
    raw_barrier();
    rd(a1, b1, smem + buf * STB);
    mm(a0, b0);
    if (++buf == NST) buf = 0;
    raw_barrier();
    rd(a0, b0, smem + buf * STB);
    mm(a1, b1);
  }
  if (KT % 2 == 0) {
    if (++buf == NST) buf = 0;
    raw_barrier();
    rd(a1, b1, smem + buf * STB);
    mm(a0, b0);
    mm(a1, b1);
  } else {
    mm(a0, b0);
  }
  STAMP(KID, 3); CSTAMP(KID, 3);
#pragma unroll
  for (int j = 0; j < NT; ++j) *(f32x4*)(smem + xb_off(wid ^ 1, j) + lane * 16) = wn ? acc[0][j] : acc[1][j];
  raw_barrier();
  STAMP(KID, 4);
  float s = 0.f, q = 0.f;
  u32x2 v[NT];
#pragma unroll
  for (int j = 0; j < NT; ++j) {
    const f32x4 o = *(const f32x4*)(smem + xb_off(wid, j) + lane * 16) + (wn ? acc[1][j] : acc[0][j]);
    const u32x2 r = resv[j];
    v[j] = (u32x2){cvtpk(o[0] + bflo(r[0]), o[1] + bfhi(r[0])), cvtpk(o[2] + bflo(r[1]), o[3] + bfhi(r[1]))};
    const float e0 = bflo(v[j][0]), e1 = bfhi(v[j][0]), e2 = bflo(v[j][1]), e3 = bfhi(v[j][1]);
    s += (e0 + e1) + (e2 + e3);
    q += (e0 * e0 + e1 * e1) + (e2 * e2 + e3 * e3);
  }
#pragma unroll
  for (int jp = 0; jp < NT; jp += 2) {
    const int j1 = jp + 1 < NT ? jp + 1 : jp;
    const u32x2 x = swap16(v[jp][0], v[j1][0]), y = swap16(v[jp][1], v[j1][1]);
    const int col = n0 + (jp + (fg & 1)) * 16 + (fg >> 1) * 8;
    if (jp + 1 < NT || !(fg & 1))
      st16sc(out + (out_raw ? (unsigned)(row * DM + col) * 2 : foff<10>(row, col)), (u32x4){x[0], y[0], x[1], y[1]});
  }
  {
    const u32x2 t1 = swap16(__float_as_uint(s), __float_as_uint(q));
    const unsigned t2 = __float_as_uint(__uint_as_float(t1[0]) + __uint_as_float(t1[1]));
    const u32x2 t3 = swap32(t2, t2);
    if (lane < 32) st4sc(ws + stat + (size_t)(row * 8 + tn) * 8 + fg * 4, __uint_as_float(t3[0]) + __uint_as_float(t3[1]));
  }
  if (MODE == 0) {
#pragma unroll
    for (int j = 0; j < NT; ++j) *(u32x2*)(smem + L_XHI + wid * 3072 + j * 512 + lane * 8) = v[j];
  }
  STAMP(KID, 5);
}

template <class C> DI void compute_ln(char* smem, char* out, char* ws, int bid, int tid, int KID) {
  constexpr int KT = C::KT, NST = C::NST, NT = C::NT, MT = C::MT, STB = C::STB, ABYTES = C::ABYTES, RS = C::RS, BN = C::BN, EPI = C::EPI, KTOT = C::KTOT;
  constexpr bool RAW = C::RAW;
  const int lane = tid & 63, wid = __builtin_amdgcn_readfirstlane(tid >> 6), fr = lane & 15, fg = lane >> 4, wm = wid >> 1, wn = wid & 1;
  int tm, tn; tile_of<C>(bid, tm, tn);
  const int m0 = tm * C::BM, n0 = tn * BN;
  float2* rowst = (float2*)(smem + L_SMALL);
  float* colsum = (float*)(smem + L_SMALL + 1024);
  const char* dummy = smem + L_SMALL + 2048;
  f32x4 acc[MT][NT];
#pragma unroll
  for (int i = 0; i < MT; ++i)
#pragma unroll
    for (int j = 0; j < NT; ++j) acc[i][j] = (f32x4){0.f, 0.f, 0.f, 0.f};
  if (EPI == EPI_QKV && tn >= 10) {
    const int vc0 = n0 - 2 * DM, head = (vc0 + DH - 1) / DH + (tid >> 4);
    if (head * DH < vc0 + BN) {
      const int tok = m0 + ((tid >> 1) & 7) * 16;
      char* p = ws + WS_KV + (size_t)((head >> 1) * 16 + (tok >> 7)) * KVP_CHUNK + (head & 1) * KVP_VO + ((tok >> 4) & 7) * VFR + ((tid & 1) * 24 + DH) * 16;
      *(u32x4*)p = (u32x4){0x3f803f80u, 0x3f803f80u, 0x3f803f80u, 0x3f803f80u};
#pragma unroll
      for (int i = 1; i < 4; ++i) *(u32x4*)(p + 16 * i) = (u32x4){0u, 0u, 0u, 0u};
    }
  }
  int aoff[2], boff[RAW ? NT : 1];
  const int pr = 8 * (fg >> 1) + 4 * (fg & 1) + (fr >> 2);
#pragma unroll
  for (int ks = 0; ks < 2; ++ks)
    aoff[ks] = RAW ? (wm * 64 + fr) * 128 + (((ks * 4 + fg) ^ (fr >> 1)) * 16) : (wm * 8 + ks) * 1024 + lane * 16;
  if (RAW) {
#pragma unroll
    for (int j = 0; j < NT; ++j) boff[j] = ABYTES + pr * RS + ((wn * NT + j) ^ (pr & 7)) * 32 + (lane & 3) * 8;
  } else boff[0] = ABYTES + wn * NT * 2048 + lane * 16;
  f32x4 srow[2], sgr[2], scol[2];
#pragma unroll
  for (int i = 0; i < 2; ++i) srow[i] = sgr[i] = scol[i] = (f32x4){0.f, 0.f, 0.f, 0.f};
  const bf16x8 ones = (bf16x8){0x3f80, 0x3f80, 0x3f80, 0x3f80, 0x3f80, 0x3f80, 0x3f80, 0x3f80};

  const bool swp = !(EPI == EPI_QKV && tn >= 10);
  unsigned so[2][2];
  if (EPI == EPI_QKV) {
    const bool isq = tn < DM / BN;
#pragma unroll
    for (int jq = 0; jq < 2; ++jq) {
      if (swp) {
        const int G = ((isq ? n0 : n0 - DM) >> 3) + (wn * NT + jq * 2 + (fg & 1)) * 2 + (fg >> 1), pair = G / 5, gi = G % 5;
        const int mo = (((gi + (gi > 2)) & 1) * 32 + fr) * 16;
        if (isq) {
          so[jq][0] = (unsigned)WS_Q + (pair * 64 + (m0 >> 5) + wm * 2) * 2560 + (gi == 2 ? 2048 + fr * 16 : (gi > 2) * 1024 + mo);
          so[jq][1] = so[jq][0] + 2560;
        } else {
          so[jq][0] = (unsigned)WS_KV + (pair * 16 + (m0 >> 7)) * KVP_CHUNK + (gi == 2 ? KVP_TAIL + (wm * 64 + fr) * 16 : (gi > 2 ? KVP_KO : KVP_KE) + wm * 2048 + mo);
          so[jq][1] = so[jq][0] + (gi == 2 ? 512 : 1024);
        }
      } else {
        const int cc = n0 - 2 * DM + (wn * NT + jq * 2 + (lane >> 5)) * 16 + fr, head = cc / DH, d = cc % DH;
        so[jq][0] = (unsigned)WS_KV + ((head >> 1) * 16 + (m0 >> 7)) * KVP_CHUNK + (head & 1) * KVP_VO + wm * 4 * VFR + ((fg & 1) * 24 + d) * 16;
        so[jq][1] = so[jq][0] + 2 * VFR;
      }
      asm volatile("" : "+v"(so[jq][0]), "+v"(so[jq][1]));
    }
  }
  auto mainloop = [&](auto SW, auto WN, auto WM) {
    constexpr bool SWP = decltype(SW)::v;
    constexpr int CWN = decltype(WN)::v, CWM = decltype(WM)::v;
    bf16x8 a0[MT], a1[MT], b0[NT], b1[NT];
    auto rdB = [&](const char* base, int j, int ks) {
      if (RAW) {
        const char* p = base + boff[RAW ? j : 0] + ks * 32 * RS;
        bf16x4 lo = tr_read(p), hi = tr_read(p + 16 * RS);
        return __builtin_shufflevector(lo, hi, 0, 1, 2, 3, 4, 5, 6, 7);
      } else return *(const bf16x8*)(base + boff[0] + j * 2048 + ks * 1024);
    };
    auto step = [&](bf16x8(&ac)[MT], bf16x8(&bc)[NT], bf16x8(&an)[MT], bf16x8(&bn)[NT], const char* nb, int nks, auto RD) {
      constexpr bool rd = decltype(RD)::v;
      constexpr int RPG = 3;
#pragma unroll
      for (int j = 0; j < NT; ++j) {
#pragma unroll
        for (int i = 0; i < MT; ++i) {
          acc[i][j] = SWP ? __builtin_amdgcn_mfma_f32_16x16x32_bf16(bc[j], ac[i], acc[i][j], 0, 0, 0)
                          : __builtin_amdgcn_mfma_f32_16x16x32_bf16(ac[i], bc[j], acc[i][j], 0, 0, 0);
          const int r = j * RPG + i;
          if (rd && i < RPG && r < MT + NT) {
            if (r == 0) bn[0] = rdB(nb, 0, nks);
            else if (r <= MT) an[r - 1] = *(const bf16x8*)(nb + aoff[nks] + (r - 1) * 2048);
            else bn[r - MT] = rdB(nb, r - MT, nks);
            SB();
          }
        }
        if (RAW) {
          if ((j >> 1) == CWM) scol[j & 1] = __builtin_amdgcn_mfma_f32_16x16x32_bf16(bc[j], ones, scol[j & 1], 0, 0, 0);
          else {
            srow[j & 1] = __builtin_amdgcn_mfma_f32_16x16x32_bf16(ones, ac[CWN * 2 + (j & 1)], srow[j & 1], 0, 0, 0);
            sgr[j & 1] = __builtin_amdgcn_mfma_f32_16x16x32_bf16(ac[CWN * 2 + (j & 1)], ac[CWN * 2 + (j & 1)], sgr[j & 1], 0, 0, 0);
          }
        }
        SB();
      }
    };
    STAMP(KID, 1);
    raw_barrier();
    STAMP(KID, 2); CSTAMP(KID, 2);
#pragma unroll
    for (int j = 0; j < NT; ++j) { b0[j] = rdB(smem, j, 0); if (j < MT) a0[j] = *(const bf16x8*)(smem + aoff[0] + j * 2048); }
    step(a0, b0, a1, b1, smem, 1, IC<1>{});
    int buf = 0;
#pragma unroll 1
    for (int kt = 1; kt < KT; ++kt) {
      if (++buf == NST) buf = 0;
      raw_barrier();
      step(a1, b1, a0, b0, smem + buf * STB, 0, IC<1>{});
      step(a0, b0, a1, b1, smem + buf * STB, 1, IC<1>{});
    }
    step(a1, b1, a0, b0, smem, 0, IC<0>{});
  };
  if (RAW) {
    if (swp) {
      if (wid == 0) mainloop(IC<1>{}, IC<0>{}, IC<0>{}); else if (wid == 1) mainloop(IC<1>{}, IC<1>{}, IC<0>{});
      else if (wid == 2) mainloop(IC<1>{}, IC<0>{}, IC<1>{}); else mainloop(IC<1>{}, IC<1>{}, IC<1>{});
    } else {
      if (wid == 0) mainloop(IC<0>{}, IC<0>{}, IC<0>{}); else if (wid == 1) mainloop(IC<0>{}, IC<1>{}, IC<0>{});
      else if (wid == 2) mainloop(IC<0>{}, IC<0>{}, IC<1>{}); else mainloop(IC<0>{}, IC<1>{}, IC<1>{});
    }
    STAMP(KID, 3); CSTAMP(KID, 3);
    if (TC && wid == 0) touch_code<6>(dummy, lane);
#pragma unroll
    for (int i = 0; i < 2; ++i) {
      const float sq = (fr & 2) ? ((fr & 1) ? sgr[i][3] : sgr[i][2]) : ((fr & 1) ? sgr[i][1] : sgr[i][0]);
      const float mean = srow[i][0] * (1.0f / KTOT);
      const float var = fmaxf(sq * (1.0f / KTOT) - mean * mean, 0.f);
      if (fg == (fr >> 2)) rowst[wm * 64 + (wn * 2 + i) * 16 + fr] = make_float2(mean, rsqrtf(var + LN_EPS));
      if (fr == 0) *(f32x4*)(colsum + (wn * NT + wm * 2 + i) * 16 + fg * 4) = scol[i];
    }
    raw_barrier();
  } else {
    if (swp) mainloop(IC<1>{}, IC<0>{}, IC<0>{}); else mainloop(IC<0>{}, IC<0>{}, IC<0>{});
    STAMP(KID, 3); CSTAMP(KID, 3);
    if (TC && EPI == EPI_QKV && wid == 0) touch_code<5>(dummy, lane);
    raw_barrier();
  }
  STAMP(KID, 4);

  if constexpr (EPI == EPI_GEGLU) {
    const int hbase = (tn >> 1) * 216 + (tn & 1) * 112;
    const int orow = m0 + wm * 64 + fg * 16 + fr;
    constexpr float C1 = -2.0f * 0.7978845608028654f * 1.4426950408889634f, C2 = C1 * 0.044715f;
    f32x2 nm[MT], k1[MT], k2[MT], iv[MT];
#pragma unroll
    for (int i = 0; i < MT; ++i) {
      const float2 ms = rowst[wm * 64 + i * 16 + fr];
      const float r2 = ms.y * ms.y, ir2 = __builtin_amdgcn_rcpf(r2);
      nm[i] = (f32x2){-ms.x, -ms.x}; k1[i] = (f32x2){C1 * ms.y, C1 * ms.y}; k2[i] = (f32x2){C2 * ms.y * r2, C2 * ms.y * r2}; iv[i] = (f32x2){ir2, ir2};
    }
    f32x2 cgs[NT], cus[NT];
#pragma unroll
    for (int j = 0; j < NT; ++j) { cgs[j] = *(const f32x2*)(colsum + (wn * NT + j) * 16 + fg * 2); cus[j] = *(const f32x2*)(colsum + (wn * NT + j) * 16 + fg * 2 + 8); }
    const unsigned ro = foff<27>(orow, 0);
#pragma unroll
    for (int j = 0; j < NT; ++j) {
      const f32x2 cg = cgs[j], cu = cus[j];
      unsigned d[MT];
#pragma unroll
      for (int i = 0; i < MT; ++i) {
        const f32x2 gg = __builtin_elementwise_fma(nm[i], cg, (f32x2){acc[i][j][0], acc[i][j][1]});
        const f32x2 uu = __builtin_elementwise_fma(nm[i], cu, (f32x2){acc[i][j][2], acc[i][j][3]});
        const f32x2 t = gg * __builtin_elementwise_fma(gg * gg, k2[i], k1[i]);
        const f32x2 e = (f32x2){__builtin_amdgcn_exp2f(t[0]), __builtin_amdgcn_exp2f(t[1])};
        const f32x2 q = __builtin_elementwise_fma(e, iv[i], iv[i]);
        const f32x2 h = (gg * uu) * (f32x2){__builtin_amdgcn_rcpf(q[0]), __builtin_amdgcn_rcpf(q[1])};
        d[i] = cvtpk(h[0], h[1]);
      }
      const u32x2 x = swap32(d[0], d[2]), y = swap32(d[1], d[3]);
      const u32x2 z = swap16(x[0], y[0]), w = swap16(x[1], y[1]);
      const int jb = wn * NT + j;
      if (jb < 13 || !(tn & 1)) stw<0>(out, ro + foff<27>(0, hbase + jb * 8), (u32x4){z[0], z[1], w[0], w[1]});
    }
  } else {
    if (swp) {
      const float sc = tn < DM / BN ? QSCALE : 1.0f;
      float2 ms[MT]; f32x4 cs[NT];
#pragma unroll
      for (int i = 0; i < MT; ++i) ms[i] = rowst[wm * 64 + i * 16 + fr];
#pragma unroll
      for (int j = 0; j < NT; ++j) cs[j] = *(const f32x4*)(colsum + (wn * NT + j) * 16 + fg * 4);
      static_for<8>([&](auto I) {
        constexpr int jq = decltype(I)::v >> 2, i = decltype(I)::v & 3;
        const float rs = ms[i].y * sc;
        const f32x4 o0 = rs * (acc[i][jq * 2] - ms[i].x * cs[jq * 2]), o1 = rs * (acc[i][jq * 2 + 1] - ms[i].x * cs[jq * 2 + 1]);
        const u32x2 x = swap16(cvtpk(o0[0], o0[1]), cvtpk(o1[0], o1[1])), y = swap16(cvtpk(o0[2], o0[3]), cvtpk(o1[2], o1[3]));
        stw<(i & 1) * 256>(ws, so[jq][i >> 1], (u32x4){x[0], y[0], x[1], y[1]});
      });
    } else {
      f32x4 ma[MT], mb[MT]; float cs[NT];
#pragma unroll
      for (int i = 0; i < MT; ++i) { ma[i] = *(const f32x4*)(rowst + wm * 64 + i * 16 + fg * 4); mb[i] = *(const f32x4*)(rowst + wm * 64 + i * 16 + fg * 4 + 2); }
#pragma unroll
      for (int j = 0; j < NT; ++j) cs[j] = colsum[(wn * NT + j) * 16 + fr];
      static_for<8>([&](auto I) {
        constexpr int jq = decltype(I)::v >> 2, i = decltype(I)::v & 3;
        const float c0 = cs[jq * 2], c1 = cs[jq * 2 + 1];
        const f32x4 &p0 = acc[i][jq * 2], &p1 = acc[i][jq * 2 + 1], &a = ma[i], &b = mb[i];
        const u32x2 x = swap32(cvtpk(a[1] * (p0[0] - a[0] * c0), a[3] * (p0[1] - a[2] * c0)), cvtpk(a[1] * (p1[0] - a[0] * c1), a[3] * (p1[1] - a[2] * c1)));
        const u32x2 y = swap32(cvtpk(b[1] * (p0[2] - b[0] * c0), b[3] * (p0[3] - b[2] * c0)), cvtpk(b[1] * (p1[2] - b[0] * c1), b[3] * (p1[3] - b[2] * c1)));
        stw<(i & 1) * VFR>(ws, so[jq][i >> 1], (u32x4){x[0], y[0], x[1], y[1]});
      });
    }
  }
  STAMP(KID, 5);
}

struct Q0Args { const char* x; const char* wqkv; char* ws; };
__global__ __launch_bounds__(512) void qkv0_kernel(Q0Args g) {
  __shared__ __attribute__((aligned(16))) char smem[L_TOTAL];
  KLABEL("kbeg_", 0);
  const int tid = threadIdx.x, bid = blockIdx.x;
  if (tid < 256) STAMP(KID, 0);
  if (bid >= 240) {
    if (bid == 240 && tid < 96) *(unsigned*)(g.ws + WS_CNT + tid * 256) = 0u;
    return;
  }
  if (tid >= 256) {
    loader_phase<CQ0, false>(smem, g.x, g.wqkv, g.ws, WS_STAT, bid, tid);
    KLABEL("kend_", 7);
    return;
  }
  compute_ln<CQ0>(smem, nullptr, g.ws, bid, tid, 0);
  STAMP(KID, 6);
  KLABEL("kend_", 0);
}

struct ChainArgs { const char* xin; char* xmid; char* xout; char* ws; long layer; };
__global__ __launch_bounds__(512) void chain_kernel(ChainArgs g) {
  __shared__ __attribute__((aligned(16))) char smem[L_TOTAL];
  KLABEL("kbeg_", 1);
  const int tid = threadIdx.x, bid = blockIdx.x, layer = (int)g.layer;
  char* ws = g.ws;
  if (tid >= 256) {
    loader_phase<CWO, false>(smem, ws + WS_O, ws + WS_PWO, ws, WS_STAT, opaque(bid), opaque_v(tid));
    loader_phase<CGU, true>(smem, g.xmid, ws + WS_PGU, ws, WS_STAT, opaque(bid), opaque_v(tid));
    loader_phase<CDN, true>(smem, ws + WS_H, ws + WS_PWD, ws, WS_STAT, opaque(bid), opaque_v(tid));
    if (layer == 0) {
      if (bid < 240) loader_phase<CQ1, true>(smem, g.xout, ws + WS_PQKV, ws, WS_STAT2, opaque(bid), opaque_v(tid));
      else { raw_barrier(); raw_barrier(); }
    }
    KLABEL("kend_", 8);
    return;
  }
  STAMP(K0 + 2, 0);
  compute_res<CWO, 0>(smem, g.xin, layer == 0, g.xmid, false, ws, WS_STAT, opaque(bid), opaque_v(tid), layer * 5 + 2);
  STAMP(K0 + 2, 6); STAMP(K0 + 3, 0);
  seam(ws, layer * 3, opaque(bid), opaque_v(tid));
  compute_ln<CGU>(smem, ws + WS_H, ws, opaque(bid), opaque_v(tid), layer * 5 + 3);
  STAMP(K0 + 3, 6); STAMP(K0 + 4, 0);
  seam(ws, layer * 3 + 1, opaque(bid), opaque_v(tid));
  compute_res<CDN, 1>(smem, nullptr, false, g.xout, layer == 1, ws, WS_STAT2, opaque(bid), opaque_v(tid), layer * 5 + 4);
  if (layer == 0) {
    STAMP(K0 + 4, 6); STAMP(K0 + 5, 0);
    seam(ws, 2, opaque(bid), opaque_v(tid));
    if (bid < 240) compute_ln<CQ1>(smem, nullptr, ws, opaque(bid), opaque_v(tid), 5);
  }
  KLABEL("kend_", 1);
}

constexpr int ATT_NBUF = 4, ATT_HALF = 12288, ATT_STB = 2 * ATT_HALF;
struct AttnArgs { char* ws; const u16* wqkv; const u16* wo; const u16* wg; const u16* wu; const u16* wd;
};

#define EX(x) __builtin_amdgcn_exp2f(x)
#define E4(P, I, S, J) P[I] = cvtpk(EX(S[J]), EX(S[J + 1])); P[I + 1] = cvtpk(EX(S[J + 2]), EX(S[J + 3]));
#define MF16(a, b, c) __builtin_amdgcn_mfma_f32_32x32x16_bf16(a, b, c, 0, 0, 0)
#define MF8(a, b, c) __builtin_amdgcn_mfma_f32_32x32x8bf16_1k(a, b, c, 0, 0, 0)
#define BC8(x) __builtin_bit_cast(bf16x8, x)

DI float rowmax16(const f32x16& s) {
  float m = s[0];
#pragma unroll
  for (int i = 1; i < 16; ++i) m = fmaxf(m, s[i]);
  return fmaxf(m, __shfl_xor(m, 32));
}
DI bool nonfinite12(const f32x16& o, int lane) {
  bool bad = false;
#pragma unroll
  for (int i = 0; i < 9; ++i) bad |= !(fabsf(o[i]) < 1.0e38f);
#pragma unroll
  for (int i = 9; i < 12; ++i) bad |= (lane < 32) && !(fabsf(o[i]) < 1.0e38f);
  return bad;
}

struct PackJob { const u16* src; int ld, kk0, kmax, s2; char* dst; float* cs; };
DI PackJob pack_job(const AttnArgs& a, int J, int wid, int lane) {
  const int prow = wid * 4 + (lane >> 4), c16 = lane & 15, ch = (c16 >> 1) ^ (prow & 7);
  const int k = 8 * (((prow >> 3) & 1) * 2 + ((prow >> 2) & 1)) + 4 * ((prow >> 4) & 1) + (prow & 3);
  const int fr = lane & 15, fg = lane >> 4;
  PackJob r;
  if (J >= 243) J -= 243;
  r.kmax = 19;
  if (J < 108) {
    const int cg = J >> 2, kq = J & 3, b = cg * 8 + wid, rr = b % 27, tn = (b / 27) * 2 + (rr >= 14), jb = rr - (rr >= 14) * 14;
    r.src = ((c16 & 1) ? a.wu : a.wg) + (size_t)k * DF + cg * 64 + ch * 8; r.ld = DF; r.kk0 = kq * 5; r.s2 = 28;
    r.dst = a.ws + WS_PGU + (size_t)(tn * 280 + jb * 2) * 1024 + (fg * 16 + ((fr & 7) >> 1) * 4 + (fr >> 3) * 2 + (fr & 1)) * 16;
    r.cs = (float*)(a.ws + WS_CGU) + (tn * 4 + kq) * 224 + jb * 16 + fg * 4;
  } else if (J < 168) {
    const int tn = (J - 108) >> 2, kq = (J - 108) & 3;
    r.src = a.wqkv + (size_t)k * (3 * DM) + tn * 128 + ch * 16 + (c16 & 1) * 8; r.ld = 3 * DM; r.kk0 = kq * 5; r.s2 = 16;
    r.dst = a.ws + WS_PQKV + (size_t)(tn * 160 + wid * 2) * 1024 + lane * 16;
    r.cs = (float*)(a.ws + WS_CQKV) + (tn * 4 + kq) * 128 + wid * 16 + fg * 4;
  } else {
    const bool d = J >= 188;
    const int cg = d ? (J - 188) / 11 : (J - 168) >> 2, kq = d ? (J - 188) % 11 : (J - 168) & 3, jb = cg * 8 + wid;
    r.src = (d ? a.wd : a.wo) + (size_t)k * DM + cg * 128 + ch * 16 + (c16 & 1) * 8; r.ld = DM; r.kk0 = kq * 5; r.s2 = 10;
    if (d) r.kmax = 53;
    r.dst = a.ws + (d ? WS_PWD : WS_PWO) + (size_t)((jb / 5) * (d ? 270 : 100) + (jb % 5) * 2) * 1024 + lane * 16;
    r.cs = nullptr;
  }
  return r;
}

template <int OFF> DI bf16x8 lds16(unsigned a) { bf16x8 r; asm volatile("ds_read_b128 %0, %1 offset:%2" : "=v"(r) : "v"(a), "i"(OFF)); return r; }
template <int OFF> DI bf16x4 lds8(unsigned a) { bf16x4 r; asm volatile("ds_read_b64 %0, %1 offset:%2" : "=v"(r) : "v"(a), "i"(OFF)); return r; }
template <int OFF> DI bf16x4 ldstr(unsigned a) { bf16x4 r; asm volatile("ds_read_b64_tr_b16 %0, %1 offset:%2" : "=v"(r) : "v"(a), "i"(OFF)); return r; }
template <int OFF> DI void gld(unsigned voff, const char* sb, unsigned m) {
  asm volatile("s_mov_b32 m0, %2\n\ts_nop 0\n\tglobal_load_lds_dwordx4 %0, %1 offset:%3" ::"v"(voff), "s"(sb), "s"(m), "i"(OFF) : "memory", "m0");
}
#define ATT_UNROLL 1

template <bool PACK>
__global__ __launch_bounds__(512) void attn_kernel(AttnArgs a) {
  __shared__ __attribute__((aligned(16))) char smem[ATT_NBUF * ATT_STB + (PACK ? 5 * 8192 : 0) + 256];
  KLABEL("kbeg_", 5 + PACK);
  const int tid = threadIdx.x, lane = tid & 63, wid = __builtin_amdgcn_readfirstlane(tid >> 6);
  char* ws = a.ws;
  STAMP(KID, 0);
  const int bid = blockIdx.x, xcd = bid & 7, idx = bid >> 3;
  const int head = xcd * 4 + (idx >> 3), qb = idx & 7, odd = head & 1;
  const int r32 = lane & 31, hh = lane >> 5, half = wid >> 2;
  const int q0 = qb * 256 + (wid & 3) * 64 + r32;
  const char* kvb = uni(ws + WS_KV + (size_t)(head >> 1) * 16 * KVP_CHUNK);
  const unsigned sm = (unsigned)(uintptr_t)smem;
  const unsigned voff = half * 8 * KVP_CHUNK + odd * KVP_TAIL + (wid & 3) * 3072 + lane * 16;
  const unsigned mb = sm + half * ATT_HALF + (wid & 3) * 3072;
  auto stage = [&](int s) {
    const char* gs = kvb + s * KVP_CHUNK; const unsigned gm = mb + (s & 3) * ATT_STB;
    gld<0>(voff, gs, gm); gld<1024>(voff, gs, gm); gld<2048>(voff, gs, gm);
  };
  u32x4 qr1[2]; u32x2 qr2[2];
  {
    const char* Q = ws + WS_Q + (size_t)((head >> 1) * 64 + (q0 >> 5)) * 2560;
#pragma unroll
    for (int i = 0; i < 2; ++i) {
      qr1[i] = gload16(Q + i * 2560 + odd * 1024 + lane * 16);
      qr2[i] = gload8(Q + i * 2560 + 2048 + r32 * 16 + odd * 8);
    }
  }
  stage(0); stage(1); stage(2);
  PackJob pj; unsigned pp; bf16x4 plo, phi; f32x4 pcs = (f32x4){0.f, 0.f, 0.f, 0.f};
  if (PACK) {
    pj = pack_job(a, bid, wid, lane);
    const int pr = 8 * (lane >> 5) + 4 * ((lane >> 4) & 1) + ((lane & 15) >> 2);
    pp = sm + ATT_NBUF * ATT_STB + pr * 256 + (wid ^ (pr & 7)) * 32 + (lane & 3) * 8;
  }
  const char* dummy = smem + ATT_NBUF * ATT_STB + (PACK ? 5 * 8192 : 0);
  unsigned fa[2][3];
  fa[0][0] = sm + half * ATT_HALF + (odd ? KVP_KO - KVP_TAIL : KVP_KE) + lane * 16;
  fa[0][1] = sm + half * ATT_HALF + (odd ? 0 : KVP_TAIL) + r32 * 16 + odd * 8;
  fa[0][2] = sm + half * ATT_HALF + (odd ? KVP_VO - KVP_TAIL : 0) + (hh * 24 + min(r32, 23)) * 16;
#pragma unroll
  for (int i = 0; i < 3; ++i) { fa[1][i] = fa[0][i] + 2 * ATT_STB; asm volatile("" : "+v"(fa[0][i]), "+v"(fa[1][i])); }
  f32x16 oa, ob, nma, nmb, sa, sb;
#pragma unroll
  for (int i = 0; i < 16; ++i) { oa[i] = 0.f; ob[i] = 0.f; }
  wait_vm<6>();
#pragma unroll
  for (int i = 0; i < 2; ++i) asm volatile("" : "+v"(qr1[i]), "+v"(qr2[i]));
  const bf16x8 qa1 = BC8(qr1[0]), qb1 = BC8(qr1[1]);
  const bf16x4 qa2 = __builtin_bit_cast(bf16x4, hh ? (u32x2){0u, 0u} : qr2[0]), qb2 = __builtin_bit_cast(bf16x4, hh ? (u32x2){0u, 0u} : qr2[1]);
  raw_barrier();
  bf16x8 kc1 = lds16<0>(fa[0][0]); bf16x4 kc2 = lds8<0>(fa[0][1]);
  bf16x8 kn1 = lds16<1024>(fa[0][0]); bf16x4 kn2 = lds8<512>(fa[0][1]);
  bf16x8 v0 = lds16<0>(fa[0][2]), v1 = lds16<VFR>(fa[0][2]), vp1 = qa1;
  if (PACK) {
#pragma unroll
    for (int b = 0; b < 5; ++b) glds16(pj.src + (size_t)min(pj.kk0 + b, pj.kmax) * 32 * pj.ld, smem + ATT_NBUF * ATT_STB + b * 8192 + wid * 1024);
  }
  asm volatile("s_waitcnt lgkmcnt(0)" : "+v"(kc1), "+v"(kc2), "+v"(kn1), "+v"(kn2), "+v"(v0), "+v"(v1));
  {
    sa = MF16(kc1, qa1, oa); sb = MF16(kc1, qb1, ob);
    sa = MF8(kc2, qa2, sa); sb = MF8(kc2, qb2, sb);
    const float ma = rowmax16(sa), mb = rowmax16(sb);
#pragma unroll
    for (int i = 0; i < 16; ++i) { nma[i] = -ma; nmb[i] = -mb; sa[i] -= ma; }
  }
  u32x4 p3 = (u32x4){0u, 0u, 0u, 0u};
  STAMP(KID, 2); CSTAMP(KID, 2);
  static_for<32>([&](auto S) {
    constexpr int c = decltype(S)::v >> 2, t = decltype(S)::v & 3;
    constexpr int s1 = decltype(S)::v + 1, s2 = s1 + 1;
    constexpr int b1 = (s1 >> 2) & 3, b2 = (s2 >> 2) & 3;
    constexpr int pb = PACK ? (decltype(S)::v - 10) >> 1 : -1;
    constexpr bool pk = pb >= 0 && pb < 5;
    const char* gs = kvb + (c + 3) * KVP_CHUNK; const unsigned gm = mb + ((c + 3) & 3) * ATT_STB;
    u32x4 p0, p1, p2; bf16x8 kx1, vx0, vx1; bf16x4 kx2;
    SB();
    sb = MF16(kc1, qb1, nmb); SB();
    if (t == 3 && c < 5) { gld<1024>(voff, gs, gm); SB(); }
    E4(p0, 0, sa, 0) SB();
    ob = MF16(vp1, BC8(p3), ob); SB();
    if (t == 3 && c < 5) { gld<2048>(voff, gs, gm); SB(); }
    E4(p0, 2, sa, 4) SB();
    sb = MF8(kc2, qb2, sb); SB();
    if (pk && (t & 1)) asm volatile("s_waitcnt lgkmcnt(0)" : "+v"(kn1), "+v"(kn2), "+v"(v0), "+v"(v1), "+v"(plo), "+v"(phi));
    else asm volatile("s_waitcnt lgkmcnt(0)" : "+v"(kn1), "+v"(kn2), "+v"(v0), "+v"(v1));
    SB();
    E4(p1, 0, sa, 8) SB();
    oa = MF16(v0, BC8(p0), oa); SB();
    if (t == 2 && c < 7) {
      constexpr int W[7] = {8, 8, 3, 5, 7, 6, 1};
      wait_vm<PACK ? W[c < 7 ? c : 0] : (c < 6 ? 3 : 0)>();
      __builtin_amdgcn_s_barrier();
      if (c < 5) gld<0>(voff, gs, gm);
      SB();
    }
    if (pk && (t & 1)) {
      const bf16x8 f = __builtin_shufflevector(plo, phi, 0, 1, 2, 3, 4, 5, 6, 7);
      const int kk = min(pj.kk0 + pb, pj.kmax);
      st16<3>(pj.dst + ((kk >> 1) * pj.s2 + (kk & 1)) * 1024, __builtin_bit_cast(u32x4, f));
      pcs = __builtin_amdgcn_mfma_f32_16x16x32_bf16(f, (bf16x8){0x3f80, 0x3f80, 0x3f80, 0x3f80, 0x3f80, 0x3f80, 0x3f80, 0x3f80}, pcs, 0, 0, 0);
      SB();
    }
    E4(p1, 2, sa, 12) SB();
    if (s1 < 32) { sa = MF16(kn1, qa1, nma); SB(); }
    if (s2 < 32) { kx1 = lds16<(b2 & 1) * ATT_STB + (s2 & 3) * 1024>(fa[b2 >> 1][0]); SB(); }
    E4(p2, 0, sb, 0) SB();
    oa = MF16(v1, BC8(p1), oa); SB();
    if (s2 < 32) { kx2 = lds8<(b2 & 1) * ATT_STB + (s2 & 3) * 512>(fa[b2 >> 1][1]); SB(); }
    if (pk && !(t & 1)) { plo = ldstr<(pk ? pb : 0) * 8192>(pp); phi = ldstr<(pk ? pb : 0) * 8192 + 4096>(pp); SB(); }
    E4(p2, 2, sb, 4) SB();
    if (s1 < 32) { sa = MF8(kn2, qa2, sa); SB(); }
    if (s1 < 32) { vx0 = lds16<(b1 & 1) * ATT_STB + (s1 & 3) * 2 * VFR>(fa[b1 >> 1][2]); SB(); }
    E4(p3, 0, sb, 8) SB();
    ob = MF16(v0, BC8(p2), ob); SB();
    if (s1 < 32) { vx1 = lds16<(b1 & 1) * ATT_STB + (s1 & 3) * 2 * VFR + VFR>(fa[b1 >> 1][2]); SB(); }
    if (TC && PACK && decltype(S)::v == 26 && wid == 0) touch_code<1>(dummy, lane);
    E4(p3, 2, sb, 12) SB();
    vp1 = v1; kc1 = kn1; kc2 = kn2;
    if (s2 < 32) { kn1 = kx1; kn2 = kx2; }
    if (s1 < 32) { v0 = vx0; v1 = vx1; }
  });
  ob = MF16(vp1, BC8(p3), ob);
  if (PACK && pj.cs != nullptr && (lane & 15) == 0) *(f32x4*)pj.cs = pcs;
  float ma = -nma[0], mb2 = -nmb[0];
  STAMP(KID, 3); CSTAMP(KID, 3);
  if (__syncthreads_or(nonfinite12(oa, lane) || nonfinite12(ob, lane))) {
    stage(0); stage(1); stage(2);
#pragma unroll
    for (int i = 0; i < 16; ++i) { oa[i] = 0.f; ob[i] = 0.f; }
    ma = -3.0e38f; mb2 = -3.0e38f;
#pragma unroll 1
    for (int c = 0; c < 8; ++c) {
      if (c < 6) wait_vm<6>(); else if (c == 6) wait_vm<3>(); else wait_vm<0>();
      raw_barrier();
      if (c + 3 < 8) stage(c + 3);
      const unsigned bo = (c & 3) * ATT_STB;
#pragma unroll 1
      for (int t = 0; t < 4; ++t) {
        kc1 = lds16<0>(fa[0][0] + bo + t * 1024); kc2 = lds8<0>(fa[0][1] + bo + t * 512);
        v0 = lds16<0>(fa[0][2] + bo + t * 2 * VFR); v1 = lds16<VFR>(fa[0][2] + bo + t * 2 * VFR);
        asm volatile("s_waitcnt lgkmcnt(0)" : "+v"(kc1), "+v"(kc2), "+v"(v0), "+v"(v1));
        auto one = [&](const bf16x8& q1, const bf16x4& q2, f32x16& o, float& m) {
          f32x16 s;
#pragma unroll
          for (int i = 0; i < 16; ++i) s[i] = 0.f;
          s = MF16(kc1, q1, s); s = MF8(kc2, q2, s);
          const float mn = fmaxf(m, rowmax16(s)), f = EX(m - mn);
          m = mn;
          u32x4 p0, p1;
#pragma unroll
          for (int i = 0; i < 16; ++i) { s[i] -= mn; o[i] *= f; }
          E4(p0, 0, s, 0) E4(p0, 2, s, 4) E4(p1, 0, s, 8) E4(p1, 2, s, 12)
          o = MF16(v0, BC8(p0), o); o = MF16(v1, BC8(p1), o);
        };
        one(qa1, qa2, oa, ma); one(qb1, qb2, ob, mb2);
      }
    }
    __syncthreads();
  }
  STAMP(KID, 4);
  float* xo = (float*)smem + (wid ^ 4) * 13 * 64 + lane;
  const float* xi = (const float*)smem + wid * 13 * 64 + lane;
#pragma unroll
  for (int i = 0; i < 12; ++i) xo[i * 64] = half ? oa[i] : ob[i];
  xo[12 * 64] = half ? ma : mb2;
  __syncthreads();
  {
    const float m1 = half ? mb2 : ma, m2 = xi[12 * 64], M = fmaxf(m1, m2);
    const float f1 = EX(m1 - M), f2 = EX(m2 - M);
    float o[12];
#pragma unroll
    for (int i = 0; i < 12; ++i) o[i] = (half ? ob[i] : oa[i]) * f1 + xi[i * 64] * f2;
    const float l = __shfl(o[8], r32 + 32);
    const float inv = 1.0f / l;
    const int q = q0 + half * 32, c0 = head * DH + hh * 4;
    char* O = ws + WS_O;
    st8<4>(O + foff<10>(q, c0), (u32x2){cvtpk(o[0] * inv, o[1] * inv), cvtpk(o[2] * inv, o[3] * inv)});
    st8<4>(O + foff<10>(q, c0 + 8), (u32x2){cvtpk(o[4] * inv, o[5] * inv), cvtpk(o[6] * inv, o[7] * inv)});
    if (hh == 0) st8<4>(O + foff<10>(q, c0 + 16), (u32x2){cvtpk(o[8] * inv, o[9] * inv), cvtpk(o[10] * inv, o[11] * inv)});
  }
  STAMP(KID, 5);
  wait_vm<0>();
  STAMP(KID, 6);
  KLABEL("kend_", 5 + PACK);
}

extern "C" void kernel_launch(void* const* d_in, const int* in_sizes, int n_in, void* d_out, int out_size, void* d_ws,
                              size_t ws_size, hipStream_t stream) {
  const char* x0 = (const char*)d_in[0];
  char* ws = (char*)d_ws;
  Q0Args q0{x0, (const char*)d_in[1], ws};
  qkv0_kernel<<<256, 512, 0, stream>>>(q0);
  for (int layer = 0; layer < 2; ++layer) {
    AttnArgs at{ws, (const u16*)d_in[1], (const u16*)d_in[2], (const u16*)d_in[4], (const u16*)d_in[5], (const u16*)d_in[6] KIDARG(layer * 5 + 1)};
    if (layer == 0) attn_kernel<true><<<256, 512, 0, stream>>>(at); else attn_kernel<false><<<256, 512, 0, stream>>>(at);
    ChainArgs c{layer ? ws + WS_X2 : x0, ws + (layer ? WS_X3 : WS_X1), layer ? (char*)d_out : ws + WS_X2, ws, layer};
    chain_kernel<<<256, 512, 0, stream>>>(c);
  }
}
```

```cpp
#include <hip/hip_runtime.h>
#include <hip/hip_bf16.h>
#include <stdint.h>

typedef __attribute__((ext_vector_type(8))) short bf16x8;
typedef __attribute__((ext_vector_type(4))) short bf16x4;
typedef __attribute__((ext_vector_type(4))) float f32x4;
typedef __attribute__((ext_vector_type(16))) float f32x16;
typedef __attribute__((ext_vector_type(4))) unsigned u32x4;
typedef __attribute__((ext_vector_type(2))) unsigned u32x2;
typedef unsigned short u16;

#define DI __device__ __forceinline__

constexpr int SEQ = 2048, DM = 640, NH = 32, DH = 20, DF = 1728;
constexpr float LN_EPS = 1e-5f;
constexpr float QSCALE = 0.22360679774997896f * 1.4426950408889634f;

constexpr size_t WS_X1   = 0;
constexpr size_t WS_X2   = WS_X1 + (size_t)SEQ * DM * 2;
constexpr size_t WS_X3   = WS_X2 + (size_t)SEQ * DM * 2;
constexpr size_t WS_Q    = WS_X3 + (size_t)SEQ * DM * 2;
constexpr size_t Q_SZ    = (size_t)SEQ * DM * 2;
constexpr size_t WS_O    = WS_Q + 2 * Q_SZ;
constexpr size_t WS_H    = WS_O + (size_t)SEQ * DM * 2;
constexpr int    KVP_CHUNK = 22528, KVP_KE = 6144, KVP_TAIL = 10240, KVP_KO = 12288, KVP_VO = 16384, VFR = 768;
constexpr size_t WS_KV   = WS_H + (size_t)SEQ * DF * 2;
constexpr size_t KV_SZ   = (size_t)16 * 16 * KVP_CHUNK;
constexpr size_t WS_PWO  = WS_KV + 2 * KV_SZ;
constexpr size_t WS_PWD  = WS_PWO + (size_t)DM * DM * 2 + 4096;
constexpr size_t WS_PGU  = WS_PWD + (size_t)DF * DM * 2;
constexpr size_t WS_PQKV = WS_PGU + (size_t)16 * 10 * 28 * 1024;
constexpr size_t WS_CGU  = WS_PQKV + (size_t)DM * 3 * DM * 2;
constexpr size_t WS_CQKV = WS_CGU + (size_t)16 * 4 * 224 * 4;
constexpr size_t WS_STAT = WS_CQKV + (size_t)15 * 4 * 128 * 4;
constexpr size_t WS_STAT2 = WS_STAT + (size_t)SEQ * 64;
constexpr size_t WS_FLG  = WS_STAT2 + (size_t)SEQ * 64;
constexpr size_t WS_PFLG = WS_FLG + 8 * 256 * 128;
constexpr size_t WS_END  = WS_PFLG + 4096;

typedef __attribute__((ext_vector_type(2))) __bf16 bf16p;
typedef __attribute__((ext_vector_type(2))) float f32x2;
DI unsigned cvtpk(float lo, float hi) { return __builtin_bit_cast(unsigned, __builtin_convertvector((f32x2){lo, hi}, bf16p)); }
DI float dot2ones(unsigned a, float s) {
  return __builtin_amdgcn_fdot2_f32_bf16(__builtin_bit_cast(bf16p, a), __builtin_bit_cast(bf16p, 0x3f803f80u), s, false);
}
DI float bflo(unsigned v) { return __uint_as_float(v << 16); }
DI float bfhi(unsigned v) { return __uint_as_float(v & 0xffff0000u); }
#define KIDARG(k)
#define STAMP(kid, i)
#define CSTAMP(kid, i)
template <int N> DI void wait_vm() { asm volatile("s_waitcnt vmcnt(%0)" ::"i"(N) : "memory"); }
DI void raw_barrier() {
  asm volatile("" ::: "memory");
  __builtin_amdgcn_s_waitcnt(0xC07F);
  __builtin_amdgcn_s_barrier();
  asm volatile("" ::: "memory");
}
DI u32x2 swap16(unsigned a, unsigned b) { return __builtin_amdgcn_permlane16_swap(a, b, false, false); }
DI u32x2 swap32(unsigned a, unsigned b) { return __builtin_amdgcn_permlane32_swap(a, b, false, false); }
DI bf16x4 tr_read(const char* p) {
  typedef short v4i16_t __attribute__((ext_vector_type(4)));
  return __builtin_bit_cast(bf16x4, __builtin_amdgcn_ds_read_tr16_b64_v4i16((__attribute__((address_space(3))) v4i16_t*)(p)));
}
template <bool SC = false> DI void glds16(const void* g, const char* l) {
  unsigned sv; const unsigned lb = __builtin_amdgcn_readfirstlane((unsigned)(uintptr_t)l);
  if (SC) asm volatile("s_mov_b32 %0, m0\n\ts_mov_b32 m0, %2\n\ts_nop 0\n\tglobal_load_lds_dwordx4 %1, off sc1\n\ts_mov_b32 m0, %0"
               : "=&s"(sv) : "v"(g), "s"(lb) : "memory");
  else asm volatile("s_mov_b32 %0, m0\n\ts_mov_b32 m0, %2\n\ts_nop 0\n\tglobal_load_lds_dwordx4 %1, off\n\ts_mov_b32 m0, %0"
               : "=&s"(sv) : "v"(g), "s"(lb) : "memory");
}
DI void gtouch(const void* g, const char* l) {
  unsigned sv; const unsigned lb = __builtin_amdgcn_readfirstlane((unsigned)(uintptr_t)l);
  asm volatile("s_mov_b32 %0, m0\n\ts_mov_b32 m0, %2\n\ts_nop 0\n\tglobal_load_lds_dword %1, off\n\ts_mov_b32 m0, %0"
               : "=&s"(sv) : "v"(g), "s"(lb) : "memory");
}
#define KLABEL(pfx, id) asm volatile(pfx "%c0:" ::"i"(id))
template <int ID> DI unsigned long long kaddr() {
  unsigned long long a;
  asm volatile("s_getpc_b64 s[20:21]\n\ts_add_u32 s20, s20, kbeg_%c1@rel32@lo+4\n\ts_addc_u32 s21, s21, kbeg_%c1@rel32@hi+12\n\ts_mov_b64 %0, s[20:21]" : "=s"(a) : "i"(ID) : "s20", "s21", "scc");
  return a;
}
template <int A, int B> DI void touch_code(const char* dummy, int part, int lane) {
  const unsigned long long lo = kaddr<A>() & ~255ull, hi = kaddr<B>();
  const unsigned o = part * 8192 + lane * 128;
  if (hi > lo && o < (unsigned)(hi - lo)) gtouch((const char*)lo + o, dummy);
}
#define B0M 1
#define ASC 1
#define NPRE 9
#define SLP 1
#define MAPR 8
#define MAPL 8
#define MAP0 2
#define AST 1
#define TC 7
DI u32x2 gload8(const void* g) {
  u32x2 r; asm volatile("global_load_dwordx2 %0, %1, off" : "=v"(r) : "v"(g) : "memory"); return r;
}
DI unsigned gload4(const void* g) {
  unsigned r; asm volatile("global_load_dword %0, %1, off" : "=v"(r) : "v"(g) : "memory"); return r;
}
DI u32x4 gload16(const void* g) {
  u32x4 r; asm volatile("global_load_dwordx4 %0, %1, off" : "=v"(r) : "v"(g) : "memory"); return r;
}
DI u32x4 gload16sc(const void* g) {
  u32x4 r; asm volatile("global_load_dwordx4 %0, %1, off sc1" : "=v"(r) : "v"(g) : "memory"); return r;
}
DI void st4sc(void* p, float v) { asm volatile("global_store_dword %0, %1, off sc1" ::"v"(p), "v"(v) : "memory"); }
DI void st16sc(void* p, u32x4 v) { asm volatile("global_store_dwordx4 %0, %1, off sc1\n\ts_nop 1" ::"v"(p), "v"(v)); }
#define ST_MODE 14
template <int CLS> DI void st16(void* p, u32x4 v) {
  if ((ST_MODE >> CLS) & 1) asm volatile("global_store_dwordx4 %0, %1, off sc1\n\ts_nop 1" ::"v"(p), "v"(v));
  else *(u32x4*)p = v;
}
template <int IMM, bool WT = true> DI void stw(const void* base, unsigned off, u32x4 v) {
  if (WT) asm volatile("global_store_dwordx4 %0, %1, %2 offset:%3 sc1\n\ts_nop 1" ::"v"(off), "v"(v), "s"(base), "i"(IMM));
  else asm volatile("global_store_dwordx4 %0, %1, %2 offset:%3\n\ts_nop 1" ::"v"(off), "v"(v), "s"(base), "i"(IMM));
}
template <int CLS> DI void st8(void* p, u32x2 v) {
  if ((ST_MODE >> CLS) & 1) asm volatile("global_store_dwordx2 %0, %1, off sc1" ::"v"(p), "v"(v) : "memory");
  else *(u32x2*)p = v;
}
template <int I> struct IC { static constexpr int v = I; };
template <int L, int NMAX> DI void wait_stages(int n) {
  if constexpr (NMAX == 0) wait_vm<0>();
  else { if (n >= NMAX) wait_vm<NMAX * L>(); else wait_stages<L, NMAX - 1>(n); }
}
template <int N, int I = 0, class F> DI void static_for(F&& f) {
  if constexpr (I < N) { f(IC<I>{}); static_for<N, I + 1>(f); }
}
#define SB() __builtin_amdgcn_sched_barrier(0)
template <int NS> DI unsigned foff(int row, int col) {
  return (unsigned)((((row >> 6) * NS + (col >> 6)) * 8 + ((row >> 4) & 3) * 2 + ((col >> 5) & 1)) * 1024 +
                    (((col >> 3) & 3) * 16 + (row & 15)) * 16 + (col & 7) * 2);
}

enum { EPI_QKV = 0, EPI_RES = 1, EPI_GEGLU = 2 };
constexpr int L_TOTAL = 163840, L_SMALL = L_TOTAL - 2304, L_CNT = L_SMALL + 1020 + 1024, L_XB = L_SMALL - 20480, L_XHI = L_XB + 8192;
constexpr int WO_BHI = 98304, WO_A = 40960, WO_NA = 7;
constexpr int wo_b(int s) { return s < 6 ? WO_BHI + s * 10240 : (s - 6) * 10240; }
DI int xb_off(int w, int j) { return j < 3 ? L_XHI + (w * 3 + j) * 1024 : L_XB + (w * 2 + j - 3) * 1024; }

template <int BM_, int BN_, int KTOT_, int NST_, int EPI_, bool RAW_> struct Cfg {
  static constexpr int BM = BM_, BN = BN_, KTOT = KTOT_, NST = NST_, EPI = EPI_;
  static constexpr bool RAW = RAW_, RES = (EPI_ == EPI_RES), LNP = !RES && !RAW_;
  static constexpr int MT = RES ? 2 : 4, NT = RES ? 5 : BN_ / 32, KT = KTOT_ / 64;
  static constexpr int PA = BM_ / 8, PB = BN_ / 8, P = PA + PB, LMAX = (P + 3) / 4;
  static constexpr int ABYTES = BM_ * 128, STB = P * 1024;
  static constexpr int RS = BN_ * 2;
};
typedef Cfg<128, 128, 640, 4, EPI_QKV, true> CQ0;
typedef Cfg<128, 128, 640, 4, EPI_QKV, false> CQ1;
typedef Cfg<64, 80, 640, 8, EPI_RES, false> CWO;
typedef Cfg<128, 224, 640, 3, EPI_GEGLU, false> CGU;
typedef Cfg<64, 80, 1728, 8, EPI_RES, false> CDN;

template <class C> DI void tile_of(int bid, int& tm, int& tn) {
  const int xcd = bid & 7, idx = bid >> 3;
  constexpr int R = C::RES ? MAPR : (C::EPI == EPI_QKV ? MAP0 : MAPL), NC = (C::RES ? 8 : 16) / (32 / R);
  tm = (xcd / NC) * R + idx % R; tn = (idx / R) * NC + xcd % NC;
}
DI const char* uni(const char* p) {
  const unsigned long long v = (unsigned long long)p;
  return (const char*)(((unsigned long long)(unsigned)__builtin_amdgcn_readfirstlane((unsigned)(v >> 32)) << 32) | (unsigned)__builtin_amdgcn_readfirstlane((unsigned)v));
}
DI char* opaque(char* p) { asm volatile("" : "+s"(p)); return p; }
DI int opaque(int x) { asm volatile("" : "+s"(x)); return x; }
DI int opaque_v(int x) { asm volatile("" : "+v"(x)); return x; }

template <int KT, int N, int L, int LA, int E, int NP> constexpr int ld_allowed(int i) {
  int pos = NP * (L - LA), last = 0;
  for (int s = 0; s < N; ++s) { pos += s < NP ? LA : L; if (s == i) last = pos; }
  for (int it = 1; it < i; ++it) {
    if (it <= KT - N) { pos += L; if (it - 1 + N == i) last = pos; }
    if (it == KT - N) pos += E;
  }
  return pos - last;
}

template <class C, int SEAM> DI void loader_phase(char* smem, const char* A, const char* B, const char* ws, size_t stat, int bid, int tid) {
  constexpr int KT = C::KT, NST = C::NST, PA = C::PA, PB = C::PB, P = C::P, LMAX = C::LMAX, STB = C::STB, BN = C::BN;
  constexpr bool RAW = C::RAW, LNP = C::LNP;
  const int lane = tid & 63, lw = __builtin_amdgcn_readfirstlane(tid >> 6) - 4;
  int tm, tn; tile_of<C>(bid, tm, tn);
  const int m0 = tm * C::BM, n0 = tn * BN;
  const char* src[LMAX];
#pragma unroll
  for (int i = 0; i < LMAX; ++i) {
    const int p = lw + 4 * i;
    if (RAW) {
      if (i < PA / 4) {
        const int row = p * 8 + (lane >> 3), c = (lane & 7) ^ ((row >> 1) & 7);
        src[i] = A + ((size_t)(m0 + row) * C::KTOT + c * 8) * 2;
      } else {
        const int u = (p - PA) * 64 + lane, prow = u >> 4, c16 = u & 15;
        const int ch = (c16 >> 1) ^ (prow & 7);
        const int k = (prow & 32) + 8 * (((prow >> 3) & 1) * 2 + ((prow >> 2) & 1)) + 4 * ((prow >> 4) & 1) + (prow & 3);
        src[i] = B + ((size_t)k * (3 * DM) + n0 + ch * 16 + (c16 & 1) * 8) * 2;
      }
    } else {
      if (i < PA / 4) src[i] = A + ((size_t)((m0 >> 6) + (p >> 3)) * KT * 8 + (p & 7)) * 1024 + lane * 16;
      else src[i] = B + ((size_t)tn * KT * PB + (p - PA < PB ? p - PA : 0)) * 1024 + lane * 16;
    }
  }
  constexpr int STR_A = RAW ? 128 : 8192, STR_B = RAW ? 64 * 3 * DM * 2 : PB * 1024;
  auto body = [&](auto LC) {
    constexpr int L = decltype(LC)::v, LA = PA / 4;
    auto issue = [&](int buf, auto I0, auto I1) {
      char* base = smem + buf * STB + lw * 1024;
#pragma unroll
      for (int i = decltype(I0)::v; i < decltype(I1)::v; ++i) {
        if (i < LA) glds16<(SEAM != 0 && ASC)>(src[i], base + i * 4096); else glds16(src[i], base + i * 4096);
        src[i] += (i < LA) ? STR_A : STR_B;
      }
    };
    const int t = tid - 256;
    unsigned c[4]; u32x4 st[4];
    auto ln_loads = [&]() {
      const float* cp = (const float*)(ws + (C::EPI == EPI_QKV ? WS_CQKV : WS_CGU)) + tn * 4 * BN + (t < BN ? t : 0);
      const char* sp = ws + stat + (size_t)(m0 + (t & 127)) * 64;
#pragma unroll
      for (int k = 0; k < 4; ++k) c[k] = gload4(cp + k * BN);
#pragma unroll
      for (int k = 0; k < 4; ++k) st[k] = gload16sc(sp + k * 16);
    };
    if constexpr (SEAM != 0) {
      constexpr int E = LNP ? 8 : 0;
      constexpr int NP = LNP ? (NPRE < NST ? NPRE : NST) : NST;
      if (SEAM == 2) raw_barrier();
#pragma unroll
      for (int s = 0; s < NP; ++s) issue(s, IC<LA>{}, IC<L>{});
      raw_barrier();
#pragma unroll
      for (int s = 0; s < NST; ++s) { issue(s, IC<0>{}, IC<LA>{}); if (s >= NP) issue(s, IC<LA>{}, IC<L>{}); }
      auto iter = [&](auto I) {
        constexpr int i = decltype(I)::v;
        wait_vm<ld_allowed<KT, NST, L, LA, E, NP>(i)>();
        raw_barrier();
        if (i >= 1 && i <= KT - NST) issue((i - 1) % NST, IC<0>{}, IC<L>{});
        if (LNP && i == KT - NST) ln_loads();
      };
      if constexpr (KT >= 3 * NST) {
        static_for<NST>(iter);
        int ibuf = NST - 1;
#pragma unroll 1
        for (int i = NST; i < KT - NST; ++i) {
          wait_vm<(NST - 2) * L>();
          raw_barrier();
          issue(ibuf, IC<0>{}, IC<L>{});
          if (++ibuf == NST) ibuf = 0;
        }
        static_for<KT, KT - NST>(iter);
      } else static_for<KT>(iter);
    } else {
      issue(0, IC<0>{}, IC<L>{}); issue(1, IC<0>{}, IC<L>{});
      int nxt = 2, ibuf = 2;
      constexpr int NMAIN = LNP ? KT - NST + 1 : KT;
      for (int i = 0; i < NMAIN; ++i) {
        wait_stages<L, NST - 1>(nxt - i - 1);
        raw_barrier();
        const int lim = min(KT, i + NST);
#pragma unroll
        for (int r = 0; r < 2; ++r)
          if (nxt < lim) { issue(ibuf, IC<0>{}, IC<L>{}); ++nxt; if (++ibuf == NST) ibuf = 0; }
      }
      if (LNP) {
        ln_loads();
        static_for<NST - 1>([&](auto I) { wait_vm<(NST - 2 - decltype(I)::v) * L + 8>(); raw_barrier(); });
      }
    }
    if (LNP) {
      float2* rowst = (float2*)(smem + L_SMALL);
      float* colsum = (float*)(smem + L_SMALL + 1024);
      wait_vm<0>();
      float cs = 0.f;
#pragma unroll
      for (int k = 0; k < 4; ++k) { asm volatile("" : "+v"(c[k])); cs += __uint_as_float(c[k]); }
#pragma unroll
      for (int k = 0; k < 4; ++k) asm volatile("" : "+v"(st[k]));
      const f32x4 sv = (__builtin_bit_cast(f32x4, st[0]) + __builtin_bit_cast(f32x4, st[1])) + (__builtin_bit_cast(f32x4, st[2]) + __builtin_bit_cast(f32x4, st[3]));
      const float mean = (sv[0] + sv[2]) * (1.0f / C::KTOT);
      const float var = fmaxf((sv[1] + sv[3]) * (1.0f / C::KTOT) - mean * mean, 0.f);
      if (t < BN) colsum[t] = cs;
      if (t >= 128) rowst[t - 128] = make_float2(mean, rsqrtf(var + LN_EPS));
    }
    raw_barrier();
  };
  if (P % 4 == 0 || lw < P % 4) body(IC<LMAX>{}); else body(IC<LMAX - 1>{});
}

DI void loader_wo(char* smem, const char* A, int bid, int tid) {
  const int lane = tid & 63, lw = __builtin_amdgcn_readfirstlane(tid >> 6) - 4;
  int tm, tn; tile_of<CWO>(bid, tm, tn);
  const char* src = A + ((size_t)tm * 80 + lw * 2) * 1024 + lane * 16;
  auto issue = [&](int s) {
    glds16<true>(src + s * 8192, smem + WO_A + (s % WO_NA) * 8192 + lw * 2048);
    glds16<true>(src + s * 8192 + 1024, smem + WO_A + (s % WO_NA) * 8192 + lw * 2048 + 1024);
  };
  raw_barrier();
#pragma unroll
  for (int s = 0; s < WO_NA; ++s) issue(s);
  static_for<10>([&](auto I) {
    constexpr int i = decltype(I)::v;
    wait_vm<ld_allowed<10, WO_NA, 2, 2, 0, 0>(i)>();
    raw_barrier();
    if (i >= 1 && i <= 10 - WO_NA) issue(i - 1 + WO_NA);
  });
  raw_barrier();
}

template <int NW> DI void publish(char* smem, char* ws, int seam, int tile, int tid) {
  wait_vm<0>();
  if ((tid & 63) == 0) {
    const unsigned old = __hip_atomic_fetch_add((unsigned*)(smem + L_CNT), 8u / NW, __ATOMIC_RELAXED, __HIP_MEMORY_SCOPE_WORKGROUP);
    if (((old + 8u / NW) & 7) == 0) __hip_atomic_store((unsigned*)(ws + WS_FLG) + (seam * 256 + tile) * 32, 1u, __ATOMIC_RELAXED, __HIP_MEMORY_SCOPE_AGENT);
  }
}
DI void await(const char* ws, int seam, int tile) {
  const unsigned* p = (const unsigned*)(ws + WS_FLG) + (seam * 256 + tile) * 32;
  unsigned v = tile < 0 ? 1u : 0u;
  for (int s = 0; s < (1 << 18); ++s) {
    if (v != 1u) v = __hip_atomic_load(p, __ATOMIC_RELAXED, __HIP_MEMORY_SCOPE_AGENT);
    if (__builtin_amdgcn_ballot_w64(v != 1u) == 0) break;
    __builtin_amdgcn_s_sleep(SLP);
  }
}

template <class C, int MODE> DI void compute_res(char* smem, const char* res, bool res_raw, char* out, bool out_raw, char* ws, size_t stat, int seam, int bid, int tid, int KID) {
  constexpr int KT = C::KT, NST = C::NST, NT = C::NT, STB = C::STB, ABYTES = C::ABYTES;
  const int lane = tid & 63, wid = __builtin_amdgcn_readfirstlane(tid >> 6), fr = lane & 15, fg = lane >> 4, wm = wid >> 1, wn = wid & 1;
  int tm, tn; tile_of<C>(bid, tm, tn);
  const int m0 = tm * C::BM, n0 = tn * C::BN;
  f32x4 acc[2][NT];
#pragma unroll
  for (int i = 0; i < 2; ++i)
#pragma unroll
    for (int j = 0; j < NT; ++j) acc[i][j] = (f32x4){0.f, 0.f, 0.f, 0.f};
  const int row = m0 + wid * 16 + fr;
  u32x2 resv[NT];
#pragma unroll
  for (int j = 0; j < NT; ++j) {
    const int col = n0 + j * 16 + fg * 4;
    if (MODE == 0) resv[j] = *(const u32x2*)(res + (res_raw ? (unsigned)(row * DM + col) * 2 : foff<10>(row, col)));
    else resv[j] = *(const u32x2*)(smem + L_XHI + wid * 3072 + j * 512 + lane * 8);
  }
  const int aoff = (wm * 4 + wn) * 1024 + lane * 16, boff = wn * 1024 + lane * 16;
  bf16x8 a0[2], b0[NT], a1[2], b1[NT];
  auto rd = [&](bf16x8(&a)[2], bf16x8(&b)[NT], const char* pa, const char* pb) {
#pragma unroll
    for (int i = 0; i < 2; ++i) a[i] = *(const bf16x8*)(pa + aoff + i * 2048);
#pragma unroll
    for (int j = 0; j < NT; ++j) b[j] = *(const bf16x8*)(pb + boff + j * 2048);
    SB();
  };
  auto mm = [&](bf16x8(&a)[2], bf16x8(&b)[NT]) {
#pragma unroll
    for (int j = 0; j < NT; ++j)
#pragma unroll
      for (int i = 0; i < 2; ++i) acc[i][j] = __builtin_amdgcn_mfma_f32_16x16x32_bf16(b[j], a[i], acc[i][j], 0, 0, 0);
    SB();
  };
  if (wid == 1) await(ws, seam, MODE == 0 ? (lane < 32 ? (tm >> 2) * 32 + lane : -1) : (lane < 16 ? (tm >> 1) * 16 + lane : -1));
  raw_barrier();
  STAMP(KID, 1);
  raw_barrier();
  STAMP(KID, 2); CSTAMP(KID, 2);
  if constexpr (MODE == 0) {
    rd(a0, b0, smem + WO_A, smem + wo_b(0));
    static_for<5>([&](auto I) {
      constexpr int k1 = decltype(I)::v * 2 + 1, k2 = k1 + 1;
      raw_barrier();
      rd(a1, b1, smem + WO_A + (k1 % WO_NA) * 8192, smem + wo_b(k1));
      mm(a0, b0);
      if (k2 < 10) {
        raw_barrier();
        rd(a0, b0, smem + WO_A + (k2 % WO_NA) * 8192, smem + wo_b(k2 < 10 ? k2 : 0));
      }
      mm(a1, b1);
    });
  } else {
    rd(a0, b0, smem, smem + ABYTES);
    int buf = 0;
#pragma unroll 1
    for (int kt = 0; kt + 2 < KT; kt += 2) {
      if (++buf == NST) buf = 0;
      raw_barrier();
      rd(a1, b1, smem + buf * STB, smem + buf * STB + ABYTES);
      mm(a0, b0);
      if (++buf == NST) buf = 0;
      raw_barrier();
      rd(a0, b0, smem + buf * STB, smem + buf * STB + ABYTES);
      mm(a1, b1);
    }
    static_assert(MODE == 0 || KT % 2 == 1, "");
    mm(a0, b0);
  }
  STAMP(KID, 3); CSTAMP(KID, 3);
#pragma unroll
  for (int j = 0; j < NT; ++j) *(f32x4*)(smem + xb_off(wid ^ 1, j) + lane * 16) = wn ? acc[0][j] : acc[1][j];
  raw_barrier();
  STAMP(KID, 4);
  float s = 0.f, q = 0.f;
  u32x2 v[NT];
#pragma unroll
  for (int j = 0; j < NT; ++j) {
    const f32x4 o = *(const f32x4*)(smem + xb_off(wid, j) + lane * 16) + (wn ? acc[1][j] : acc[0][j]);
    const u32x2 r = resv[j];
    v[j] = (u32x2){cvtpk(o[0] + bflo(r[0]), o[1] + bfhi(r[0])), cvtpk(o[2] + bflo(r[1]), o[3] + bfhi(r[1]))};
    const float e0 = bflo(v[j][0]), e1 = bfhi(v[j][0]), e2 = bflo(v[j][1]), e3 = bfhi(v[j][1]);
    s += (e0 + e1) + (e2 + e3);
    q += (e0 * e0 + e1 * e1) + (e2 * e2 + e3 * e3);
  }
#pragma unroll
  for (int jp = 0; jp < NT; jp += 2) {
    const int j1 = jp + 1 < NT ? jp + 1 : jp;
    const u32x2 x = swap16(v[jp][0], v[j1][0]), y = swap16(v[jp][1], v[j1][1]);
    const int col = n0 + (jp + (fg & 1)) * 16 + (fg >> 1) * 8;
    if (jp + 1 < NT || !(fg & 1))
      st16sc(out + (out_raw ? (unsigned)(row * DM + col) * 2 : foff<10>(row, col)), (u32x4){x[0], y[0], x[1], y[1]});
  }
  {
    const u32x2 t1 = swap16(__float_as_uint(s), __float_as_uint(q));
    const unsigned t2 = __float_as_uint(__uint_as_float(t1[0]) + __uint_as_float(t1[1]));
    const u32x2 t3 = swap32(t2, t2);
    if (lane < 32) st4sc(ws + stat + (size_t)(row * 8 + tn) * 8 + fg * 4, __uint_as_float(t3[0]) + __uint_as_float(t3[1]));
  }
  if (MODE == 0) {
#pragma unroll
    for (int j = 0; j < NT; ++j) *(u32x2*)(smem + L_XHI + wid * 3072 + j * 512 + lane * 8) = v[j];
  }
  STAMP(KID, 5);
  if (!out_raw) {
    if (B0M == 2) raw_barrier();
    publish<4>(smem, ws, seam + 1, tm * 8 + tn, tid);
  }
  STAMP(KID, 6);
}

template <class C> DI void compute_ln(char* smem, char* out, char* ws, int seam, int bid, int tid, int KID) {
  constexpr int KT = C::KT, NST = C::NST, NT = C::NT, MT = C::MT, STB = C::STB, ABYTES = C::ABYTES, RS = C::RS, BN = C::BN, EPI = C::EPI, KTOT = C::KTOT;
  constexpr bool RAW = C::RAW;
  constexpr unsigned OQ = (unsigned)(WS_Q + (RAW ? 0 : Q_SZ)), OKV = (unsigned)(WS_KV + (RAW ? 0 : KV_SZ));
  const int lane = tid & 63, wid = __builtin_amdgcn_readfirstlane(tid >> 6), fr = lane & 15, fg = lane >> 4, wm = wid >> 1, wn = wid & 1;
  int tm, tn; tile_of<C>(bid, tm, tn);
  const int m0 = tm * C::BM, n0 = tn * BN;
  float2* rowst = (float2*)(smem + L_SMALL);
  float* colsum = (float*)(smem + L_SMALL + 1024);
  const char* dummy = smem + L_SMALL + 2048;
  f32x4 acc[MT][NT];
#pragma unroll
  for (int i = 0; i < MT; ++i)
#pragma unroll
    for (int j = 0; j < NT; ++j) acc[i][j] = (f32x4){0.f, 0.f, 0.f, 0.f};
  if (RAW && tn >= 10) {
    const int vc0 = n0 - 2 * DM, head = (vc0 + DH - 1) / DH + (tid >> 4);
    if (head * DH < vc0 + BN) {
      const int tok = m0 + ((tid >> 1) & 7) * 16;
      char* p = ws + OKV + (size_t)((head >> 1) * 16 + (tok >> 7)) * KVP_CHUNK + (head & 1) * KVP_VO + ((tok >> 4) & 7) * VFR + ((tid & 1) * 24 + DH) * 16;
#pragma unroll
      for (int l = 0; l < 2; ++l) {
        *(u32x4*)(p + l * KV_SZ) = (u32x4){0x3f803f80u, 0x3f803f80u, 0x3f803f80u, 0x3f803f80u};
#pragma unroll
        for (int i = 1; i < 4; ++i) *(u32x4*)(p + l * KV_SZ + 16 * i) = (u32x4){0u, 0u, 0u, 0u};
      }
    }
  }
  int aoff[2], boff[RAW ? NT : 1];
  const int pr = 8 * (fg >> 1) + 4 * (fg & 1) + (fr >> 2);
#pragma unroll
  for (int ks = 0; ks < 2; ++ks)
    aoff[ks] = RAW ? (wm * 64 + fr) * 128 + (((ks * 4 + fg) ^ (fr >> 1)) * 16) : (wm * 8 + ks) * 1024 + lane * 16;
  if (RAW) {
#pragma unroll
    for (int j = 0; j < NT; ++j) boff[j] = ABYTES + pr * RS + ((wn * NT + j) ^ (pr & 7)) * 32 + (lane & 3) * 8;
  } else boff[0] = ABYTES + wn * NT * 2048 + lane * 16;
  f32x4 srow[2], sgr[2], scol[2];
#pragma unroll
  for (int i = 0; i < 2; ++i) srow[i] = sgr[i] = scol[i] = (f32x4){0.f, 0.f, 0.f, 0.f};
  const bf16x8 ones = (bf16x8){0x3f80, 0x3f80, 0x3f80, 0x3f80, 0x3f80, 0x3f80, 0x3f80, 0x3f80};

  const bool swp = !(EPI == EPI_QKV && tn >= 10);
  unsigned so[2][2];
  if (EPI == EPI_QKV) {
    const bool isq = tn < DM / BN;
#pragma unroll
    for (int jq = 0; jq < 2; ++jq) {
      if (swp) {
        const int G = ((isq ? n0 : n0 - DM) >> 3) + (wn * NT + jq * 2 + (fg & 1)) * 2 + (fg >> 1), pair = G / 5, gi = G % 5;
        const int mo = (((gi + (gi > 2)) & 1) * 32 + fr) * 16;
        if (isq) {
          so[jq][0] = OQ + (pair * 64 + (m0 >> 5) + wm * 2) * 2560 + (gi == 2 ? 2048 + fr * 16 : (gi > 2) * 1024 + mo);
          so[jq][1] = so[jq][0] + 2560;
        } else {
          so[jq][0] = OKV + (pair * 16 + (m0 >> 7)) * KVP_CHUNK + (gi == 2 ? KVP_TAIL + (wm * 64 + fr) * 16 : (gi > 2 ? KVP_KO : KVP_KE) + wm * 2048 + mo);
          so[jq][1] = so[jq][0] + (gi == 2 ? 512 : 1024);
        }
      } else {
        const int cc = n0 - 2 * DM + (wn * NT + jq * 2 + (lane >> 5)) * 16 + fr, head = cc / DH, d = cc % DH;
        const int vr = (head & 1) ? (d < 4 ? d + 16 : d - 4) : d;
        so[jq][0] = OKV + ((head >> 1) * 16 + (m0 >> 7)) * KVP_CHUNK + (head & 1) * KVP_VO + wm * 4 * VFR + ((fg & 1) * 24 + vr) * 16;
        so[jq][1] = so[jq][0] + 2 * VFR;
      }
      asm volatile("" : "+v"(so[jq][0]), "+v"(so[jq][1]));
    }
  }
  if (!RAW) {
    if (wid == 1) await(ws, seam, lane < 16 ? tm * 16 + lane : -1);
    raw_barrier();
  }
  auto mainloop = [&](auto SW, auto WN, auto WM) {
    constexpr bool SWP = decltype(SW)::v;
    constexpr int CWN = decltype(WN)::v, CWM = decltype(WM)::v;
    bf16x8 a0[MT], a1[MT], b0[NT], b1[NT];
    auto rdB = [&](const char* base, int j, int ks) {
      if (RAW) {
        const char* p = base + boff[RAW ? j : 0] + ks * 32 * RS;
        bf16x4 lo = tr_read(p), hi = tr_read(p + 16 * RS);
        return __builtin_shufflevector(lo, hi, 0, 1, 2, 3, 4, 5, 6, 7);
      } else return *(const bf16x8*)(base + boff[0] + j * 2048 + ks * 1024);
    };
    auto step = [&](bf16x8(&ac)[MT], bf16x8(&bc)[NT], bf16x8(&an)[MT], bf16x8(&bn)[NT], const char* nb, int nks, auto RD) {
      constexpr bool rd = decltype(RD)::v;
      constexpr int RPG = 3;
#pragma unroll
      for (int j = 0; j < NT; ++j) {
#pragma unroll
        for (int i = 0; i < MT; ++i) {
          acc[i][j] = SWP ? __builtin_amdgcn_mfma_f32_16x16x32_bf16(bc[j], ac[i], acc[i][j], 0, 0, 0)
                          : __builtin_amdgcn_mfma_f32_16x16x32_bf16(ac[i], bc[j], acc[i][j], 0, 0, 0);
          const int r = j * RPG + i;
          if (rd && i < RPG && r < MT + NT) {
            if (r == 0) bn[0] = rdB(nb, 0, nks);
            else if (r <= MT) an[r - 1] = *(const bf16x8*)(nb + aoff[nks] + (r - 1) * 2048);
            else bn[r - MT] = rdB(nb, r - MT, nks);
            SB();
          }
        }
        if (RAW) {
          if ((j >> 1) == CWM) scol[j & 1] = __builtin_amdgcn_mfma_f32_16x16x32_bf16(bc[j], ones, scol[j & 1], 0, 0, 0);
          else {
            srow[j & 1] = __builtin_amdgcn_mfma_f32_16x16x32_bf16(ones, ac[CWN * 2 + (j & 1)], srow[j & 1], 0, 0, 0);
            sgr[j & 1] = __builtin_amdgcn_mfma_f32_16x16x32_bf16(ac[CWN * 2 + (j & 1)], ac[CWN * 2 + (j & 1)], sgr[j & 1], 0, 0, 0);
          }
        }
        SB();
      }
    };
    STAMP(KID, 1);
    raw_barrier();
    STAMP(KID, 2); CSTAMP(KID, 2);
#pragma unroll
    for (int j = 0; j < NT; ++j) { b0[j] = rdB(smem, j, 0); if (j < MT) a0[j] = *(const bf16x8*)(smem + aoff[0] + j * 2048); }
    step(a0, b0, a1, b1, smem, 1, IC<1>{});
    int buf = 0;
#pragma unroll 1
    for (int kt = 1; kt < KT; ++kt) {
      if (++buf == NST) buf = 0;
      raw_barrier();
      step(a1, b1, a0, b0, smem + buf * STB, 0, IC<1>{});
      step(a0, b0, a1, b1, smem + buf * STB, 1, IC<1>{});
    }
    step(a1, b1, a0, b0, smem, 0, IC<0>{});
  };
  if (RAW) {
    if (swp) {
      if (wid == 0) mainloop(IC<1>{}, IC<0>{}, IC<0>{}); else if (wid == 1) mainloop(IC<1>{}, IC<1>{}, IC<0>{});
      else if (wid == 2) mainloop(IC<1>{}, IC<0>{}, IC<1>{}); else mainloop(IC<1>{}, IC<1>{}, IC<1>{});
    } else {
      if (wid == 0) mainloop(IC<0>{}, IC<0>{}, IC<0>{}); else if (wid == 1) mainloop(IC<0>{}, IC<1>{}, IC<0>{});
      else if (wid == 2) mainloop(IC<0>{}, IC<0>{}, IC<1>{}); else mainloop(IC<0>{}, IC<1>{}, IC<1>{});
    }
    STAMP(KID, 3); CSTAMP(KID, 3);
#pragma unroll
    for (int i = 0; i < 2; ++i) {
      const float sq = (fr & 2) ? ((fr & 1) ? sgr[i][3] : sgr[i][2]) : ((fr & 1) ? sgr[i][1] : sgr[i][0]);
      const float mean = srow[i][0] * (1.0f / KTOT);
      const float var = fmaxf(sq * (1.0f / KTOT) - mean * mean, 0.f);
      if (fg == (fr >> 2)) rowst[wm * 64 + (wn * 2 + i) * 16 + fr] = make_float2(mean, rsqrtf(var + LN_EPS));
      if (fr == 0) *(f32x4*)(colsum + (wn * NT + wm * 2 + i) * 16 + fg * 4) = scol[i];
    }
    raw_barrier();
  } else {
    if (swp) mainloop(IC<1>{}, IC<0>{}, IC<0>{}); else mainloop(IC<0>{}, IC<0>{}, IC<0>{});
    STAMP(KID, 3); CSTAMP(KID, 3);
    raw_barrier();
  }
  STAMP(KID, 4);

  if constexpr (EPI == EPI_GEGLU) {
    const int hbase = (tn >> 1) * 216 + (tn & 1) * 112;
    const int orow = m0 + wm * 64 + fg * 16 + fr;
    constexpr float C1 = -2.0f * 0.7978845608028654f * 1.4426950408889634f, C2 = C1 * 0.044715f;
    f32x2 nm[MT], k1[MT], k2[MT], iv[MT];
#pragma unroll
    for (int i = 0; i < MT; ++i) {
      const float2 ms = rowst[wm * 64 + i * 16 + fr];
      const float r2 = ms.y * ms.y, ir2 = __builtin_amdgcn_rcpf(r2);
      nm[i] = (f32x2){-ms.x, -ms.x}; k1[i] = (f32x2){C1 * ms.y, C1 * ms.y}; k2[i] = (f32x2){C2 * ms.y * r2, C2 * ms.y * r2}; iv[i] = (f32x2){ir2, ir2};
    }
    f32x2 cgs[NT], cus[NT];
#pragma unroll
    for (int j = 0; j < NT; ++j) { cgs[j] = *(const f32x2*)(colsum + (wn * NT + j) * 16 + fg * 2); cus[j] = *(const f32x2*)(colsum + (wn * NT + j) * 16 + fg * 2 + 8); }
    const unsigned ro = foff<27>(orow, 0);
#pragma unroll
    for (int j = 0; j < NT; ++j) {
      const f32x2 cg = cgs[j], cu = cus[j];
      unsigned d[MT];
#pragma unroll
      for (int i = 0; i < MT; ++i) {
        const f32x2 gg = __builtin_elementwise_fma(nm[i], cg, (f32x2){acc[i][j][0], acc[i][j][1]});
        const f32x2 uu = __builtin_elementwise_fma(nm[i], cu, (f32x2){acc[i][j][2], acc[i][j][3]});
        const f32x2 t = gg * __builtin_elementwise_fma(gg * gg, k2[i], k1[i]);
        const f32x2 e = (f32x2){__builtin_amdgcn_exp2f(t[0]), __builtin_amdgcn_exp2f(t[1])};
        const f32x2 q = __builtin_elementwise_fma(e, iv[i], iv[i]);
        const f32x2 h = (gg * uu) * (f32x2){__builtin_amdgcn_rcpf(q[0]), __builtin_amdgcn_rcpf(q[1])};
        d[i] = cvtpk(h[0], h[1]);
      }
      const u32x2 x = swap32(d[0], d[2]), y = swap32(d[1], d[3]);
      const u32x2 z = swap16(x[0], y[0]), w = swap16(x[1], y[1]);
      const int jb = wn * NT + j;
      if (jb < 13 || !(tn & 1)) stw<0>(out, ro + foff<27>(0, hbase + jb * 8), (u32x4){z[0], z[1], w[0], w[1]});
    }
  } else {
    if (swp) {
      const float sc = tn < DM / BN ? QSCALE : 1.0f;
      float2 ms[MT]; f32x4 cs[NT];
#pragma unroll
      for (int i = 0; i < MT; ++i) ms[i] = rowst[wm * 64 + i * 16 + fr];
#pragma unroll
      for (int j = 0; j < NT; ++j) cs[j] = *(const f32x4*)(colsum + (wn * NT + j) * 16 + fg * 4);
      static_for<8>([&](auto I) {
        constexpr int jq = decltype(I)::v >> 2, i = decltype(I)::v & 3;
        const float rs = ms[i].y * sc;
        const f32x4 o0 = rs * (acc[i][jq * 2] - ms[i].x * cs[jq * 2]), o1 = rs * (acc[i][jq * 2 + 1] - ms[i].x * cs[jq * 2 + 1]);
        const u32x2 x = swap16(cvtpk(o0[0], o0[1]), cvtpk(o1[0], o1[1])), y = swap16(cvtpk(o0[2], o0[3]), cvtpk(o1[2], o1[3]));
        stw<(i & 1) * 256>(ws, so[jq][i >> 1], (u32x4){x[0], y[0], x[1], y[1]});
      });
    } else {
      f32x4 ma[MT], mb[MT]; float cs[NT];
#pragma unroll
      for (int i = 0; i < MT; ++i) { ma[i] = *(const f32x4*)(rowst + wm * 64 + i * 16 + fg * 4); mb[i] = *(const f32x4*)(rowst + wm * 64 + i * 16 + fg * 4 + 2); }
#pragma unroll
      for (int j = 0; j < NT; ++j) cs[j] = colsum[(wn * NT + j) * 16 + fr];
      static_for<8>([&](auto I) {
        constexpr int jq = decltype(I)::v >> 2, i = decltype(I)::v & 3;
        const float c0 = cs[jq * 2], c1 = cs[jq * 2 + 1];
        const f32x4 &p0 = acc[i][jq * 2], &p1 = acc[i][jq * 2 + 1], &a = ma[i], &b = mb[i];
        const u32x2 x = swap32(cvtpk(a[1] * (p0[0] - a[0] * c0), a[3] * (p0[1] - a[2] * c0)), cvtpk(a[1] * (p1[0] - a[0] * c1), a[3] * (p1[1] - a[2] * c1)));
        const u32x2 y = swap32(cvtpk(b[1] * (p0[2] - b[0] * c0), b[3] * (p0[3] - b[2] * c0)), cvtpk(b[1] * (p1[2] - b[0] * c1), b[3] * (p1[3] - b[2] * c1)));
        stw<(i & 1) * VFR>(ws, so[jq][i >> 1], (u32x4){x[0], y[0], x[1], y[1]});
      });
    }
  }
  STAMP(KID, 5);
  if (!RAW) publish<4>(smem, ws, seam + 1, tm * 16 + tn, tid);
  STAMP(KID, 6);
}

constexpr int ATT_NBUF = 4, ATT_HALF = 12288, ATT_STB = 2 * ATT_HALF;
constexpr int ATT_PK = ATT_NBUF * ATT_STB, ATT_X = 45056;
struct MegaArgs { char* ws; const char* x; char* out; const u16* wqkv; const u16* wg; const u16* wu; const u16* wd; };

#define EX(x) __builtin_amdgcn_exp2f(x)
#define E4(P, I, S, J) P[I] = cvtpk(EX(S[J]), EX(S[J + 1])); P[I + 1] = cvtpk(EX(S[J + 2]), EX(S[J + 3]));
#define MF16(a, b, c) __builtin_amdgcn_mfma_f32_32x32x16_bf16(a, b, c, 0, 0, 0)
#define MF8(a, b, c) __builtin_amdgcn_mfma_f32_32x32x8bf16_1k(a, b, c, 0, 0, 0)
#define BC8(x) __builtin_bit_cast(bf16x8, x)

DI float rowmax16(const f32x16& s) {
  float m = s[0];
#pragma unroll
  for (int i = 1; i < 16; ++i) m = fmaxf(m, s[i]);
  return fmaxf(m, __shfl_xor(m, 32));
}
DI bool nonfinite12(const f32x16& o, int lane) {
  bool bad = false;
#pragma unroll
  for (int i = 0; i < 9; ++i) bad |= !(fabsf(o[i]) < 1.0e38f);
#pragma unroll
  for (int i = 9; i < 12; ++i) bad |= (lane < 32) && !(fabsf(o[i]) < 1.0e38f);
  return bad;
}

struct PackJob { const u16* src; int ld, kk0, kmax, s2; char* dst; float* cs; };
constexpr int NJOB = 223;
DI PackJob pack_job(const MegaArgs& a, int J, int wid, int lane) {
  const int prow = wid * 4 + (lane >> 4), c16 = lane & 15, ch = (c16 >> 1) ^ (prow & 7);
  const int k = 8 * (((prow >> 3) & 1) * 2 + ((prow >> 2) & 1)) + 4 * ((prow >> 4) & 1) + (prow & 3);
  const int fr = lane & 15, fg = lane >> 4;
  PackJob r;
  if (J >= NJOB) J -= NJOB;
  r.kmax = 19;
  if (J < 108) {
    const int cg = J >> 2, kq = J & 3, b = cg * 8 + wid, rr = b % 27, tn = (b / 27) * 2 + (rr >= 14), jb = rr - (rr >= 14) * 14;
    r.src = ((c16 & 1) ? a.wu : a.wg) + (size_t)k * DF + cg * 64 + ch * 8; r.ld = DF; r.kk0 = kq * 5; r.s2 = 28;
    r.dst = a.ws + WS_PGU + (size_t)(tn * 280 + jb * 2) * 1024 + (fg * 16 + ((fr & 7) >> 1) * 4 + (fr >> 3) * 2 + (fr & 1)) * 16;
    r.cs = (float*)(a.ws + WS_CGU) + (tn * 4 + kq) * 224 + jb * 16 + fg * 4;
  } else if (J < 168) {
    const int tn = (J - 108) >> 2, kq = (J - 108) & 3;
    r.src = a.wqkv + (size_t)k * (3 * DM) + tn * 128 + ch * 16 + (c16 & 1) * 8; r.ld = 3 * DM; r.kk0 = kq * 5; r.s2 = 16;
    r.dst = a.ws + WS_PQKV + (size_t)(tn * 160 + wid * 2) * 1024 + lane * 16;
    r.cs = (float*)(a.ws + WS_CQKV) + (tn * 4 + kq) * 128 + wid * 16 + fg * 4;
  } else {
    const int cg = (J - 168) / 11, kq = (J - 168) % 11, jb = cg * 8 + wid;
    r.src = a.wd + (size_t)k * DM + cg * 128 + ch * 16 + (c16 & 1) * 8; r.ld = DM; r.kk0 = kq * 5; r.s2 = 10;
    r.kmax = 53;
    r.dst = a.ws + WS_PWD + (size_t)((jb / 5) * 270 + (jb % 5) * 2) * 1024 + lane * 16;
    r.cs = (float*)(a.ws + WS_PFLG + 2048) + wid * 16 + fg * 4;
  }
  return r;
}

template <int OFF> DI bf16x8 lds16(unsigned a) { bf16x8 r; asm volatile("ds_read_b128 %0, %1 offset:%2" : "=v"(r) : "v"(a), "i"(OFF)); return r; }
template <int OFF> DI bf16x4 lds8(unsigned a) { bf16x4 r; asm volatile("ds_read_b64 %0, %1 offset:%2" : "=v"(r) : "v"(a), "i"(OFF)); return r; }
template <int OFF> DI bf16x4 ldstr(unsigned a) { bf16x4 r; asm volatile("ds_read_b64_tr_b16 %0, %1 offset:%2" : "=v"(r) : "v"(a), "i"(OFF)); return r; }
template <int OFF, bool SC> DI void gld(unsigned voff, const char* sb, unsigned m) {
  if (SC) asm volatile("s_mov_b32 m0, %2\n\ts_nop 0\n\tglobal_load_lds_dwordx4 %0, %1 offset:%3 sc1" ::"v"(voff), "s"(sb), "s"(m), "i"(OFF) : "memory", "m0");
  else asm volatile("s_mov_b32 m0, %2\n\ts_nop 0\n\tglobal_load_lds_dwordx4 %0, %1 offset:%3" ::"v"(voff), "s"(sb), "s"(m), "i"(OFF) : "memory", "m0");
}
DI u32x2 gload8sc(const void* g) {
  u32x2 r; asm volatile("global_load_dwordx2 %0, %1, off sc1" : "=v"(r) : "v"(g) : "memory"); return r;
}
DI void st8sc(void* p, u32x2 v) { asm volatile("global_store_dwordx2 %0, %1, off sc1" ::"v"(p), "v"(v) : "memory"); }
DI void st4m(void* p, unsigned v, unsigned m) {
  asm volatile("s_mov_b32 exec_lo, %2\n\ts_mov_b32 exec_hi, 0\n\tglobal_store_dword %0, %1, off sc1\n\ts_mov_b64 exec, -1" ::"v"(p), "v"(v), "s"(m) : "memory");
}
DI void st16m(void* p, f32x4 v) {
  asm volatile("s_mov_b32 exec_lo, 0x10001\n\ts_mov_b32 exec_hi, 0x10001\n\tglobal_store_dwordx4 %0, %1, off sc1\n\ts_mov_b64 exec, -1\n\ts_nop 0" ::"v"(p), "v"(v) : "memory");
}

template <bool PACK> DI void attn_phase(char* smem, const MegaArgs& a, char* ws, int bid, int tid) {
  constexpr int LAYER = PACK ? 0 : 1;
  constexpr bool SC = !PACK;
  const int lane = tid & 63, wid = __builtin_amdgcn_readfirstlane(tid >> 6);
  STAMP(KID, 0);
  const int xcd = bid & 7, idx = bid >> 3;
  const int head = xcd * 4 + (idx >> 3), qb = idx & 7, odd = head & 1;
  const int r32 = lane & 31, hh = lane >> 5, half = wid >> 2;
  const int q0 = qb * 256 + (wid & 3) * 64 + r32;
  const char* kvb = uni(ws + WS_KV + LAYER * KV_SZ + (size_t)(head >> 1) * 16 * KVP_CHUNK);
  const unsigned sm = (unsigned)(uintptr_t)smem;
  const unsigned voff = half * 8 * KVP_CHUNK + odd * KVP_TAIL + (wid & 3) * 3072 + lane * 16;
  const unsigned mb = sm + half * ATT_HALF + (wid & 3) * 3072;
  auto stage = [&](int s, int buf) {
    const char* gs = kvb + s * KVP_CHUNK; const unsigned gm = mb + buf * ATT_STB;
    gld<0, SC>(voff, gs, gm); gld<1024, SC>(voff, gs, gm); gld<2048, SC>(voff, gs, gm);
  };
  if (!PACK) {
    const int c = head * DH + (lane & 16 ? DH - 1 : 0);
    if (wid == 0) await(ws, 4, (lane & 15) * 16 + ((c + (lane & 32 ? 2 * DM : DM)) >> 7));
    if (wid == 1) await(ws, 4, (lane & 46) == 0 ? (qb * 2 + (lane & 1)) * 16 + (c >> 7) : -1);
    raw_barrier();
  }
  STAMP(KID, 1);
  u32x4 qr1[2]; u32x2 qr2[2];
  {
    const char* Q = ws + WS_Q + LAYER * Q_SZ + (size_t)((head >> 1) * 64 + (q0 >> 5)) * 2560;
#pragma unroll
    for (int i = 0; i < 2; ++i) {
      qr1[i] = gload16sc(Q + i * 2560 + odd * 1024 + lane * 16);
      qr2[i] = gload8sc(Q + i * 2560 + 2048 + r32 * 16 + odd * 8);
    }
  }
  stage(0, 0);
  if (!AST) { stage(1, 1); stage(2, 2); }
  int tmw, tnw; tile_of<CWO>(bid, tmw, tnw);
  const char* wb = uni(ws + WS_PWO + (size_t)tnw * 102400);
  const unsigned wv = lane * 16, wm1 = sm + (wid < 6 ? WO_BHI + wid * 10240 : (wid - 6) * 10240), wm2 = sm + 20480 + wid * 3072;
  auto wpre = [&](auto K) {
    constexpr int k = decltype(K)::v;
    if (k < 10) gld<(k & 3) * 1024, false>(wv, wb + wid * 10240 + (k >> 2) * 4096, wm1 + (k >> 2) * 4096);
    else gld<(k < 10 ? 0 : k - 10) * 1024, false>(wv, wb + 81920 + wid * 3072, wm2);
    SB();
  };
  const unsigned m_fl = 1u >> wid;
  PackJob pj; unsigned pp; bf16x4 plo, phi; f32x4 pcs = (f32x4){0.f, 0.f, 0.f, 0.f};
  if (PACK) {
    pj = pack_job(a, bid, wid, lane);
    const int pr = 8 * (lane >> 5) + 4 * ((lane >> 4) & 1) + ((lane & 15) >> 2);
    pp = sm + ATT_PK + pr * 256 + (wid ^ (pr & 7)) * 32 + (lane & 3) * 8;
  }
  unsigned fa[2][3];
  fa[0][0] = sm + half * ATT_HALF + (odd ? KVP_KO - KVP_TAIL : KVP_KE) + lane * 16;
  fa[0][1] = sm + half * ATT_HALF + (odd ? 0 : KVP_TAIL) + r32 * 16 + odd * 8;
  fa[0][2] = sm + half * ATT_HALF + (odd ? KVP_VO - KVP_TAIL : 0) + (hh * 24 + min(r32, 23)) * 16;
#pragma unroll
  for (int i = 0; i < 3; ++i) { fa[1][i] = fa[0][i] + 2 * ATT_STB; asm volatile("" : "+v"(fa[0][i]), "+v"(fa[1][i])); }
  f32x16 oa, ob, nma, nmb, sa, sb;
#pragma unroll
  for (int i = 0; i < 16; ++i) { oa[i] = 0.f; ob[i] = 0.f; }
  wait_vm<AST ? 0 : 6>();
  if (AST) { stage(1, 1); stage(2, 2); }
#pragma unroll
  for (int i = 0; i < 2; ++i) asm volatile("" : "+v"(qr1[i]), "+v"(qr2[i]));
  const bf16x8 qa1 = BC8(qr1[0]), qb1 = BC8(qr1[1]);
  const bf16x4 qa2 = __builtin_bit_cast(bf16x4, hh ? (u32x2){0u, 0u} : qr2[0]), qb2 = __builtin_bit_cast(bf16x4, hh ? (u32x2){0u, 0u} : qr2[1]);
  raw_barrier();
  bf16x8 kc1 = lds16<0>(fa[0][0]); bf16x4 kc2 = lds8<0>(fa[0][1]);
  bf16x8 kn1 = lds16<1024>(fa[0][0]); bf16x4 kn2 = lds8<512>(fa[0][1]);
  bf16x8 v0 = lds16<0>(fa[0][2]), v1 = lds16<VFR>(fa[0][2]), vp1 = qa1;
  if (PACK) {
#pragma unroll
    for (int b = 0; b < 5; ++b) glds16(pj.src + (size_t)min(pj.kk0 + b, pj.kmax) * 32 * pj.ld, smem + ATT_PK + b * 8192 + wid * 1024);
  }
  asm volatile("s_waitcnt lgkmcnt(0)" : "+v"(kc1), "+v"(kc2), "+v"(kn1), "+v"(kn2), "+v"(v0), "+v"(v1));
  {
    sa = MF16(kc1, qa1, oa); sb = MF16(kc1, qb1, ob);
    sa = MF8(kc2, qa2, sa); sb = MF8(kc2, qb2, sb);
    const float ma = rowmax16(sa), mb = rowmax16(sb);
#pragma unroll
    for (int i = 0; i < 16; ++i) { nma[i] = -ma; nmb[i] = -mb; sa[i] -= ma; }
  }
  u32x4 p3 = (u32x4){0u, 0u, 0u, 0u};
  STAMP(KID, 2); CSTAMP(KID, 2);
  static_for<32>([&](auto S) {
    constexpr int c = decltype(S)::v >> 2, t = decltype(S)::v & 3;
    constexpr int s0 = decltype(S)::v, s1 = s0 + 1, s2 = s1 + 1;
    constexpr int wk = s0 <= 25 ? (s0 - 23) * 3 + 1 : (s0 - 27) * 3 + 10;
    constexpr bool wp = (s0 >= 23 && s0 <= 25) || s0 == 27;
    constexpr int b1 = (s1 >> 2) & 3, b2 = (s2 >> 2) & 3;
    constexpr int pb = PACK ? (decltype(S)::v - 10) >> 1 : -1;
    constexpr bool pk = pb >= 0 && pb < 5;
    const char* gs = kvb + (c + 3) * KVP_CHUNK; const unsigned gm = mb + ((c + 3) & 3) * ATT_STB;
    u32x4 p0, p1, p2; bf16x8 kx1, vx0, vx1; bf16x4 kx2;
    SB();
    sb = MF16(kc1, qb1, nmb); SB();
    if (t == 3 && c < 5) { gld<1024, SC>(voff, gs, gm); SB(); }
    if (wp) wpre(IC<(wp ? wk : 0)>{});
    E4(p0, 0, sa, 0) SB();
    ob = MF16(vp1, BC8(p3), ob); SB();
    if (t == 3 && c < 5) { gld<2048, SC>(voff, gs, gm); SB(); }
    if (wp) wpre(IC<(wp ? wk + 1 : 0)>{});
    if (PACK && s0 == 20) { st16m(pj.cs, pcs); SB(); }
    E4(p0, 2, sa, 4) SB();
    sb = MF8(kc2, qb2, sb); SB();
    if (pk && (t & 1)) asm volatile("s_waitcnt lgkmcnt(0)" : "+v"(kn1), "+v"(kn2), "+v"(v0), "+v"(v1), "+v"(plo), "+v"(phi));
    else asm volatile("s_waitcnt lgkmcnt(0)" : "+v"(kn1), "+v"(kn2), "+v"(v0), "+v"(v1));
    SB();
    E4(p1, 0, sa, 8) SB();
    oa = MF16(v0, BC8(p0), oa); SB();
    if (t == 2) {
      constexpr int W[7] = {8, 8, 3, 5, 7, 7, 10};
      if (c < 7) wait_vm<PACK ? W[c < 7 ? c : 0] : (c < 6 ? 3 : 10)>();
      __builtin_amdgcn_s_barrier();
      if (c < 5) gld<0, SC>(voff, gs, gm);
      if (PACK && c == 6) st4m(ws + WS_PFLG + bid * 4, 1u, m_fl);
      SB();
    }
    if (pk && (t & 1)) {
      const bf16x8 f = __builtin_shufflevector(plo, phi, 0, 1, 2, 3, 4, 5, 6, 7);
      const int kk = min(pj.kk0 + pb, pj.kmax);
      st16<3>(pj.dst + ((kk >> 1) * pj.s2 + (kk & 1)) * 1024, __builtin_bit_cast(u32x4, f));
      pcs = __builtin_amdgcn_mfma_f32_16x16x32_bf16(f, (bf16x8){0x3f80, 0x3f80, 0x3f80, 0x3f80, 0x3f80, 0x3f80, 0x3f80, 0x3f80}, pcs, 0, 0, 0);
      SB();
    }
    E4(p1, 2, sa, 12) SB();
    if (s1 < 32) { sa = MF16(kn1, qa1, nma); SB(); }
    if (s2 < 32) { kx1 = lds16<(b2 & 1) * ATT_STB + (s2 & 3) * 1024>(fa[b2 >> 1][0]); SB(); }
    E4(p2, 0, sb, 0) SB();
    oa = MF16(v1, BC8(p1), oa); SB();
    if (s2 < 32) { kx2 = lds8<(b2 & 1) * ATT_STB + (s2 & 3) * 512>(fa[b2 >> 1][1]); SB(); }
    if (wp || s0 == 22) wpre(IC<(wp ? wk + 2 : 0)>{});
    if (pk && !(t & 1)) { plo = ldstr<(pk ? pb : 0) * 8192>(pp); phi = ldstr<(pk ? pb : 0) * 8192 + 4096>(pp); SB(); }
    E4(p2, 2, sb, 4) SB();
    if (s1 < 32) { sa = MF8(kn2, qa2, sa); SB(); }
    if (s1 < 32) { vx0 = lds16<(b1 & 1) * ATT_STB + (s1 & 3) * 2 * VFR>(fa[b1 >> 1][2]); SB(); }
    E4(p3, 0, sb, 8) SB();
    ob = MF16(v0, BC8(p2), ob); SB();
    if (s1 < 32) { vx1 = lds16<(b1 & 1) * ATT_STB + (s1 & 3) * 2 * VFR + VFR>(fa[b1 >> 1][2]); SB(); }
    E4(p3, 2, sb, 12) SB();
    vp1 = v1; kc1 = kn1; kc2 = kn2;
    if (s2 < 32) { kn1 = kx1; kn2 = kx2; }
    if (s1 < 32) { v0 = vx0; v1 = vx1; }
  });
  ob = MF16(vp1, BC8(p3), ob);
  float ma = -nma[0], mb2 = -nmb[0];
  STAMP(KID, 3); CSTAMP(KID, 3);
  float* xo = (float*)(smem + ATT_X) + (wid ^ 4) * 13 * 64 + lane;
  const float* xi = (const float*)(smem + ATT_X) + wid * 13 * 64 + lane;
  unsigned* badw = (unsigned*)(smem + L_SMALL);
  auto hand_over = [&]() {
#pragma unroll
    for (int i = 0; i < 12; ++i) xo[i * 64] = half ? oa[i] : ob[i];
    xo[12 * 64] = half ? ma : mb2;
  };
  hand_over();
  {
    const bool bad = __builtin_amdgcn_ballot_w64(nonfinite12(oa, lane) || nonfinite12(ob, lane)) != 0;
    if (lane == 0) badw[wid] = bad;
  }
  raw_barrier();
  const u32x4 bw = *(const u32x4*)badw | *(const u32x4*)(badw + 4);
  if (__builtin_amdgcn_readfirstlane(bw[0] | bw[1] | bw[2] | bw[3])) {
    wait_vm<0>();
    raw_barrier();
    stage(0, 2);
#pragma unroll
    for (int i = 0; i < 16; ++i) { oa[i] = 0.f; ob[i] = 0.f; }
    ma = -3.0e38f; mb2 = -3.0e38f;
#pragma unroll 1
    for (int c = 0; c < 8; ++c) {
      wait_vm<0>();
      raw_barrier();
      if (c + 1 < 8) stage(c + 1, 2 + ((c + 1) & 1));
      const unsigned bo = (c & 1) * ATT_STB;
#pragma unroll 1
      for (int t = 0; t < 4; ++t) {
        kc1 = lds16<0>(fa[1][0] + bo + t * 1024); kc2 = lds8<0>(fa[1][1] + bo + t * 512);
        v0 = lds16<0>(fa[1][2] + bo + t * 2 * VFR); v1 = lds16<VFR>(fa[1][2] + bo + t * 2 * VFR);
        asm volatile("s_waitcnt lgkmcnt(0)" : "+v"(kc1), "+v"(kc2), "+v"(v0), "+v"(v1));
        auto one = [&](const bf16x8& q1, const bf16x4& q2, f32x16& o, float& m) {
          f32x16 s;
#pragma unroll
          for (int i = 0; i < 16; ++i) s[i] = 0.f;
          s = MF16(kc1, q1, s); s = MF8(kc2, q2, s);
          const float mn = fmaxf(m, rowmax16(s)), f = EX(m - mn);
          m = mn;
          u32x4 p0, p1;
#pragma unroll
          for (int i = 0; i < 16; ++i) { s[i] -= mn; o[i] *= f; }
          E4(p0, 0, s, 0) E4(p0, 2, s, 4) E4(p1, 0, s, 8) E4(p1, 2, s, 12)
          o = MF16(v0, BC8(p0), o); o = MF16(v1, BC8(p1), o);
        };
        one(qa1, qa2, oa, ma); one(qb1, qb2, ob, mb2);
      }
    }
    raw_barrier();
    hand_over();
    raw_barrier();
  }
  STAMP(KID, 4);
  {
    const float m1 = half ? mb2 : ma, m2 = xi[12 * 64], M = fmaxf(m1, m2);
    const float f1 = EX(m1 - M), f2 = EX(m2 - M);
    float o[12];
#pragma unroll
    for (int i = 0; i < 12; ++i) o[i] = (half ? ob[i] : oa[i]) * f1 + xi[i * 64] * f2;
    const float l = __uint_as_float(swap32(__float_as_uint(o[8]), __float_as_uint(o[8]))[1]);
    const float inv = 1.0f / l;
    const int q = q0 + half * 32, c0 = head * DH;
    char* O = ws + WS_O;
    const u32x2 x = swap32(cvtpk(o[0] * inv, o[1] * inv), cvtpk(o[4] * inv, o[5] * inv));
    const u32x2 y = swap32(cvtpk(o[2] * inv, o[3] * inv), cvtpk(o[6] * inv, o[7] * inv));
    st16sc(O + foff<10>(q, c0 + odd * 4 + hh * 8), (u32x4){x[0], y[0], x[1], y[1]});
    if (hh == 0) st8sc(O + foff<10>(q, c0 + (odd ? 0 : 16)), (u32x2){cvtpk(o[8] * inv, o[9] * inv), cvtpk(o[10] * inv, o[11] * inv)});
  }
  STAMP(KID, 5);
  publish<8>(smem, ws, LAYER * 5, qb * 32 + head, tid);
  STAMP(KID, 6);
}

__global__ __launch_bounds__(512) void mega_kernel(MegaArgs g) {
  __shared__ __attribute__((aligned(16))) char smem[L_TOTAL];
  KLABEL("kbeg_", 1);
  const int tid = threadIdx.x, bid = blockIdx.x;
  char* ws = g.ws;
  if (tid == 0) *(unsigned*)(smem + L_CNT) = 0u;
  attn_phase<true>(smem, g, ws, bid, tid);
#pragma unroll 1
  for (int layer = 0;; ++layer) {
    int tmq, tnq; tile_of<CQ1>(opaque(bid), tmq, tnq);
    const bool hasq = layer == 0 && tnq < 15;
    const unsigned xmid = layer ? WS_X3 : WS_X1;
    const int K0 = layer * 5;
    if (__builtin_amdgcn_readfirstlane(tid) >= 256) {
      if (layer == 0) {
        const char* p = ws + WS_PFLG + (tid & 63) * 16;
        for (int s = 0; s < (1 << 18); ++s) {
          u32x4 v = gload16sc(p);
          wait_vm<0>();
          asm volatile("" : "+v"(v));
          if (__builtin_amdgcn_ballot_w64((v[0] & v[1] & v[2] & v[3]) != 1u) == 0) break;
          __builtin_amdgcn_s_sleep(4);
        }
      }
      char* w;
      w = opaque(ws); loader_wo(smem, w + WS_O, opaque(bid), opaque_v(tid));
      w = opaque(ws); loader_phase<CGU, B0M>(smem, w + xmid, w + WS_PGU, w, WS_STAT, opaque(bid), opaque_v(tid));
      w = opaque(ws); loader_phase<CDN, 1>(smem, w + WS_H, w + WS_PWD, w, WS_STAT, opaque(bid), opaque_v(tid));
      w = opaque(ws);
      if (hasq) loader_phase<CQ1, B0M>(smem, w + WS_X2, w + WS_PQKV, w, WS_STAT2, opaque(bid), opaque_v(tid));
      else if (B0M == 2 && layer == 0) raw_barrier();
    } else {
      char* w;
      STAMP(K0 + 2, 0);
      w = opaque(ws); compute_res<CWO, 0>(smem, layer ? w + WS_X2 : g.x, layer == 0, w + xmid, false, w, WS_STAT, K0, opaque(bid), opaque_v(tid), K0 + 2);
      STAMP(K0 + 3, 0);
      w = opaque(ws); compute_ln<CGU>(smem, w + WS_H, w, K0 + 1, opaque(bid), opaque_v(tid), K0 + 3);
      STAMP(K0 + 4, 0);
      w = opaque(ws); compute_res<CDN, 1>(smem, nullptr, false, layer ? g.out : w + WS_X2, layer == 1, w, WS_STAT2, K0 + 2, opaque(bid), opaque_v(tid), K0 + 4);
      if (hasq) {
        STAMP(5, 0);
        w = opaque(ws); compute_ln<CQ1>(smem, nullptr, w, 3, opaque(bid), opaque_v(tid), 5);
      }
    }
    if (layer) break;
    attn_phase<false>(smem, g, opaque(ws), opaque(bid), opaque_v(tid));
  }
}

struct Q0Args { const char* x; const char* wqkv; const u16* wo; char* ws; const char* wg; const char* wu; const char* wd; const char* out; };
__global__ __launch_bounds__(512) void qkv0_kernel(Q0Args g) {
  __shared__ __attribute__((aligned(16))) char smem[L_TOTAL];
  KLABEL("kbeg_", 0);
  const int tid = threadIdx.x, bid = blockIdx.x;
  int tm0, tn0; tile_of<CQ0>(bid, tm0, tn0);
  if (tn0 == 15) {
    {
      const int lane = tid & 63, part = __builtin_amdgcn_readfirstlane(tid >> 6) * 2 + ((bid >> 3) & 1);
      const char* dummy = smem + L_SMALL + 2048;
      if (TC & 2) touch_code<0, 2>(dummy, part, lane);
      if (TC & 4) {
        const unsigned o = (part * 64 + lane) << 16;
        if (o < WS_END) gtouch(g.ws + o, dummy);
        const char* q = part & 2 ? (part & 1 ? g.out : g.wd) : (part & 1 ? g.wu : g.wg);
        if (part >= 12 && lane * 65536 < DM * DF * 2) gtouch(q + lane * 65536, dummy);
      }
    }
    if (tid < 128) *(unsigned*)(g.ws + WS_FLG + (tm0 * 128 + tid) * 128) = 0u;
    else if (tid < 144) *(unsigned*)(g.ws + WS_PFLG + (tm0 * 16 + tid - 128) * 4) = 0u;
    const int lane = tid & 63, wid = __builtin_amdgcn_readfirstlane(tid >> 6);
    const int prow = wid * 4 + (lane >> 4), c16 = lane & 15, ch = (c16 >> 1) ^ (prow & 7);
    const int k = 8 * (((prow >> 3) & 1) * 2 + ((prow >> 2) & 1)) + 4 * ((prow >> 4) & 1) + (prow & 3);
    const int pr = 8 * (lane >> 5) + 4 * ((lane >> 4) & 1) + ((lane & 15) >> 2);
#pragma unroll
    for (int b = 0; b < 7; ++b) {
      const int id = min(tm0 * 7 + b, 99), cg = id / 20, kk = id % 20;
      glds16(g.wo + (size_t)(kk * 32 + k) * DM + cg * 128 + ch * 16 + (c16 & 1) * 8, smem + b * 8192 + wid * 1024);
    }
    wait_vm<0>();
    raw_barrier();
#pragma unroll
    for (int b = 0; b < 7; ++b) {
      const int id = min(tm0 * 7 + b, 99), cg = id / 20, kk = id % 20, jb = cg * 8 + wid;
      const char* p = smem + b * 8192 + pr * 256 + (wid ^ (pr & 7)) * 32 + (lane & 3) * 8;
      const bf16x4 lo = tr_read(p), hi = tr_read(p + 4096);
      *(bf16x8*)(g.ws + WS_PWO + (size_t)((jb / 5) * 100 + (jb % 5) * 2 + (kk >> 1) * 10 + (kk & 1)) * 1024 + lane * 16) = __builtin_shufflevector(lo, hi, 0, 1, 2, 3, 4, 5, 6, 7);
    }
    if (TC & 1) touch_code<1, 0>(smem + L_SMALL + 2048, wid * 2 + ((bid >> 3) & 1), lane);
    wait_vm<0>();
    KLABEL("kbeg_", 2);
    return;
  }
  if (tid < 256) STAMP(KID, 0);
  if (tid >= 256) {
    loader_phase<CQ0, 0>(smem, g.x, g.wqkv, g.ws, WS_STAT, bid, tid);
    return;
  }
  compute_ln<CQ0>(smem, nullptr, g.ws, 0, bid, tid, 0);
  STAMP(KID, 6);
}

extern "C" void kernel_launch(void* const* d_in, const int* in_sizes, int n_in, void* d_out, int out_size, void* d_ws,
                              size_t ws_size, hipStream_t stream) {
  char* ws = (char*)d_ws;
  Q0Args q0{(const char*)d_in[0], (const char*)d_in[1], (const u16*)d_in[2], ws, (const char*)d_in[4], (const char*)d_in[5], (const char*)d_in[6], (const char*)d_out};
  qkv0_kernel<<<256, 512, 0, stream>>>(q0);
  MegaArgs m{ws, (const char*)d_in[0], (char*)d_out, (const u16*)d_in[1], (const u16*)d_in[4], (const u16*)d_in[5], (const u16*)d_in[6]};
  mega_kernel<<<256, 512, 0, stream>>>(m);
}
```

```cpp
#include <hip/hip_runtime.h>
#include <hip/hip_bf16.h>
#include <stdint.h>

typedef __attribute__((ext_vector_type(8))) short bf16x8;
typedef __attribute__((ext_vector_type(4))) short bf16x4;
typedef __attribute__((ext_vector_type(4))) float f32x4;
typedef __attribute__((ext_vector_type(16))) float f32x16;
typedef __attribute__((ext_vector_type(4))) unsigned u32x4;
typedef __attribute__((ext_vector_type(2))) unsigned u32x2;
typedef unsigned short u16;

#define DI __device__ __forceinline__

constexpr int SEQ = 2048, DM = 640, NH = 32, DH = 20, DF = 1728;
constexpr float LN_EPS = 1e-5f;
constexpr float QSCALE = 0.22360679774997896f * 1.4426950408889634f;

constexpr size_t WS_X1   = 0;
constexpr size_t WS_X2   = WS_X1 + (size_t)SEQ * DM * 2;
constexpr size_t WS_X3   = WS_X2 + (size_t)SEQ * DM * 2;
constexpr size_t WS_Q    = WS_X3 + (size_t)SEQ * DM * 2;
constexpr size_t Q_SZ    = (size_t)SEQ * DM * 2;
constexpr size_t WS_O    = WS_Q + 2 * Q_SZ;
constexpr size_t WS_H    = WS_O + (size_t)SEQ * DM * 2;
constexpr int    KVP_CHUNK = 22528, KVP_KE = 6144, KVP_TAIL = 10240, KVP_KO = 12288, KVP_VO = 16384, VFR = 768;
constexpr size_t WS_KV   = WS_H + (size_t)SEQ * DF * 2;
constexpr size_t KV_SZ   = (size_t)16 * 16 * KVP_CHUNK;
constexpr size_t WS_PWO  = WS_KV + 2 * KV_SZ;
constexpr size_t WS_PWD  = WS_PWO + (size_t)DM * DM * 2 + 4096;
constexpr size_t WS_PGU  = WS_PWD + (size_t)DF * DM * 2;
constexpr size_t WS_PQKV = WS_PGU + (size_t)16 * 10 * 28 * 1024;
constexpr size_t WS_CGU  = WS_PQKV + (size_t)DM * 3 * DM * 2;
constexpr size_t WS_CQKV = WS_CGU + (size_t)16 * 4 * 224 * 4;
constexpr size_t WS_STAT = WS_CQKV + (size_t)15 * 4 * 128 * 4;
constexpr size_t WS_STAT2 = WS_STAT + (size_t)SEQ * 64;
constexpr size_t WS_FLG  = WS_STAT2 + (size_t)SEQ * 64;
constexpr size_t WS_PFLG = WS_FLG + 8 * 256 * 128;
constexpr size_t WS_END  = WS_PFLG + 4096;

typedef __attribute__((ext_vector_type(2))) __bf16 bf16p;
typedef __attribute__((ext_vector_type(2))) float f32x2;
DI unsigned cvtpk(float lo, float hi) { return __builtin_bit_cast(unsigned, __builtin_convertvector((f32x2){lo, hi}, bf16p)); }
DI float dot2ones(unsigned a, float s) {
  return __builtin_amdgcn_fdot2_f32_bf16(__builtin_bit_cast(bf16p, a), __builtin_bit_cast(bf16p, 0x3f803f80u), s, false);
}
DI float bflo(unsigned v) { return __uint_as_float(v << 16); }
DI float bfhi(unsigned v) { return __uint_as_float(v & 0xffff0000u); }
#define KIDARG(k)
#define STAMP(kid, i)
#define CSTAMP(kid, i)
template <int N> DI void wait_vm() { asm volatile("s_waitcnt vmcnt(%0)" ::"i"(N) : "memory"); }
DI void raw_barrier() {
  asm volatile("" ::: "memory");
  __builtin_amdgcn_s_waitcnt(0xC07F);
  __builtin_amdgcn_s_barrier();
  asm volatile("" ::: "memory");
}
DI u32x2 swap16(unsigned a, unsigned b) { return __builtin_amdgcn_permlane16_swap(a, b, false, false); }
DI u32x2 swap32(unsigned a, unsigned b) { return __builtin_amdgcn_permlane32_swap(a, b, false, false); }
DI bf16x4 tr_read(const char* p) {
  typedef short v4i16_t __attribute__((ext_vector_type(4)));
  return __builtin_bit_cast(bf16x4, __builtin_amdgcn_ds_read_tr16_b64_v4i16((__attribute__((address_space(3))) v4i16_t*)(p)));
}
template <bool SC = false> DI void glds16(const void* g, const char* l) {
  unsigned sv; const unsigned lb = __builtin_amdgcn_readfirstlane((unsigned)(uintptr_t)l);
  if (SC) asm volatile("s_mov_b32 %0, m0\n\ts_mov_b32 m0, %2\n\ts_nop 0\n\tglobal_load_lds_dwordx4 %1, off sc1\n\ts_mov_b32 m0, %0"
               : "=&s"(sv) : "v"(g), "s"(lb) : "memory");
  else asm volatile("s_mov_b32 %0, m0\n\ts_mov_b32 m0, %2\n\ts_nop 0\n\tglobal_load_lds_dwordx4 %1, off\n\ts_mov_b32 m0, %0"
               : "=&s"(sv) : "v"(g), "s"(lb) : "memory");
}
DI void gtouch(const void* g, const char* l) {
  unsigned sv; const unsigned lb = __builtin_amdgcn_readfirstlane((unsigned)(uintptr_t)l);
  asm volatile("s_mov_b32 %0, m0\n\ts_mov_b32 m0, %2\n\ts_nop 0\n\tglobal_load_lds_dword %1, off\n\ts_mov_b32 m0, %0"
               : "=&s"(sv) : "v"(g), "s"(lb) : "memory");
}
#define KLABEL(pfx, id) asm volatile(pfx "%c0:" ::"i"(id))
template <int ID> DI unsigned long long kaddr() {
  unsigned long long a;
  asm volatile("s_getpc_b64 s[20:21]\n\ts_add_u32 s20, s20, kbeg_%c1@rel32@lo+4\n\ts_addc_u32 s21, s21, kbeg_%c1@rel32@hi+12\n\ts_mov_b64 %0, s[20:21]" : "=s"(a) : "i"(ID) : "s20", "s21", "scc");
  return a;
}
template <int A, int B> DI void touch_code(const char* dummy, int part, int lane) {
  const unsigned long long lo = kaddr<A>() & ~255ull, hi = kaddr<B>();
  const unsigned o = part * 8192 + lane * 128;
  if (hi > lo && o < (unsigned)(hi - lo)) gtouch((const char*)lo + o, dummy);
}
#define SLP 1
#define MAPR 8
#define MAPL 8
#define MAPQ 2
#define MAP0 2
#define AST 1
#define TC 7
DI u32x2 gload8(const void* g) {
  u32x2 r; asm volatile("global_load_dwordx2 %0, %1, off" : "=v"(r) : "v"(g) : "memory"); return r;
}
DI unsigned gload4(const void* g) {
  unsigned r; asm volatile("global_load_dword %0, %1, off" : "=v"(r) : "v"(g) : "memory"); return r;
}
DI u32x4 gload16(const void* g) {
  u32x4 r; asm volatile("global_load_dwordx4 %0, %1, off" : "=v"(r) : "v"(g) : "memory"); return r;
}
DI u32x4 gload16sc(const void* g) {
  u32x4 r; asm volatile("global_load_dwordx4 %0, %1, off sc1" : "=v"(r) : "v"(g) : "memory"); return r;
}
DI void st4sc(void* p, float v) { asm volatile("global_store_dword %0, %1, off sc1" ::"v"(p), "v"(v) : "memory"); }
DI void st16sc(void* p, u32x4 v) { asm volatile("global_store_dwordx4 %0, %1, off sc1\n\ts_nop 1" ::"v"(p), "v"(v)); }
#define ST_MODE 14
template <int CLS> DI void st16(void* p, u32x4 v) {
  if ((ST_MODE >> CLS) & 1) asm volatile("global_store_dwordx4 %0, %1, off sc1\n\ts_nop 1" ::"v"(p), "v"(v));
  else *(u32x4*)p = v;
}
template <int IMM, bool WT = true> DI void stw(const void* base, unsigned off, u32x4 v) {
  if (WT) asm volatile("global_store_dwordx4 %0, %1, %2 offset:%3 sc1\n\ts_nop 1" ::"v"(off), "v"(v), "s"(base), "i"(IMM));
  else asm volatile("global_store_dwordx4 %0, %1, %2 offset:%3\n\ts_nop 1" ::"v"(off), "v"(v), "s"(base), "i"(IMM));
}
template <int CLS> DI void st8(void* p, u32x2 v) {
  if ((ST_MODE >> CLS) & 1) asm volatile("global_store_dwordx2 %0, %1, off sc1" ::"v"(p), "v"(v) : "memory");
  else *(u32x2*)p = v;
}
template <int I> struct IC { static constexpr int v = I; };
template <int L, int NMAX> DI void wait_stages(int n) {
  if constexpr (NMAX == 0) wait_vm<0>();
  else { if (n >= NMAX) wait_vm<NMAX * L>(); else wait_stages<L, NMAX - 1>(n); }
}
template <int N, int I = 0, class F> DI void static_for(F&& f) {
  if constexpr (I < N) { f(IC<I>{}); static_for<N, I + 1>(f); }
}
#define SB() __builtin_amdgcn_sched_barrier(0)
template <int NS> DI unsigned foff(int row, int col) {
  return (unsigned)((((row >> 6) * NS + (col >> 6)) * 8 + ((row >> 4) & 3) * 2 + ((col >> 5) & 1)) * 1024 +
                    (((col >> 3) & 3) * 16 + (row & 15)) * 16 + (col & 7) * 2);
}

enum { EPI_QKV = 0, EPI_RES = 1, EPI_GEGLU = 2 };
constexpr int L_TOTAL = 163840, L_SMALL = L_TOTAL - 2304, L_CNT = L_SMALL + 1020 + 1024, L_XB = L_SMALL - 20480, L_XHI = L_XB + 8192;
constexpr int WO_BHI = 98304;
constexpr int wo_b(int s) { return s < 6 ? WO_BHI + s * 10240 : (s - 6) * 10240; }
DI int xb_off(int w, int j) { return j < 3 ? L_XHI + (w * 3 + j) * 1024 : L_XB + (w * 2 + j - 3) * 1024; }

template <int BM_, int BN_, int KTOT_, int NST_, int EPI_, bool RAW_, unsigned TA_, unsigned TB_> struct Cfg {
  static constexpr int BM = BM_, BN = BN_, KTOT = KTOT_, NST = NST_, EPI = EPI_;
  static constexpr bool RAW = RAW_, RES = (EPI_ == EPI_RES), LNP = !RES && !RAW_;
  static constexpr int MT = RES ? 2 : 4, NT = RES ? 5 : BN_ / 32, KT = KTOT_ / 64;
  static constexpr int RS = BN_ * 2;
  static constexpr DI int aslot(int b) { return (int)((TA_ >> (8 * b)) & 255u) << 10; }
  static constexpr DI int bslot(int b) { return (int)((TB_ >> (8 * b)) & 255u) << 10; }
};
constexpr unsigned tb4(unsigned a, unsigned b, unsigned c, unsigned d) { return a | b << 8 | c << 16 | d << 24; }
typedef Cfg<128, 128, 640, 4, EPI_QKV, true, tb4(0, 32, 64, 96), tb4(16, 48, 80, 112)> CQ0;
typedef Cfg<128, 128, 640, 4, EPI_QKV, false, tb4(0, 32, 136, 48), tb4(68, 16, 96, 116)> CQ1;
typedef Cfg<64, 80, 640, 8, EPI_RES, false, 0, 0> CWO;
typedef Cfg<128, 224, 640, 3, EPI_GEGLU, false, tb4(0, 16, 124, 0), tb4(40, 68, 96, 0)> CGU;
typedef Cfg<64, 80, 1728, 8, EPI_RES, false, 0, 0> CDN;
constexpr int dn_b(int s) {
  constexpr int S[11] = {69632, 79872, 16384, 26624, 98304, 108544, 118784, 129024, 40960, 51200, 0};
  return s < 24 ? S[s & 7] : S[s - 16];
}

template <class C> DI void tile_of(int bid, int& tm, int& tn) {
  const int xcd = bid & 7, idx = bid >> 3;
  constexpr int R = C::RES ? MAPR : (C::EPI == EPI_QKV ? (C::RAW ? MAP0 : MAPQ) : MAPL), NC = (C::RES ? 8 : 16) / (32 / R);
  tm = (xcd / NC) * R + idx % R; tn = (idx / R) * NC + xcd % NC;
}
DI const char* uni(const char* p) {
  const unsigned long long v = (unsigned long long)p;
  return (const char*)(((unsigned long long)(unsigned)__builtin_amdgcn_readfirstlane((unsigned)(v >> 32)) << 32) | (unsigned)__builtin_amdgcn_readfirstlane((unsigned)v));
}
DI char* opaque(char* p) { asm volatile("" : "+s"(p)); return p; }
DI int opaque(int x) { asm volatile("" : "+s"(x)); return x; }
DI int opaque_v(int x) { asm volatile("" : "+v"(x)); return x; }

template <int OFF, bool SC> DI void gld(unsigned voff, const char* sb, unsigned m) {
  if (SC) asm volatile("s_mov_b32 m0, %2\n\ts_nop 0\n\tglobal_load_lds_dwordx4 %0, %1 offset:%3 sc1" ::"v"(voff), "s"(sb), "s"(m), "i"(OFF) : "memory", "m0");
  else asm volatile("s_mov_b32 m0, %2\n\ts_nop 0\n\tglobal_load_lds_dwordx4 %0, %1 offset:%3" ::"v"(voff), "s"(sb), "s"(m), "i"(OFF) : "memory", "m0");
}
template <int OFF> DI void gldh(unsigned voff, const char* sb, unsigned m) {
  asm volatile("s_mov_b32 m0, %2\n\ts_mov_b32 exec_hi, 0\n\tglobal_load_lds_dwordx4 %0, %1 offset:%3\n\ts_mov_b32 exec_hi, -1" ::"v"(voff), "s"(sb), "s"(m), "i"(OFF) : "memory", "m0");
}
template <int N, bool SC> DI void gldn(unsigned voff, const char* sb, unsigned m) {
  static_for<N>([&](auto I) { gld<decltype(I)::v * 1024, SC>(voff, sb, m); });
}
template <int OFF> DI u32x4 gload16s(unsigned voff, const char* sb) {
  u32x4 r; asm volatile("global_load_dwordx4 %0, %1, %2 offset:%3 sc1" : "=v"(r) : "v"(voff), "s"(sb), "i"(OFF) : "memory"); return r;
}

DI void loader_raw(char* smem, const char* A, const char* B, int bid, int tid) {
  typedef CQ0 C;
  constexpr int KT = C::KT, NST = C::NST, L = 8, LA = 4;
  const int lane = tid & 63, lw = __builtin_amdgcn_readfirstlane(tid >> 6) - 4;
  int tm, tn; tile_of<C>(bid, tm, tn);
  const int m0 = tm * C::BM, n0 = tn * C::BN;
  const char* src[L];
#pragma unroll
  for (int i = 0; i < L; ++i) {
    const int p = lw + 4 * i;
    if (i < LA) {
      const int row = p * 8 + (lane >> 3), c = (lane & 7) ^ ((row >> 1) & 7);
      src[i] = A + ((size_t)(m0 + row) * C::KTOT + c * 8) * 2;
    } else {
      const int u = (p - 16) * 64 + lane, prow = u >> 4, c16 = u & 15;
      const int ch = (c16 >> 1) ^ (prow & 7);
      const int k = (prow & 32) + 8 * (((prow >> 3) & 1) * 2 + ((prow >> 2) & 1)) + 4 * ((prow >> 4) & 1) + (prow & 3);
      src[i] = B + ((size_t)k * (3 * DM) + n0 + ch * 16 + (c16 & 1) * 8) * 2;
    }
  }
  auto issue = [&](int buf) {
    char* base = smem + buf * 32768 + lw * 1024;
#pragma unroll
    for (int i = 0; i < L; ++i) {
      glds16(src[i], base + i * 4096);
      src[i] += (i < LA) ? 128 : 64 * 3 * DM * 2;
    }
  };
  issue(0); issue(1);
  int nxt = 2, ibuf = 2;
  for (int i = 0; i < KT; ++i) {
    wait_stages<L, NST - 1>(nxt - i - 1);
    raw_barrier();
    const int lim = min(KT, i + NST);
#pragma unroll
    for (int r = 0; r < 2; ++r)
      if (nxt < lim) { issue(ibuf); ++nxt; if (++ibuf == NST) ibuf = 0; }
  }
  raw_barrier();
}

#define THN 4
#define SDL 1
#define THG 4
template <int N, int TH> DI void gldt(unsigned voff, const char* sb, unsigned m) {
  static_for<N>([&](auto I) { gld<decltype(I)::v * 1024, false>(voff, sb, m); if (TH) wait_vm<(TH ? TH - 1 : 0)>(); });
}
template <int TH = 0> DI void ld_gu_b(unsigned sm, const char* ws, int tn, int lw, unsigned voff, int s, int slot) {
  const char* p = ws + WS_PGU + (size_t)(tn * 10 + s) * 28672 + lw * 7168;
  const unsigned m = sm + CGU::bslot(slot) + lw * 7168;
  gldt<4, TH>(voff, p, m); gldt<3, TH>(voff, p + 4096, m + 4096);
}
template <int TH = 0> DI void ld_q_b(unsigned sm, const char* ws, int tn, int lw, unsigned voff, int s, int slot) {
  gldt<4, TH>(voff, ws + WS_PQKV + (size_t)(tn * 10 + s) * 16384 + lw * 4096, sm + CQ1::bslot(slot) + lw * 4096);
}
template <int TH = 0> DI void ld_dn_b(unsigned sm, const char* ws, int tn, int lw, unsigned voff, int s) {
  const char* p = ws + WS_PWD + (size_t)(tn * 27 + s) * 10240 + lw * 2560;
  const unsigned m = sm + dn_b(s) + lw * 2560;
  gldt<2, TH>(voff, p, m); gldh<2048>(voff, p, m);
}

DI void loader_wo(char* smem, const char* ws, int bid, int tid) {
  const int lw = __builtin_amdgcn_readfirstlane(tid >> 6) - 4;
  const unsigned voff = (tid & 63) * 16, sm = (unsigned)(uintptr_t)smem;
  int tm, tn; tile_of<CGU>(bid, tm, tn);
  raw_barrier();
  raw_barrier();
  ld_gu_b(sm, ws, tn, lw, voff, 0, 0);
  ld_gu_b(sm, ws, tn, lw, voff, 1, 1);
  raw_barrier();
  __builtin_amdgcn_s_sleep(SDL);
  ld_gu_b<THN>(sm, ws, tn, lw, voff, 2, 2);
}

template <class C> DI void loader_ln(char* smem, const char* A, const char* ws, size_t stat, int bid, int tid) {
  constexpr bool GU = C::EPI == EPI_GEGLU;
  constexpr int KT = C::KT, NST = C::NST, BN = C::BN;
  const int lw = __builtin_amdgcn_readfirstlane(tid >> 6) - 4;
  const unsigned voff = (tid & 63) * 16, sm = (unsigned)(uintptr_t)smem;
  int tm, tn; tile_of<C>(bid, tm, tn);
  int tmd, tnd; tile_of<CDN>(bid, tmd, tnd);
  const char* pa = A + (size_t)(tm * 2 + (lw >> 1)) * KT * 8192 + (lw & 1) * 4096;
  auto lda = [&](int s) { gldn<4, true>(voff, pa + s * 8192, sm + C::aslot(s % NST) + lw * 4096); };
  auto ldb = [&](int s) { if (GU) ld_gu_b(sm, ws, tn, lw, voff, s, s % NST); else ld_q_b(sm, ws, tn, lw, voff, s, s % NST); };
  const int t = tid - 256;
  unsigned c[4]; u32x4 st[4];
  raw_barrier();
#pragma unroll
  for (int s = 0; s < NST; ++s) lda(s);
  static_for<KT>([&](auto I) {
    constexpr int i = decltype(I)::v;
    constexpr int WG[10] = {8, 4, 11, 11, 11, 11, 11, 11, 19, 8}, WQ[10] = {12, 8, 12, 16, 16, 16, 16, 24, 16, 8};
    wait_vm<GU ? WG[i] : WQ[i]>();
    raw_barrier();
    if (i >= 1 && i <= KT - NST) { lda(i - 1 + NST); ldb(i - 1 + NST); }
    if (i == KT - NST) {
      const float* cp = (const float*)(ws + (GU ? WS_CGU : WS_CQKV)) + tn * 4 * BN + (t < BN ? t : 0);
      const char* sp = ws + stat + (size_t)(tm * 128 + (t & 127)) * 64;
#pragma unroll
      for (int k = 0; k < 4; ++k) c[k] = gload4(cp + k * BN);
#pragma unroll
      for (int k = 0; k < 4; ++k) st[k] = gload16sc(sp + k * 16);
    }
  });
  float2* rowst = (float2*)(smem + L_SMALL);
  float* colsum = (float*)(smem + L_SMALL + 1024);
  wait_vm<0>();
  float cs = 0.f;
#pragma unroll
  for (int k = 0; k < 4; ++k) { asm volatile("" : "+v"(c[k])); cs += __uint_as_float(c[k]); }
#pragma unroll
  for (int k = 0; k < 4; ++k) asm volatile("" : "+v"(st[k]));
  const f32x4 sv = (__builtin_bit_cast(f32x4, st[0]) + __builtin_bit_cast(f32x4, st[1])) + (__builtin_bit_cast(f32x4, st[2]) + __builtin_bit_cast(f32x4, st[3]));
  const float mean = (sv[0] + sv[2]) * (1.0f / C::KTOT);
  const float var = fmaxf((sv[1] + sv[3]) * (1.0f / C::KTOT) - mean * mean, 0.f);
  if (t < BN) colsum[t] = cs;
  if (t >= 128) rowst[t - 128] = make_float2(mean, rsqrtf(var + LN_EPS));
  raw_barrier();
  if (GU) {
#pragma unroll
    for (int k = 0; k < 8; ++k) ld_dn_b<THG>(sm, ws, tnd, lw, voff, k);
  }
}

DI void loader_dn(char* smem, const char* ws, bool hasq, int bid, int tid) {
  const int lw = __builtin_amdgcn_readfirstlane(tid >> 6) - 4;
  const unsigned voff = (tid & 63) * 16, sm = (unsigned)(uintptr_t)smem;
  int tm, tn; tile_of<CDN>(bid, tm, tn);
  int tmq, tnq; tile_of<CQ1>(bid, tmq, tnq);
  raw_barrier();
  static_for<27>([&](auto S) {
    constexpr int s = decltype(S)::v;
    wait_vm<s == 0 ? 21 : (s <= 20 ? 18 : 3 * (26 - s))>();
    raw_barrier();
    if (s >= 1 && s <= 19) ld_dn_b(sm, ws, tn, lw, voff, s + 7);
  });
  raw_barrier();
  if (hasq) {
    __builtin_amdgcn_s_sleep(SDL);
#pragma unroll
    for (int k = 0; k < 4; ++k) ld_q_b<THN>(sm, ws, tnq, lw, voff, k, k);
  }
}

template <int NW> DI void publish(char* smem, char* ws, int seam, int tile, int tid) {
  wait_vm<0>();
  if ((tid & 63) == 0) {
    const unsigned old = __hip_atomic_fetch_add((unsigned*)(smem + L_CNT), 8u / NW, __ATOMIC_RELAXED, __HIP_MEMORY_SCOPE_WORKGROUP);
    if (((old + 8u / NW) & 7) == 0) __hip_atomic_store((unsigned*)(ws + WS_FLG) + (seam * 256 + tile) * 32, 1u, __ATOMIC_RELAXED, __HIP_MEMORY_SCOPE_AGENT);
  }
}
DI void await(const char* ws, int seam, int tile) {
  const unsigned* p = (const unsigned*)(ws + WS_FLG) + (seam * 256 + tile) * 32;
  unsigned v = tile < 0 ? 1u : 0u;
  for (int s = 0; s < (1 << 18); ++s) {
    if (v != 1u) v = __hip_atomic_load(p, __ATOMIC_RELAXED, __HIP_MEMORY_SCOPE_AGENT);
    if (__builtin_amdgcn_ballot_w64(v != 1u) == 0) break;
    __builtin_amdgcn_s_sleep(SLP);
  }
}

template <class C, int MODE> DI void compute_res(char* smem, const char* A, const char* res, bool res_raw, char* out, bool out_raw, char* ws, size_t stat, int seam, int bid, int tid, int KID) {
  constexpr int KT = C::KT, NT = C::NT;
  const int lane = tid & 63, wid = __builtin_amdgcn_readfirstlane(tid >> 6), fr = lane & 15, fg = lane >> 4, wm = wid >> 1, wn = wid & 1;
  int tm, tn; tile_of<C>(bid, tm, tn);
  const int m0 = tm * C::BM, n0 = tn * C::BN;
  f32x4 acc[2][NT];
#pragma unroll
  for (int i = 0; i < 2; ++i)
#pragma unroll
    for (int j = 0; j < NT; ++j) acc[i][j] = (f32x4){0.f, 0.f, 0.f, 0.f};
  const int row = m0 + wid * 16 + fr;
  u32x2 resv[NT];
#pragma unroll
  for (int j = 0; j < NT; ++j) {
    const int col = n0 + j * 16 + fg * 4;
    if (MODE == 0) resv[j] = *(const u32x2*)(res + (res_raw ? (unsigned)(row * DM + col) * 2 : foff<10>(row, col)));
    else resv[j] = *(const u32x2*)(smem + L_XHI + wid * 3072 + j * 512 + lane * 8);
  }
  constexpr int D = MODE == 0 ? 10 : 8;
  const char* pa = A + (size_t)(tm * KT * 8 + wm * 4 + wn) * 1024;
  const unsigned voff = lane * 16;
  const char* pb = smem + wn * 1024 + lane * 16;
  u32x4 ar[D][2];
  bf16x8 b[2][NT];
  constexpr int ORD[10] = {4, 5, 0, 1, 2, 3, 6, 7, 8, 9};
  auto lda = [&](auto S) {
    constexpr int s = decltype(S)::v, st = MODE == 0 ? ORD[s % 10] : s;
    ar[s % D][0] = gload16s<0>(voff, pa + st * 8192); ar[s % D][1] = gload16s<2048>(voff, pa + st * 8192);
  };
  auto rdb = [&](auto S) {
    constexpr int s = decltype(S)::v, o = MODE == 0 ? wo_b(ORD[s % 10]) : dn_b(s);
#pragma unroll
    for (int j = 0; j < NT; ++j) b[s & 1][j] = *(const bf16x8*)(pb + o + j * 2048);
    SB();
  };
  auto mm = [&](auto S) {
    constexpr int s = decltype(S)::v, n = (s + D - 1 < KT ? s + D - 1 : KT - 1) - s;
    asm volatile("s_waitcnt vmcnt(%2)" : "+v"(ar[s % D][0]), "+v"(ar[s % D][1]) : "i"(2 * n) : "memory");
#pragma unroll
    for (int j = 0; j < NT; ++j)
#pragma unroll
      for (int i = 0; i < 2; ++i) acc[i][j] = __builtin_amdgcn_mfma_f32_16x16x32_bf16(b[s & 1][j], __builtin_bit_cast(bf16x8, ar[s % D][i]), acc[i][j], 0, 0, 0);
    SB();
  };
  if (wid == 1) await(ws, seam, MODE == 0 ? (lane < 32 ? (tm >> 2) * 32 + lane : -1) : (lane < 16 ? (tm >> 1) * 16 + lane : -1));
  raw_barrier();
  STAMP(KID, 1);
  static_for<D>(lda);
  if (MODE == 1) raw_barrier();
  STAMP(KID, 2); CSTAMP(KID, 2);
  rdb(IC<0>{});
  static_for<KT, 1>([&](auto S) {
    constexpr int s = decltype(S)::v;
    if (MODE == 1 || s == 2) raw_barrier();
    rdb(S);
    mm(IC<s - 1>{});
    if (s - 1 + D < KT) lda(IC<(s - 1 + D < KT ? s - 1 + D : 0)>{});
  });
  mm(IC<KT - 1>{});
  STAMP(KID, 3); CSTAMP(KID, 3);
#pragma unroll
  for (int j = 0; j < NT; ++j) *(f32x4*)(smem + xb_off(wid ^ 1, j) + lane * 16) = wn ? acc[0][j] : acc[1][j];
  raw_barrier();
  STAMP(KID, 4);
  float s = 0.f, q = 0.f;
  u32x2 v[NT];
#pragma unroll
  for (int j = 0; j < NT; ++j) {
    const f32x4 o = *(const f32x4*)(smem + xb_off(wid, j) + lane * 16) + (wn ? acc[1][j] : acc[0][j]);
    const u32x2 r = resv[j];
    v[j] = (u32x2){cvtpk(o[0] + bflo(r[0]), o[1] + bfhi(r[0])), cvtpk(o[2] + bflo(r[1]), o[3] + bfhi(r[1]))};
    const float e0 = bflo(v[j][0]), e1 = bfhi(v[j][0]), e2 = bflo(v[j][1]), e3 = bfhi(v[j][1]);
    s += (e0 + e1) + (e2 + e3);
    q += (e0 * e0 + e1 * e1) + (e2 * e2 + e3 * e3);
  }
#pragma unroll
  for (int jp = 0; jp < NT; jp += 2) {
    const int j1 = jp + 1 < NT ? jp + 1 : jp;
    const u32x2 x = swap16(v[jp][0], v[j1][0]), y = swap16(v[jp][1], v[j1][1]);
    const int col = n0 + (jp + (fg & 1)) * 16 + (fg >> 1) * 8;
    if (jp + 1 < NT || !(fg & 1))
      st16sc(out + (out_raw ? (unsigned)(row * DM + col) * 2 : foff<10>(row, col)), (u32x4){x[0], y[0], x[1], y[1]});
  }
  {
    const u32x2 t1 = swap16(__float_as_uint(s), __float_as_uint(q));
    const unsigned t2 = __float_as_uint(__uint_as_float(t1[0]) + __uint_as_float(t1[1]));
    const u32x2 t3 = swap32(t2, t2);
    if (lane < 32) st4sc(ws + stat + (size_t)(row * 8 + tn) * 8 + fg * 4, __uint_as_float(t3[0]) + __uint_as_float(t3[1]));
  }
  if (MODE == 0) {
#pragma unroll
    for (int j = 0; j < NT; ++j) *(u32x2*)(smem + L_XHI + wid * 3072 + j * 512 + lane * 8) = v[j];
  }
  STAMP(KID, 5);
  if (!out_raw) publish<4>(smem, ws, seam + 1, tm * 8 + tn, tid);
  STAMP(KID, 6);
}

template <class C> DI void compute_ln(char* smem, char* out, char* ws, int seam, int bid, int tid, int KID) {
  constexpr int KT = C::KT, NST = C::NST, NT = C::NT, MT = C::MT, RS = C::RS, BN = C::BN, EPI = C::EPI, KTOT = C::KTOT;
  constexpr bool RAW = C::RAW;
  constexpr unsigned OQ = (unsigned)(WS_Q + (RAW ? 0 : Q_SZ)), OKV = (unsigned)(WS_KV + (RAW ? 0 : KV_SZ));
  const int lane = tid & 63, wid = __builtin_amdgcn_readfirstlane(tid >> 6), fr = lane & 15, fg = lane >> 4, wm = wid >> 1, wn = wid & 1;
  int tm, tn; tile_of<C>(bid, tm, tn);
  const int m0 = tm * C::BM, n0 = tn * BN;
  float2* rowst = (float2*)(smem + L_SMALL);
  float* colsum = (float*)(smem + L_SMALL + 1024);
  const char* dummy = smem + L_SMALL + 2048;
  f32x4 acc[MT][NT];
#pragma unroll
  for (int i = 0; i < MT; ++i)
#pragma unroll
    for (int j = 0; j < NT; ++j) acc[i][j] = (f32x4){0.f, 0.f, 0.f, 0.f};
  if (RAW && tn >= 10) {
    const int vc0 = n0 - 2 * DM, head = (vc0 + DH - 1) / DH + (tid >> 4);
    if (head * DH < vc0 + BN) {
      const int tok = m0 + ((tid >> 1) & 7) * 16;
      char* p = ws + OKV + (size_t)((head >> 1) * 16 + (tok >> 7)) * KVP_CHUNK + (head & 1) * KVP_VO + ((tok >> 4) & 7) * VFR + ((tid & 1) * 24 + DH) * 16;
#pragma unroll
      for (int l = 0; l < 2; ++l) {
        *(u32x4*)(p + l * KV_SZ) = (u32x4){0x3f803f80u, 0x3f803f80u, 0x3f803f80u, 0x3f803f80u};
#pragma unroll
        for (int i = 1; i < 4; ++i) *(u32x4*)(p + l * KV_SZ + 16 * i) = (u32x4){0u, 0u, 0u, 0u};
      }
    }
  }
  int aoff[2], boff[RAW ? NT : 1];
  const int pr = 8 * (fg >> 1) + 4 * (fg & 1) + (fr >> 2);
#pragma unroll
  for (int ks = 0; ks < 2; ++ks)
    aoff[ks] = RAW ? (wm * 64 + fr) * 128 + (((ks * 4 + fg) ^ (fr >> 1)) * 16) : (wm * 8 + ks) * 1024 + lane * 16;
  if (RAW) {
#pragma unroll
    for (int j = 0; j < NT; ++j) boff[j] = pr * RS + ((wn * NT + j) ^ (pr & 7)) * 32 + (lane & 3) * 8;
  } else boff[0] = wn * NT * 2048 + lane * 16;
  f32x4 srow[2], sgr[2], scol[2];
#pragma unroll
  for (int i = 0; i < 2; ++i) srow[i] = sgr[i] = scol[i] = (f32x4){0.f, 0.f, 0.f, 0.f};
  const bf16x8 ones = (bf16x8){0x3f80, 0x3f80, 0x3f80, 0x3f80, 0x3f80, 0x3f80, 0x3f80, 0x3f80};

  const bool swp = !(EPI == EPI_QKV && tn >= 10);
  unsigned so[2][2];
  if (EPI == EPI_QKV) {
    const bool isq = tn < DM / BN;
#pragma unroll
    for (int jq = 0; jq < 2; ++jq) {
      if (swp) {
        const int G = ((isq ? n0 : n0 - DM) >> 3) + (wn * NT + jq * 2 + (fg & 1)) * 2 + (fg >> 1), pair = G / 5, gi = G % 5;
        const int mo = (((gi + (gi > 2)) & 1) * 32 + fr) * 16;
        if (isq) {
          so[jq][0] = OQ + (pair * 64 + (m0 >> 5) + wm * 2) * 2560 + (gi == 2 ? 2048 + fr * 16 : (gi > 2) * 1024 + mo);
          so[jq][1] = so[jq][0] + 2560;
        } else {
          so[jq][0] = OKV + (pair * 16 + (m0 >> 7)) * KVP_CHUNK + (gi == 2 ? KVP_TAIL + (wm * 64 + fr) * 16 : (gi > 2 ? KVP_KO : KVP_KE) + wm * 2048 + mo);
          so[jq][1] = so[jq][0] + (gi == 2 ? 512 : 1024);
        }
      } else {
        const int cc = n0 - 2 * DM + (wn * NT + jq * 2 + (lane >> 5)) * 16 + fr, head = cc / DH, d = cc % DH;
        const int vr = (head & 1) ? (d < 4 ? d + 16 : d - 4) : d;
        so[jq][0] = OKV + ((head >> 1) * 16 + (m0 >> 7)) * KVP_CHUNK + (head & 1) * KVP_VO + wm * 4 * VFR + ((fg & 1) * 24 + vr) * 16;
        so[jq][1] = so[jq][0] + 2 * VFR;
      }
      asm volatile("" : "+v"(so[jq][0]), "+v"(so[jq][1]));
    }
  }
  if (!RAW) {
    if (wid == 1) await(ws, seam, lane < 16 ? tm * 16 + lane : -1);
    raw_barrier();
  }
  auto mainloop = [&](auto SW, auto WN, auto WM) {
    constexpr bool SWP = decltype(SW)::v;
    constexpr int CWN = decltype(WN)::v, CWM = decltype(WM)::v;
    bf16x8 a0[MT], a1[MT], b0[NT], b1[NT];
    auto rdB = [&](const char* base, int j, int ks) {
      if (RAW) {
        const char* p = base + boff[RAW ? j : 0] + ks * 32 * RS;
        bf16x4 lo = tr_read(p), hi = tr_read(p + 16 * RS);
        return __builtin_shufflevector(lo, hi, 0, 1, 2, 3, 4, 5, 6, 7);
      } else return *(const bf16x8*)(base + boff[0] + j * 2048 + ks * 1024);
    };
    auto step = [&](bf16x8(&ac)[MT], bf16x8(&bc)[NT], bf16x8(&an)[MT], bf16x8(&bn)[NT], const char* na, const char* nb, int nks, auto RD) {
      constexpr bool rd = decltype(RD)::v;
      constexpr int RPG = 3;
#pragma unroll
      for (int j = 0; j < NT; ++j) {
#pragma unroll
        for (int i = 0; i < MT; ++i) {
          acc[i][j] = SWP ? __builtin_amdgcn_mfma_f32_16x16x32_bf16(bc[j], ac[i], acc[i][j], 0, 0, 0)
                          : __builtin_amdgcn_mfma_f32_16x16x32_bf16(ac[i], bc[j], acc[i][j], 0, 0, 0);
          const int r = j * RPG + i;
          if (rd && i < RPG && r < MT + NT) {
            if (r == 0) bn[0] = rdB(nb, 0, nks);
            else if (r <= MT) an[r - 1] = *(const bf16x8*)(na + aoff[nks] + (r - 1) * 2048);
            else bn[r - MT] = rdB(nb, r - MT, nks);
            SB();
          }
        }
        if (RAW) {
          if ((j >> 1) == CWM) scol[j & 1] = __builtin_amdgcn_mfma_f32_16x16x32_bf16(bc[j], ones, scol[j & 1], 0, 0, 0);
          else {
            srow[j & 1] = __builtin_amdgcn_mfma_f32_16x16x32_bf16(ones, ac[CWN * 2 + (j & 1)], srow[j & 1], 0, 0, 0);
            sgr[j & 1] = __builtin_amdgcn_mfma_f32_16x16x32_bf16(ac[CWN * 2 + (j & 1)], ac[CWN * 2 + (j & 1)], sgr[j & 1], 0, 0, 0);
          }
        }
        SB();
      }
    };
    STAMP(KID, 1);
    raw_barrier();
    STAMP(KID, 2); CSTAMP(KID, 2);
#pragma unroll
    for (int j = 0; j < NT; ++j) { b0[j] = rdB(smem + C::bslot(0), j, 0); if (j < MT) a0[j] = *(const bf16x8*)(smem + C::aslot(0) + aoff[0] + j * 2048); }
    step(a0, b0, a1, b1, smem + C::aslot(0), smem + C::bslot(0), 1, IC<1>{});
    int buf = 0;
#pragma unroll 1
    for (int kt = 1; kt < KT; ++kt) {
      if (++buf == NST) buf = 0;
      const char *na = smem + C::aslot(buf), *nb = smem + C::bslot(buf);
      raw_barrier();
      step(a1, b1, a0, b0, na, nb, 0, IC<1>{});
      step(a0, b0, a1, b1, na, nb, 1, IC<1>{});
    }
    step(a1, b1, a0, b0, smem, smem, 0, IC<0>{});
  };
  if (RAW) {
    if (swp) {
      if (wid == 0) mainloop(IC<1>{}, IC<0>{}, IC<0>{}); else if (wid == 1) mainloop(IC<1>{}, IC<1>{}, IC<0>{});
      else if (wid == 2) mainloop(IC<1>{}, IC<0>{}, IC<1>{}); else mainloop(IC<1>{}, IC<1>{}, IC<1>{});
    } else {
      if (wid == 0) mainloop(IC<0>{}, IC<0>{}, IC<0>{}); else if (wid == 1) mainloop(IC<0>{}, IC<1>{}, IC<0>{});
      else if (wid == 2) mainloop(IC<0>{}, IC<0>{}, IC<1>{}); else mainloop(IC<0>{}, IC<1>{}, IC<1>{});
    }
    STAMP(KID, 3); CSTAMP(KID, 3);
    wait_vm<0>();
#pragma unroll
    for (int i = 0; i < 2; ++i) {
      const float sq = (fr & 2) ? ((fr & 1) ? sgr[i][3] : sgr[i][2]) : ((fr & 1) ? sgr[i][1] : sgr[i][0]);
      const float mean = srow[i][0] * (1.0f / KTOT);
      const float var = fmaxf(sq * (1.0f / KTOT) - mean * mean, 0.f);
      if (fg == (fr >> 2)) rowst[wm * 64 + (wn * 2 + i) * 16 + fr] = make_float2(mean, rsqrtf(var + LN_EPS));
      if (fr == 0) *(f32x4*)(colsum + (wn * NT + wm * 2 + i) * 16 + fg * 4) = scol[i];
    }
    raw_barrier();
  } else {
    if (swp) mainloop(IC<1>{}, IC<0>{}, IC<0>{}); else mainloop(IC<0>{}, IC<0>{}, IC<0>{});
    STAMP(KID, 3); CSTAMP(KID, 3);
    raw_barrier();
  }
  STAMP(KID, 4);

  if constexpr (EPI == EPI_GEGLU) {
    const int hbase = (tn >> 1) * 216 + (tn & 1) * 112;
    const int orow = m0 + wm * 64 + fg * 16 + fr;
    constexpr float C1 = -2.0f * 0.7978845608028654f * 1.4426950408889634f, C2 = C1 * 0.044715f;
    f32x2 nm[MT], k1[MT], k2[MT], iv[MT];
#pragma unroll
    for (int i = 0; i < MT; ++i) {
      const float2 ms = rowst[wm * 64 + i * 16 + fr];
      const float r2 = ms.y * ms.y, ir2 = __builtin_amdgcn_rcpf(r2);
      nm[i] = (f32x2){-ms.x, -ms.x}; k1[i] = (f32x2){C1 * ms.y, C1 * ms.y}; k2[i] = (f32x2){C2 * ms.y * r2, C2 * ms.y * r2}; iv[i] = (f32x2){ir2, ir2};
    }
    f32x2 cgs[NT], cus[NT];
#pragma unroll
    for (int j = 0; j < NT; ++j) { cgs[j] = *(const f32x2*)(colsum + (wn * NT + j) * 16 + fg * 2); cus[j] = *(const f32x2*)(colsum + (wn * NT + j) * 16 + fg * 2 + 8); }
    const unsigned ro = foff<27>(orow, 0);
#pragma unroll
    for (int j = 0; j < NT; ++j) {
      const f32x2 cg = cgs[j], cu = cus[j];
      unsigned d[MT];
#pragma unroll
      for (int i = 0; i < MT; ++i) {
        const f32x2 gg = __builtin_elementwise_fma(nm[i], cg, (f32x2){acc[i][j][0], acc[i][j][1]});
        const f32x2 uu = __builtin_elementwise_fma(nm[i], cu, (f32x2){acc[i][j][2], acc[i][j][3]});
        const f32x2 t = gg * __builtin_elementwise_fma(gg * gg, k2[i], k1[i]);
        const f32x2 e = (f32x2){__builtin_amdgcn_exp2f(t[0]), __builtin_amdgcn_exp2f(t[1])};
        const f32x2 q = __builtin_elementwise_fma(e, iv[i], iv[i]);
        const f32x2 h = (gg * uu) * (f32x2){__builtin_amdgcn_rcpf(q[0]), __builtin_amdgcn_rcpf(q[1])};
        d[i] = cvtpk(h[0], h[1]);
      }
      const u32x2 x = swap32(d[0], d[2]), y = swap32(d[1], d[3]);
      const u32x2 z = swap16(x[0], y[0]), w = swap16(x[1], y[1]);
      const int jb = wn * NT + j;
      if (jb < 13 || !(tn & 1)) stw<0>(out, ro + foff<27>(0, hbase + jb * 8), (u32x4){z[0], z[1], w[0], w[1]});
    }
  } else {
    if (swp) {
      const float sc = tn < DM / BN ? QSCALE : 1.0f;
      float2 ms[MT]; f32x4 cs[NT];
#pragma unroll
      for (int i = 0; i < MT; ++i) ms[i] = rowst[wm * 64 + i * 16 + fr];
#pragma unroll
      for (int j = 0; j < NT; ++j) cs[j] = *(const f32x4*)(colsum + (wn * NT + j) * 16 + fg * 4);
      static_for<8>([&](auto I) {
        constexpr int jq = decltype(I)::v >> 2, i = decltype(I)::v & 3;
        const float rs = ms[i].y * sc;
        const f32x4 o0 = rs * (acc[i][jq * 2] - ms[i].x * cs[jq * 2]), o1 = rs * (acc[i][jq * 2 + 1] - ms[i].x * cs[jq * 2 + 1]);
        const u32x2 x = swap16(cvtpk(o0[0], o0[1]), cvtpk(o1[0], o1[1])), y = swap16(cvtpk(o0[2], o0[3]), cvtpk(o1[2], o1[3]));
        stw<(i & 1) * 256>(ws, so[jq][i >> 1], (u32x4){x[0], y[0], x[1], y[1]});
      });
    } else {
      f32x4 ma[MT], mb[MT]; float cs[NT];
#pragma unroll
      for (int i = 0; i < MT; ++i) { ma[i] = *(const f32x4*)(rowst + wm * 64 + i * 16 + fg * 4); mb[i] = *(const f32x4*)(rowst + wm * 64 + i * 16 + fg * 4 + 2); }
#pragma unroll
      for (int j = 0; j < NT; ++j) cs[j] = colsum[(wn * NT + j) * 16 + fr];
      static_for<8>([&](auto I) {
        constexpr int jq = decltype(I)::v >> 2, i = decltype(I)::v & 3;
        const float c0 = cs[jq * 2], c1 = cs[jq * 2 + 1];
        const f32x4 &p0 = acc[i][jq * 2], &p1 = acc[i][jq * 2 + 1], &a = ma[i], &b = mb[i];
        const u32x2 x = swap32(cvtpk(a[1] * (p0[0] - a[0] * c0), a[3] * (p0[1] - a[2] * c0)), cvtpk(a[1] * (p1[0] - a[0] * c1), a[3] * (p1[1] - a[2] * c1)));
        const u32x2 y = swap32(cvtpk(b[1] * (p0[2] - b[0] * c0), b[3] * (p0[3] - b[2] * c0)), cvtpk(b[1] * (p1[2] - b[0] * c1), b[3] * (p1[3] - b[2] * c1)));
        stw<(i & 1) * VFR>(ws, so[jq][i >> 1], (u32x4){x[0], y[0], x[1], y[1]});
      });
    }
  }
  STAMP(KID, 5);
  if (!RAW) publish<4>(smem, ws, seam + 1, tm * 16 + tn, tid);
  STAMP(KID, 6);
}

constexpr int ATT_NBUF = 4, ATT_HALF = 12288, ATT_STB = 2 * ATT_HALF;
constexpr int ATT_PK = ATT_NBUF * ATT_STB, ATT_X = 45056;
struct MegaArgs { char* ws; const char* x; char* out; const u16* wqkv; const u16* wg; const u16* wu; const u16* wd; };

#define EX(x) __builtin_amdgcn_exp2f(x)
#define E4(P, I, S, J) P[I] = cvtpk(EX(S[J]), EX(S[J + 1])); P[I + 1] = cvtpk(EX(S[J + 2]), EX(S[J + 3]));
#define MF16(a, b, c) __builtin_amdgcn_mfma_f32_32x32x16_bf16(a, b, c, 0, 0, 0)
#define MF8(a, b, c) __builtin_amdgcn_mfma_f32_32x32x8bf16_1k(a, b, c, 0, 0, 0)
#define BC8(x) __builtin_bit_cast(bf16x8, x)

DI float rowmax16(const f32x16& s) {
  float m = s[0];
#pragma unroll
  for (int i = 1; i < 16; ++i) m = fmaxf(m, s[i]);
  return fmaxf(m, __shfl_xor(m, 32));
}
DI bool nonfinite12(const f32x16& o, int lane) {
  bool bad = false;
#pragma unroll
  for (int i = 0; i < 9; ++i) bad |= !(fabsf(o[i]) < 1.0e38f);
#pragma unroll
  for (int i = 9; i < 12; ++i) bad |= (lane < 32) && !(fabsf(o[i]) < 1.0e38f);
  return bad;
}

struct PackJob { const u16* src; int ld, kk0, kmax, s2; char* dst; float* cs; };
constexpr int NJOB = 223;
DI PackJob pack_job(const MegaArgs& a, int J, int wid, int lane) {
  const int prow = wid * 4 + (lane >> 4), c16 = lane & 15, ch = (c16 >> 1) ^ (prow & 7);
  const int k = 8 * (((prow >> 3) & 1) * 2 + ((prow >> 2) & 1)) + 4 * ((prow >> 4) & 1) + (prow & 3);
  const int fr = lane & 15, fg = lane >> 4;
  PackJob r;
  if (J >= NJOB) J -= NJOB;
  r.kmax = 19;
  if (J < 108) {
    const int cg = J >> 2, kq = J & 3, b = cg * 8 + wid, rr = b % 27, tn = (b / 27) * 2 + (rr >= 14), jb = rr - (rr >= 14) * 14;
    r.src = ((c16 & 1) ? a.wu : a.wg) + (size_t)k * DF + cg * 64 + ch * 8; r.ld = DF; r.kk0 = kq * 5; r.s2 = 28;
    r.dst = a.ws + WS_PGU + (size_t)(tn * 280 + jb * 2) * 1024 + (fg * 16 + ((fr & 7) >> 1) * 4 + (fr >> 3) * 2 + (fr & 1)) * 16;
    r.cs = (float*)(a.ws + WS_CGU) + (tn * 4 + kq) * 224 + jb * 16 + fg * 4;
  } else if (J < 168) {
    const int tn = (J - 108) >> 2, kq = (J - 108) & 3;
    r.src = a.wqkv + (size_t)k * (3 * DM) + tn * 128 + ch * 16 + (c16 & 1) * 8; r.ld = 3 * DM; r.kk0 = kq * 5; r.s2 = 16;
    r.dst = a.ws + WS_PQKV + (size_t)(tn * 160 + wid * 2) * 1024 + lane * 16;
    r.cs = (float*)(a.ws + WS_CQKV) + (tn * 4 + kq) * 128 + wid * 16 + fg * 4;
  } else {
    const int cg = (J - 168) / 11, kq = (J - 168) % 11, jb = cg * 8 + wid;
    r.src = a.wd + (size_t)k * DM + cg * 128 + ch * 16 + (c16 & 1) * 8; r.ld = DM; r.kk0 = kq * 5; r.s2 = 10;
    r.kmax = 53;
    r.dst = a.ws + WS_PWD + (size_t)((jb / 5) * 270 + (jb % 5) * 2) * 1024 + lane * 16;
    r.cs = (float*)(a.ws + WS_PFLG + 2048) + wid * 16 + fg * 4;
  }
  return r;
}

template <int OFF> DI bf16x8 lds16(unsigned a) { bf16x8 r; asm volatile("ds_read_b128 %0, %1 offset:%2" : "=v"(r) : "v"(a), "i"(OFF)); return r; }
template <int OFF> DI bf16x4 lds8(unsigned a) { bf16x4 r; asm volatile("ds_read_b64 %0, %1 offset:%2" : "=v"(r) : "v"(a), "i"(OFF)); return r; }
template <int OFF> DI bf16x4 ldstr(unsigned a) { bf16x4 r; asm volatile("ds_read_b64_tr_b16 %0, %1 offset:%2" : "=v"(r) : "v"(a), "i"(OFF)); return r; }
DI u32x2 gload8sc(const void* g) {
  u32x2 r; asm volatile("global_load_dwordx2 %0, %1, off sc1" : "=v"(r) : "v"(g) : "memory"); return r;
}
DI void st8sc(void* p, u32x2 v) { asm volatile("global_store_dwordx2 %0, %1, off sc1" ::"v"(p), "v"(v) : "memory"); }
DI void st4m(void* p, unsigned v, unsigned m) {
  asm volatile("s_mov_b32 exec_lo, %2\n\ts_mov_b32 exec_hi, 0\n\tglobal_store_dword %0, %1, off sc1\n\ts_mov_b64 exec, -1" ::"v"(p), "v"(v), "s"(m) : "memory");
}
DI void st16m(void* p, f32x4 v) {
  asm volatile("s_mov_b32 exec_lo, 0x10001\n\ts_mov_b32 exec_hi, 0x10001\n\tglobal_store_dwordx4 %0, %1, off sc1\n\ts_mov_b64 exec, -1\n\ts_nop 0" ::"v"(p), "v"(v) : "memory");
}

template <bool PACK> DI void attn_phase(char* smem, const MegaArgs& a, char* ws, int bid, int tid) {
  constexpr int LAYER = PACK ? 0 : 1;
  constexpr bool SC = !PACK;
  const int lane = tid & 63, wid = __builtin_amdgcn_readfirstlane(tid >> 6);
  STAMP(KID, 0);
  const int xcd = bid & 7, idx = bid >> 3;
  const int head = xcd * 4 + (idx >> 3), qb = idx & 7, odd = head & 1;
  const int r32 = lane & 31, hh = lane >> 5, half = wid >> 2;
  const int q0 = qb * 256 + (wid & 3) * 64 + r32;
  const char* kvb = uni(ws + WS_KV + LAYER * KV_SZ + (size_t)(head >> 1) * 16 * KVP_CHUNK);
  const unsigned sm = (unsigned)(uintptr_t)smem;
  const unsigned voff = half * 8 * KVP_CHUNK + odd * KVP_TAIL + (wid & 3) * 3072 + lane * 16;
  const unsigned mb = sm + half * ATT_HALF + (wid & 3) * 3072;
  auto stage = [&](int s, int buf) {
    const char* gs = kvb + s * KVP_CHUNK; const unsigned gm = mb + buf * ATT_STB;
    gld<0, SC>(voff, gs, gm); gld<1024, SC>(voff, gs, gm); gld<2048, SC>(voff, gs, gm);
  };
  if (!PACK) {
    const int c = head * DH + (lane & 16 ? DH - 1 : 0);
    if (wid == 0) await(ws, 4, (lane & 15) * 16 + ((c + (lane & 32 ? 2 * DM : DM)) >> 7));
    if (wid == 1) await(ws, 4, (lane & 46) == 0 ? (qb * 2 + (lane & 1)) * 16 + (c >> 7) : -1);
    raw_barrier();
  }
  STAMP(KID, 1);
  u32x4 qr1[2]; u32x2 qr2[2];
  {
    const char* Q = ws + WS_Q + LAYER * Q_SZ + (size_t)((head >> 1) * 64 + (q0 >> 5)) * 2560;
#pragma unroll
    for (int i = 0; i < 2; ++i) {
      qr1[i] = gload16sc(Q + i * 2560 + odd * 1024 + lane * 16);
      qr2[i] = gload8sc(Q + i * 2560 + 2048 + r32 * 16 + odd * 8);
    }
  }
  stage(0, 0);
  if (!AST) { stage(1, 1); stage(2, 2); }
  int tmw, tnw; tile_of<CWO>(bid, tmw, tnw);
  const char* wb = uni(ws + WS_PWO + (size_t)tnw * 102400);
  const unsigned wv = lane * 16, wm1 = sm + (wid < 6 ? WO_BHI + wid * 10240 : (wid - 6) * 10240), wm2 = sm + 20480 + wid * 3072;
  auto wpre = [&](auto K) {
    constexpr int k = decltype(K)::v;
    if (k < 10) gld<(k & 3) * 1024, false>(wv, wb + wid * 10240 + (k >> 2) * 4096, wm1 + (k >> 2) * 4096);
    else gld<(k < 10 ? 0 : k - 10) * 1024, false>(wv, wb + 81920 + wid * 3072, wm2);
    SB();
  };
  const unsigned m_fl = 1u >> wid;
  PackJob pj; unsigned pp; bf16x4 plo, phi; f32x4 pcs = (f32x4){0.f, 0.f, 0.f, 0.f};
  if (PACK) {
    pj = pack_job(a, bid, wid, lane);
    const int pr = 8 * (lane >> 5) + 4 * ((lane >> 4) & 1) + ((lane & 15) >> 2);
    pp = sm + ATT_PK + pr * 256 + (wid ^ (pr & 7)) * 32 + (lane & 3) * 8;
  }
  unsigned fa[2][3];
  fa[0][0] = sm + half * ATT_HALF + (odd ? KVP_KO - KVP_TAIL : KVP_KE) + lane * 16;
  fa[0][1] = sm + half * ATT_HALF + (odd ? 0 : KVP_TAIL) + r32 * 16 + odd * 8;
  fa[0][2] = sm + half * ATT_HALF + (odd ? KVP_VO - KVP_TAIL : 0) + (hh * 24 + min(r32, 23)) * 16;
#pragma unroll
  for (int i = 0; i < 3; ++i) { fa[1][i] = fa[0][i] + 2 * ATT_STB; asm volatile("" : "+v"(fa[0][i]), "+v"(fa[1][i])); }
  f32x16 oa, ob, nma, nmb, sa, sb;
#pragma unroll
  for (int i = 0; i < 16; ++i) { oa[i] = 0.f; ob[i] = 0.f; }
  wait_vm<AST ? 0 : 6>();
  if (AST) { stage(1, 1); stage(2, 2); }
#pragma unroll
  for (int i = 0; i < 2; ++i) asm volatile("" : "+v"(qr1[i]), "+v"(qr2[i]));
  const bf16x8 qa1 = BC8(qr1[0]), qb1 = BC8(qr1[1]);
  const bf16x4 qa2 = __builtin_bit_cast(bf16x4, hh ? (u32x2){0u, 0u} : qr2[0]), qb2 = __builtin_bit_cast(bf16x4, hh ? (u32x2){0u, 0u} : qr2[1]);
  raw_barrier();
  bf16x8 kc1 = lds16<0>(fa[0][0]); bf16x4 kc2 = lds8<0>(fa[0][1]);
  bf16x8 kn1 = lds16<1024>(fa[0][0]); bf16x4 kn2 = lds8<512>(fa[0][1]);
  bf16x8 v0 = lds16<0>(fa[0][2]), v1 = lds16<VFR>(fa[0][2]), vp1 = qa1;
  if (PACK) {
#pragma unroll
    for (int b = 0; b < 5; ++b) glds16(pj.src + (size_t)min(pj.kk0 + b, pj.kmax) * 32 * pj.ld, smem + ATT_PK + b * 8192 + wid * 1024);
  }
  asm volatile("s_waitcnt lgkmcnt(0)" : "+v"(kc1), "+v"(kc2), "+v"(kn1), "+v"(kn2), "+v"(v0), "+v"(v1));
  {
    sa = MF16(kc1, qa1, oa); sb = MF16(kc1, qb1, ob);
    sa = MF8(kc2, qa2, sa); sb = MF8(kc2, qb2, sb);
    const float ma = rowmax16(sa), mb = rowmax16(sb);
#pragma unroll
    for (int i = 0; i < 16; ++i) { nma[i] = -ma; nmb[i] = -mb; sa[i] -= ma; }
  }
  u32x4 p3 = (u32x4){0u, 0u, 0u, 0u};
  STAMP(KID, 2); CSTAMP(KID, 2);
  static_for<32>([&](auto S) {
    constexpr int c = decltype(S)::v >> 2, t = decltype(S)::v & 3;
    constexpr int s0 = decltype(S)::v, s1 = s0 + 1, s2 = s1 + 1;
    constexpr int wk = s0 <= 25 ? (s0 - 23) * 3 + 1 : (s0 - 27) * 3 + 10;
    constexpr bool wp = (s0 >= 23 && s0 <= 25) || s0 == 27;
    constexpr int b1 = (s1 >> 2) & 3, b2 = (s2 >> 2) & 3;
    constexpr int pb = PACK ? (decltype(S)::v - 10) >> 1 : -1;
    constexpr bool pk = pb >= 0 && pb < 5;
    const char* gs = kvb + (c + 3) * KVP_CHUNK; const unsigned gm = mb + ((c + 3) & 3) * ATT_STB;
    u32x4 p0, p1, p2; bf16x8 kx1, vx0, vx1; bf16x4 kx2;
    SB();
    sb = MF16(kc1, qb1, nmb); SB();
    if (t == 3 && c < 5) { gld<1024, SC>(voff, gs, gm); SB(); }
    if (wp) wpre(IC<(wp ? wk : 0)>{});
    E4(p0, 0, sa, 0) SB();
    ob = MF16(vp1, BC8(p3), ob); SB();
    if (t == 3 && c < 5) { gld<2048, SC>(voff, gs, gm); SB(); }
    if (wp) wpre(IC<(wp ? wk + 1 : 0)>{});
    if (PACK && s0 == 20) { st16m(pj.cs, pcs); SB(); }
    E4(p0, 2, sa, 4) SB();
    sb = MF8(kc2, qb2, sb); SB();
    if (pk && (t & 1)) asm volatile("s_waitcnt lgkmcnt(0)" : "+v"(kn1), "+v"(kn2), "+v"(v0), "+v"(v1), "+v"(plo), "+v"(phi));
    else asm volatile("s_waitcnt lgkmcnt(0)" : "+v"(kn1), "+v"(kn2), "+v"(v0), "+v"(v1));
    SB();
    E4(p1, 0, sa, 8) SB();
    oa = MF16(v0, BC8(p0), oa); SB();
    if (t == 2) {
      constexpr int W[7] = {8, 8, 3, 5, 7, 7, 10};
      if (c < 7) wait_vm<PACK ? W[c < 7 ? c : 0] : (c < 6 ? 3 : 10)>();
      __builtin_amdgcn_s_barrier();
      if (c < 5) gld<0, SC>(voff, gs, gm);
      if (PACK && c == 6) st4m(ws + WS_PFLG + bid * 4, 1u, m_fl);
      SB();
    }
    if (pk && (t & 1)) {
      const bf16x8 f = __builtin_shufflevector(plo, phi, 0, 1, 2, 3, 4, 5, 6, 7);
      const int kk = min(pj.kk0 + pb, pj.kmax);
      st16<3>(pj.dst + ((kk >> 1) * pj.s2 + (kk & 1)) * 1024, __builtin_bit_cast(u32x4, f));
      pcs = __builtin_amdgcn_mfma_f32_16x16x32_bf16(f, (bf16x8){0x3f80, 0x3f80, 0x3f80, 0x3f80, 0x3f80, 0x3f80, 0x3f80, 0x3f80}, pcs, 0, 0, 0);
      SB();
    }
    E4(p1, 2, sa, 12) SB();
    if (s1 < 32) { sa = MF16(kn1, qa1, nma); SB(); }
    if (s2 < 32) { kx1 = lds16<(b2 & 1) * ATT_STB + (s2 & 3) * 1024>(fa[b2 >> 1][0]); SB(); }
    E4(p2, 0, sb, 0) SB();
    oa = MF16(v1, BC8(p1), oa); SB();
    if (s2 < 32) { kx2 = lds8<(b2 & 1) * ATT_STB + (s2 & 3) * 512>(fa[b2 >> 1][1]); SB(); }
    if (wp || s0 == 22) wpre(IC<(wp ? wk + 2 : 0)>{});
    if (pk && !(t & 1)) { plo = ldstr<(pk ? pb : 0) * 8192>(pp); phi = ldstr<(pk ? pb : 0) * 8192 + 4096>(pp); SB(); }
    E4(p2, 2, sb, 4) SB();
    if (s1 < 32) { sa = MF8(kn2, qa2, sa); SB(); }
    if (s1 < 32) { vx0 = lds16<(b1 & 1) * ATT_STB + (s1 & 3) * 2 * VFR>(fa[b1 >> 1][2]); SB(); }
    E4(p3, 0, sb, 8) SB();
    ob = MF16(v0, BC8(p2), ob); SB();
    if (s1 < 32) { vx1 = lds16<(b1 & 1) * ATT_STB + (s1 & 3) * 2 * VFR + VFR>(fa[b1 >> 1][2]); SB(); }
    E4(p3, 2, sb, 12) SB();
    vp1 = v1; kc1 = kn1; kc2 = kn2;
    if (s2 < 32) { kn1 = kx1; kn2 = kx2; }
    if (s1 < 32) { v0 = vx0; v1 = vx1; }
  });
  ob = MF16(vp1, BC8(p3), ob);
  float ma = -nma[0], mb2 = -nmb[0];
  STAMP(KID, 3); CSTAMP(KID, 3);
  float* xo = (float*)(smem + ATT_X) + (wid ^ 4) * 13 * 64 + lane;
  const float* xi = (const float*)(smem + ATT_X) + wid * 13 * 64 + lane;
  unsigned* badw = (unsigned*)(smem + L_SMALL);
  auto hand_over = [&]() {
#pragma unroll
    for (int i = 0; i < 12; ++i) xo[i * 64] = half ? oa[i] : ob[i];
    xo[12 * 64] = half ? ma : mb2;
  };
  hand_over();
  {
    const bool bad = __builtin_amdgcn_ballot_w64(nonfinite12(oa, lane) || nonfinite12(ob, lane)) != 0;
    if (lane == 0) badw[wid] = bad;
  }
  raw_barrier();
  const u32x4 bw = *(const u32x4*)badw | *(const u32x4*)(badw + 4);
  if (__builtin_amdgcn_readfirstlane(bw[0] | bw[1] | bw[2] | bw[3])) {
    wait_vm<0>();
    raw_barrier();
    stage(0, 2);
#pragma unroll
    for (int i = 0; i < 16; ++i) { oa[i] = 0.f; ob[i] = 0.f; }
    ma = -3.0e38f; mb2 = -3.0e38f;
#pragma unroll 1
    for (int c = 0; c < 8; ++c) {
      wait_vm<0>();
      raw_barrier();
      if (c + 1 < 8) stage(c + 1, 2 + ((c + 1) & 1));
      const unsigned bo = (c & 1) * ATT_STB;
#pragma unroll 1
      for (int t = 0; t < 4; ++t) {
        kc1 = lds16<0>(fa[1][0] + bo + t * 1024); kc2 = lds8<0>(fa[1][1] + bo + t * 512);
        v0 = lds16<0>(fa[1][2] + bo + t * 2 * VFR); v1 = lds16<VFR>(fa[1][2] + bo + t * 2 * VFR);
        asm volatile("s_waitcnt lgkmcnt(0)" : "+v"(kc1), "+v"(kc2), "+v"(v0), "+v"(v1));
        auto one = [&](const bf16x8& q1, const bf16x4& q2, f32x16& o, float& m) {
          f32x16 s;
#pragma unroll
          for (int i = 0; i < 16; ++i) s[i] = 0.f;
          s = MF16(kc1, q1, s); s = MF8(kc2, q2, s);
          const float mn = fmaxf(m, rowmax16(s)), f = EX(m - mn);
          m = mn;
          u32x4 p0, p1;
#pragma unroll
          for (int i = 0; i < 16; ++i) { s[i] -= mn; o[i] *= f; }
          E4(p0, 0, s, 0) E4(p0, 2, s, 4) E4(p1, 0, s, 8) E4(p1, 2, s, 12)
          o = MF16(v0, BC8(p0), o); o = MF16(v1, BC8(p1), o);
        };
        one(qa1, qa2, oa, ma); one(qb1, qb2, ob, mb2);
      }
    }
    raw_barrier();
    hand_over();
    raw_barrier();
  }
  STAMP(KID, 4);
  {
    const float m1 = half ? mb2 : ma, m2 = xi[12 * 64], M = fmaxf(m1, m2);
    const float f1 = EX(m1 - M), f2 = EX(m2 - M);
    float o[12];
#pragma unroll
    for (int i = 0; i < 12; ++i) o[i] = (half ? ob[i] : oa[i]) * f1 + xi[i * 64] * f2;
    const float l = __uint_as_float(swap32(__float_as_uint(o[8]), __float_as_uint(o[8]))[1]);
    const float inv = 1.0f / l;
    const int q = q0 + half * 32, c0 = head * DH;
    char* O = ws + WS_O;
    const u32x2 x = swap32(cvtpk(o[0] * inv, o[1] * inv), cvtpk(o[4] * inv, o[5] * inv));
    const u32x2 y = swap32(cvtpk(o[2] * inv, o[3] * inv), cvtpk(o[6] * inv, o[7] * inv));
    st16sc(O + foff<10>(q, c0 + odd * 4 + hh * 8), (u32x4){x[0], y[0], x[1], y[1]});
    if (hh == 0) st8sc(O + foff<10>(q, c0 + (odd ? 0 : 16)), (u32x2){cvtpk(o[8] * inv, o[9] * inv), cvtpk(o[10] * inv, o[11] * inv)});
  }
  STAMP(KID, 5);
  publish<8>(smem, ws, LAYER * 5, qb * 32 + head, tid);
  STAMP(KID, 6);
}

__global__ __launch_bounds__(512) void mega_kernel(MegaArgs g) {
  __shared__ __attribute__((aligned(16))) char smem[L_TOTAL];
  KLABEL("kbeg_", 1);
  const int tid = threadIdx.x, bid = blockIdx.x;
  char* ws = g.ws;
  if (tid == 0) *(unsigned*)(smem + L_CNT) = 0u;
  attn_phase<true>(smem, g, ws, bid, tid);
#pragma unroll 1
  for (int layer = 0;; ++layer) {
    int tmq, tnq; tile_of<CQ1>(opaque(bid), tmq, tnq);
    const bool hasq = layer == 0 && tnq < 15;
    const unsigned xmid = layer ? WS_X3 : WS_X1;
    const int K0 = layer * 5;
    if (__builtin_amdgcn_readfirstlane(tid) >= 256) {
      if (layer == 0) {
        const char* p = ws + WS_PFLG + (tid & 63) * 16;
        for (int s = 0; s < (1 << 18); ++s) {
          u32x4 v = gload16sc(p);
          wait_vm<0>();
          asm volatile("" : "+v"(v));
          if (__builtin_amdgcn_ballot_w64((v[0] & v[1] & v[2] & v[3]) != 1u) == 0) break;
          __builtin_amdgcn_s_sleep(4);
        }
      }
      char* w;
      w = opaque(ws); loader_wo(smem, w, opaque(bid), opaque_v(tid));
      w = opaque(ws); loader_ln<CGU>(smem, w + xmid, w, WS_STAT, opaque(bid), opaque_v(tid));
      w = opaque(ws); loader_dn(smem, w, hasq, opaque(bid), opaque_v(tid));
      w = opaque(ws);
      if (hasq) loader_ln<CQ1>(smem, w + WS_X2, w, WS_STAT2, opaque(bid), opaque_v(tid));
    } else {
      char* w;
      STAMP(K0 + 2, 0);
      w = opaque(ws); compute_res<CWO, 0>(smem, w + WS_O, layer ? w + WS_X2 : g.x, layer == 0, w + xmid, false, w, WS_STAT, K0, opaque(bid), opaque_v(tid), K0 + 2);
      STAMP(K0 + 3, 0);
      w = opaque(ws); compute_ln<CGU>(smem, w + WS_H, w, K0 + 1, opaque(bid), opaque_v(tid), K0 + 3);
      STAMP(K0 + 4, 0);
      w = opaque(ws); compute_res<CDN, 1>(smem, w + WS_H, nullptr, false, layer ? g.out : w + WS_X2, layer == 1, w, WS_STAT2, K0 + 2, opaque(bid), opaque_v(tid), K0 + 4);
      if (hasq) {
        STAMP(5, 0);
        w = opaque(ws); compute_ln<CQ1>(smem, nullptr, w, 3, opaque(bid), opaque_v(tid), 5);
      }
    }
    if (layer) break;
    attn_phase<false>(smem, g, opaque(ws), opaque(bid), opaque_v(tid));
  }
}

struct Q0Args { const char* x; const char* wqkv; const u16* wo; char* ws; const char* wg; const char* wu; const char* wd; const char* out; };
__global__ __launch_bounds__(512) void qkv0_kernel(Q0Args g) {
  __shared__ __attribute__((aligned(16))) char smem[L_TOTAL];
  KLABEL("kbeg_", 0);
  const int tid = threadIdx.x, bid = blockIdx.x;
  int tm0, tn0; tile_of<CQ0>(bid, tm0, tn0);
  if (tn0 == 15) {
    {
      const int lane = tid & 63, part = __builtin_amdgcn_readfirstlane(tid >> 6) * 2 + ((bid >> 3) & 1);
      const char* dummy = smem + L_SMALL + 2048;
      if (TC & 2) touch_code<0, 2>(dummy, part, lane);
      if (TC & 4) {
        const unsigned o = (part * 64 + lane) << 16;
        if (o < WS_END) gtouch(g.ws + o, dummy);
        const char* q = part & 2 ? (part & 1 ? g.out : g.wd) : (part & 1 ? g.wu : g.wg);
        if (part >= 12 && lane * 65536 < DM * DF * 2) gtouch(q + lane * 65536, dummy);
      }
    }
    if (tid < 128) *(unsigned*)(g.ws + WS_FLG + (tm0 * 128 + tid) * 128) = 0u;
    else if (tid < 144) *(unsigned*)(g.ws + WS_PFLG + (tm0 * 16 + tid - 128) * 4) = 0u;
    const int lane = tid & 63, wid = __builtin_amdgcn_readfirstlane(tid >> 6);
    const int prow = wid * 4 + (lane >> 4), c16 = lane & 15, ch = (c16 >> 1) ^ (prow & 7);
    const int k = 8 * (((prow >> 3) & 1) * 2 + ((prow >> 2) & 1)) + 4 * ((prow >> 4) & 1) + (prow & 3);
    const int pr = 8 * (lane >> 5) + 4 * ((lane >> 4) & 1) + ((lane & 15) >> 2);
#pragma unroll
    for (int b = 0; b < 7; ++b) {
      const int id = min(tm0 * 7 + b, 99), cg = id / 20, kk = id % 20;
      glds16(g.wo + (size_t)(kk * 32 + k) * DM + cg * 128 + ch * 16 + (c16 & 1) * 8, smem + b * 8192 + wid * 1024);
    }
    wait_vm<0>();
    raw_barrier();
#pragma unroll
    for (int b = 0; b < 7; ++b) {
      const int id = min(tm0 * 7 + b, 99), cg = id / 20, kk = id % 20, jb = cg * 8 + wid;
      const char* p = smem + b * 8192 + pr * 256 + (wid ^ (pr & 7)) * 32 + (lane & 3) * 8;
      const bf16x4 lo = tr_read(p), hi = tr_read(p + 4096);
      *(bf16x8*)(g.ws + WS_PWO + (size_t)((jb / 5) * 100 + (jb % 5) * 2 + (kk >> 1) * 10 + (kk & 1)) * 1024 + lane * 16) = __builtin_shufflevector(lo, hi, 0, 1, 2, 3, 4, 5, 6, 7);
    }
    if (TC & 1) touch_code<1, 0>(smem + L_SMALL + 2048, wid * 2 + ((bid >> 3) & 1), lane);
#define WARM 1
    if (WARM & 1) {
#pragma unroll
      for (int i = 0; i < 2; ++i) glds16(g.ws + WS_STAT + (size_t)((tm0 * 2 + i) * 512 + tid) * 16, smem + 65536 + wid * 1024);
    }
    if (WARM & 2) {
#pragma unroll 4
      for (int i = 0; i < 20; ++i) glds16(g.ws + WS_O + (size_t)((tm0 * 20 + i) * 512 + tid) * 16, smem + 65536 + wid * 1024);
    }
    if (WARM & 4) {
#pragma unroll
      for (int i = 0; i < 2; ++i) *(u32x4*)(g.ws + WS_STAT + (size_t)((tm0 * 2 + i) * 512 + tid) * 16) = (u32x4){0u, 0u, 0u, 0u};
    }
    if (WARM & 8) {
#pragma unroll 4
      for (int i = 0; i < 20; ++i) *(u32x4*)(g.ws + WS_O + (size_t)((tm0 * 20 + i) * 512 + tid) * 16) = (u32x4){0u, 0u, 0u, 0u};
    }
    wait_vm<0>();
    KLABEL("kbeg_", 2);
    return;
  }
  if (tid < 256) STAMP(KID, 0);
  if (tid >= 256) {
    loader_raw(smem, g.x, g.wqkv, bid, tid);
    return;
  }
#define L2W 0
#define L2S 4
#define L2F 2
  if (L2W) {
    const int wid = __builtin_amdgcn_readfirstlane(tid >> 6), gw = (bid >> 3) * 4 + wid;
    const unsigned voff = (tid & 63) * 16, m = (unsigned)(uintptr_t)smem + 131072 + wid * 1024;
    __builtin_amdgcn_s_sleep(L2S);
    const char* px = uni(g.x + (size_t)(bid & 7) * 327680 + gw * 1024);
    gld<0, false>(voff, px, m); gld<0, false>(voff, px + 120 * 1024, m);
    if (gw < 80) gld<0, false>(voff, px + 240 * 1024, m);
    const char* pw = uni(g.wqkv + gw * 2048);
#pragma unroll
    for (int st = L2F; st < 10; ++st) { gld<0, false>(voff, pw + st * 245760, m); gld<0, false>(voff, pw + st * 245760 + 1024, m); }
  }
  compute_ln<CQ0>(smem, nullptr, g.ws, 0, bid, tid, 0);
  STAMP(KID, 6);
}

extern "C" void kernel_launch(void* const* d_in, const int* in_sizes, int n_in, void* d_out, int out_size, void* d_ws,
                              size_t ws_size, hipStream_t stream) {
  char* ws = (char*)d_ws;
  Q0Args q0{(const char*)d_in[0], (const char*)d_in[1], (const u16*)d_in[2], ws, (const char*)d_in[4], (const char*)d_in[5], (const char*)d_in[6], (const char*)d_out};
  qkv0_kernel<<<256, 512, 0, stream>>>(q0);
  MegaArgs m{ws, (const char*)d_in[0], (char*)d_out, (const u16*)d_in[1], (const u16*)d_in[4], (const u16*)d_in[5], (const u16*)d_in[6]};
  mega_kernel<<<256, 512, 0, stream>>>(m);
}
```

```cpp
#include <hip/hip_runtime.h>
#include <hip/hip_bf16.h>
#include <stdint.h>

typedef __attribute__((ext_vector_type(8))) short bf16x8;
typedef __attribute__((ext_vector_type(4))) short bf16x4;
typedef __attribute__((ext_vector_type(4))) float f32x4;
typedef __attribute__((ext_vector_type(16))) float f32x16;
typedef __attribute__((ext_vector_type(4))) unsigned u32x4;
typedef __attribute__((ext_vector_type(2))) unsigned u32x2;
typedef unsigned short u16;

#define DI __device__ __forceinline__

constexpr int SEQ = 2048, DM = 640, NH = 32, DH = 20, DF = 1728;
constexpr float LN_EPS = 1e-5f;
constexpr float QSCALE = 0.22360679774997896f * 1.4426950408889634f;

constexpr size_t WS_X1   = 0;
constexpr size_t WS_X2   = WS_X1 + (size_t)SEQ * DM * 2;
constexpr size_t WS_X3   = WS_X2 + (size_t)SEQ * DM * 2;
constexpr size_t WS_Q    = WS_X3 + (size_t)SEQ * DM * 2;
constexpr size_t Q_SZ    = (size_t)SEQ * DM * 2;
constexpr size_t WS_O    = WS_Q + 2 * Q_SZ;
constexpr size_t WS_H    = WS_O + (size_t)SEQ * DM * 2;
constexpr int    KVP_CHUNK = 22528, KVP_KE = 6144, KVP_TAIL = 10240, KVP_KO = 12288, KVP_VO = 16384, VFR = 768;
constexpr size_t WS_KV   = WS_H + (size_t)SEQ * DF * 2;
constexpr size_t KV_SZ   = (size_t)16 * 16 * KVP_CHUNK;
constexpr size_t WS_PWO  = WS_KV + 2 * KV_SZ;
constexpr size_t WS_PWD  = WS_PWO + (size_t)DM * DM * 2 + 4096;
constexpr size_t WS_PGU  = WS_PWD + (size_t)DF * DM * 2;
constexpr size_t WS_PQKV = WS_PGU + (size_t)16 * 10 * 28 * 1024;
constexpr size_t WS_CGU  = WS_PQKV + (size_t)DM * 3 * DM * 2;
constexpr size_t WS_CQKV = WS_CGU + (size_t)16 * 4 * 224 * 4;
constexpr size_t WS_STAT = WS_CQKV + (size_t)15 * 4 * 128 * 4;
constexpr size_t WS_STAT2 = WS_STAT + (size_t)SEQ * 64;
constexpr size_t WS_FLG  = WS_STAT2 + (size_t)SEQ * 64;
constexpr size_t WS_PFLG = WS_FLG + 8 * 256 * 128;
constexpr size_t WS_END  = WS_PFLG + 4096;

typedef __attribute__((ext_vector_type(2))) __bf16 bf16p;
typedef __attribute__((ext_vector_type(2))) float f32x2;
DI unsigned cvtpk(float lo, float hi) { return __builtin_bit_cast(unsigned, __builtin_convertvector((f32x2){lo, hi}, bf16p)); }
DI float dot2ones(unsigned a, float s) {
  return __builtin_amdgcn_fdot2_f32_bf16(__builtin_bit_cast(bf16p, a), __builtin_bit_cast(bf16p, 0x3f803f80u), s, false);
}
DI float bflo(unsigned v) { return __uint_as_float(v << 16); }
DI float bfhi(unsigned v) { return __uint_as_float(v & 0xffff0000u); }
#define KIDARG(k)
#define STAMP(kid, i)
#define CSTAMP(kid, i)
template <int N> DI void wait_vm() { asm volatile("s_waitcnt vmcnt(%0)" ::"i"(N) : "memory"); }
DI void raw_barrier() {
  asm volatile("" ::: "memory");
  __builtin_amdgcn_s_waitcnt(0xC07F);
  __builtin_amdgcn_s_barrier();
  asm volatile("" ::: "memory");
}
DI u32x2 swap16(unsigned a, unsigned b) { return __builtin_amdgcn_permlane16_swap(a, b, false, false); }
DI u32x2 swap32(unsigned a, unsigned b) { return __builtin_amdgcn_permlane32_swap(a, b, false, false); }
DI bf16x4 tr_read(const char* p) {
  typedef short v4i16_t __attribute__((ext_vector_type(4)));
  return __builtin_bit_cast(bf16x4, __builtin_amdgcn_ds_read_tr16_b64_v4i16((__attribute__((address_space(3))) v4i16_t*)(p)));
}
template <bool SC = false> DI void glds16(const void* g, const char* l) {
  unsigned sv; const unsigned lb = __builtin_amdgcn_readfirstlane((unsigned)(uintptr_t)l);
  if (SC) asm volatile("s_mov_b32 %0, m0\n\ts_mov_b32 m0, %2\n\ts_nop 0\n\tglobal_load_lds_dwordx4 %1, off sc1\n\ts_mov_b32 m0, %0"
               : "=&s"(sv) : "v"(g), "s"(lb) : "memory");
  else asm volatile("s_mov_b32 %0, m0\n\ts_mov_b32 m0, %2\n\ts_nop 0\n\tglobal_load_lds_dwordx4 %1, off\n\ts_mov_b32 m0, %0"
               : "=&s"(sv) : "v"(g), "s"(lb) : "memory");
}
DI void gtouch(const void* g, const char* l) {
  unsigned sv; const unsigned lb = __builtin_amdgcn_readfirstlane((unsigned)(uintptr_t)l);
  asm volatile("s_mov_b32 %0, m0\n\ts_mov_b32 m0, %2\n\ts_nop 0\n\tglobal_load_lds_dword %1, off\n\ts_mov_b32 m0, %0"
               : "=&s"(sv) : "v"(g), "s"(lb) : "memory");
}
#define KLABEL(pfx, id) asm volatile(pfx "%c0:" ::"i"(id))
template <int ID> DI unsigned long long kaddr() {
  unsigned long long a;
  asm volatile("s_getpc_b64 s[20:21]\n\ts_add_u32 s20, s20, kbeg_%c1@rel32@lo+4\n\ts_addc_u32 s21, s21, kbeg_%c1@rel32@hi+12\n\ts_mov_b64 %0, s[20:21]" : "=s"(a) : "i"(ID) : "s20", "s21", "scc");
  return a;
}
template <int A, int B> DI void touch_code(const char* dummy, int part, int lane) {
  const unsigned long long lo = kaddr<A>() & ~255ull, hi = kaddr<B>();
  const unsigned o = part * 8192 + lane * 128;
  if (hi > lo && o < (unsigned)(hi - lo)) gtouch((const char*)lo + o, dummy);
}
#define SLP 1
#define MAPR 8
#define MAPL 8
#define MAPQ 2
#define MAP0 2
#define AST 1
#define TC 7
DI u32x2 gload8(const void* g) {
  u32x2 r; asm volatile("global_load_dwordx2 %0, %1, off" : "=v"(r) : "v"(g) : "memory"); return r;
}
DI unsigned gload4(const void* g) {
  unsigned r; asm volatile("global_load_dword %0, %1, off" : "=v"(r) : "v"(g) : "memory"); return r;
}
DI u32x4 gload16(const void* g) {
  u32x4 r; asm volatile("global_load_dwordx4 %0, %1, off" : "=v"(r) : "v"(g) : "memory"); return r;
}
DI u32x4 gload16sc(const void* g) {
  u32x4 r; asm volatile("global_load_dwordx4 %0, %1, off sc1" : "=v"(r) : "v"(g) : "memory"); return r;
}
DI void st4sc(void* p, float v) { asm volatile("global_store_dword %0, %1, off sc1" ::"v"(p), "v"(v) : "memory"); }
DI void st16sc(void* p, u32x4 v) { asm volatile("global_store_dwordx4 %0, %1, off sc1\n\ts_nop 1" ::"v"(p), "v"(v)); }
#define ST_MODE 14
template <int CLS> DI void st16(void* p, u32x4 v) {
  if ((ST_MODE >> CLS) & 1) asm volatile("global_store_dwordx4 %0, %1, off sc1\n\ts_nop 1" ::"v"(p), "v"(v));
  else *(u32x4*)p = v;
}
template <int IMM, bool WT = true> DI void stw(const void* base, unsigned off, u32x4 v) {
  if (WT) asm volatile("global_store_dwordx4 %0, %1, %2 offset:%3 sc1\n\ts_nop 1" ::"v"(off), "v"(v), "s"(base), "i"(IMM));
  else asm volatile("global_store_dwordx4 %0, %1, %2 offset:%3\n\ts_nop 1" ::"v"(off), "v"(v), "s"(base), "i"(IMM));
}
template <int CLS> DI void st8(void* p, u32x2 v) {
  if ((ST_MODE >> CLS) & 1) asm volatile("global_store_dwordx2 %0, %1, off sc1" ::"v"(p), "v"(v) : "memory");
  else *(u32x2*)p = v;
}
template <int I> struct IC { static constexpr int v = I; };
template <int L, int NMAX> DI void wait_stages(int n) {
  if constexpr (NMAX == 0) wait_vm<0>();
  else { if (n >= NMAX) wait_vm<NMAX * L>(); else wait_stages<L, NMAX - 1>(n); }
}
template <int N, int I = 0, class F> DI void static_for(F&& f) {
  if constexpr (I < N) { f(IC<I>{}); static_for<N, I + 1>(f); }
}
#define SB() __builtin_amdgcn_sched_barrier(0)
template <int NS> DI unsigned foff(int row, int col) {
  return (unsigned)((((row >> 6) * NS + (col >> 6)) * 8 + ((row >> 4) & 3) * 2 + ((col >> 5) & 1)) * 1024 +
                    (((col >> 3) & 3) * 16 + (row & 15)) * 16 + (col & 7) * 2);
}

enum { EPI_QKV = 0, EPI_RES = 1, EPI_GEGLU = 2 };
constexpr int L_TOTAL = 163840, L_SMALL = L_TOTAL - 2304, L_CNT = L_SMALL + 1020 + 1024, L_XB = L_SMALL - 20480, L_XHI = L_XB + 8192;
constexpr int WO_BHI = 98304;
constexpr int wo_b(int s) { return s < 6 ? WO_BHI + s * 10240 : (s - 6) * 10240; }
DI int xb_off(int w, int j) { return j < 3 ? L_XHI + (w * 3 + j) * 1024 : L_XB + (w * 2 + j - 3) * 1024; }

template <int BM_, int BN_, int KTOT_, int NST_, int EPI_, bool RAW_, unsigned TA_, unsigned TB_> struct Cfg {
  static constexpr int BM = BM_, BN = BN_, KTOT = KTOT_, NST = NST_, EPI = EPI_;
  static constexpr bool RAW = RAW_, RES = (EPI_ == EPI_RES), LNP = !RES && !RAW_;
  static constexpr int MT = RES ? 2 : 4, NT = RES ? 5 : BN_ / 32, KT = KTOT_ / 64;
  static constexpr int RS = BN_ * 2;
  static constexpr DI int aslot(int b) { return (int)((TA_ >> (8 * b)) & 255u) << 10; }
  static constexpr DI int bslot(int b) { return (int)((TB_ >> (8 * b)) & 255u) << 10; }
};
constexpr unsigned tb4(unsigned a, unsigned b, unsigned c, unsigned d) { return a | b << 8 | c << 16 | d << 24; }
typedef Cfg<128, 128, 640, 4, EPI_QKV, true, tb4(0, 32, 64, 96), tb4(16, 48, 80, 112)> CQ0;
typedef Cfg<128, 128, 640, 4, EPI_QKV, false, tb4(0, 32, 136, 48), tb4(68, 16, 96, 116)> CQ1;
typedef Cfg<64, 80, 640, 8, EPI_RES, false, 0, 0> CWO;
typedef Cfg<128, 224, 640, 3, EPI_GEGLU, false, tb4(0, 16, 124, 0), tb4(40, 68, 96, 0)> CGU;
typedef Cfg<64, 80, 1728, 8, EPI_RES, false, 0, 0> CDN;
constexpr int dn_b(int s) {
  constexpr int Q[7] = {20480, 71680, 92160, 112640, 0, 40960, 61440};
  return (s >> 1) % 7 == 6 && (s & 1) ? 133120 : Q[(s >> 1) % 7] + (s & 1) * 10240;
}

template <class C> DI void tile_of(int bid, int& tm, int& tn) {
  const int xcd = bid & 7, idx = bid >> 3;
  constexpr int R = C::RES ? MAPR : (C::EPI == EPI_QKV ? (C::RAW ? MAP0 : MAPQ) : MAPL), NC = (C::RES ? 8 : 16) / (32 / R);
  tm = (xcd / NC) * R + idx % R; tn = (idx / R) * NC + xcd % NC;
}
DI const char* uni(const char* p) {
  const unsigned long long v = (unsigned long long)p;
  return (const char*)(((unsigned long long)(unsigned)__builtin_amdgcn_readfirstlane((unsigned)(v >> 32)) << 32) | (unsigned)__builtin_amdgcn_readfirstlane((unsigned)v));
}
DI char* opaque(char* p) { asm volatile("" : "+s"(p)); return p; }
DI int opaque(int x) { asm volatile("" : "+s"(x)); return x; }
DI int opaque_v(int x) { asm volatile("" : "+v"(x)); return x; }

template <int OFF, bool SC> DI void gld(unsigned voff, const char* sb, unsigned m) {
  if (SC) asm volatile("s_mov_b32 m0, %2\n\ts_nop 0\n\tglobal_load_lds_dwordx4 %0, %1 offset:%3 sc1" ::"v"(voff), "s"(sb), "s"(m), "i"(OFF) : "memory", "m0");
  else asm volatile("s_mov_b32 m0, %2\n\ts_nop 0\n\tglobal_load_lds_dwordx4 %0, %1 offset:%3" ::"v"(voff), "s"(sb), "s"(m), "i"(OFF) : "memory", "m0");
}
template <int OFF> DI void gldh(unsigned voff, const char* sb, unsigned m) {
  asm volatile("s_mov_b32 m0, %2\n\ts_mov_b32 exec_hi, 0\n\tglobal_load_lds_dwordx4 %0, %1 offset:%3\n\ts_mov_b32 exec_hi, -1" ::"v"(voff), "s"(sb), "s"(m), "i"(OFF) : "memory", "m0");
}
template <int N, bool SC> DI void gldn(unsigned voff, const char* sb, unsigned m) {
  static_for<N>([&](auto I) { gld<decltype(I)::v * 1024, SC>(voff, sb, m); });
}
template <int OFF> DI u32x4 gload16s(unsigned voff, const char* sb) {
  u32x4 r; asm volatile("global_load_dwordx4 %0, %1, %2 offset:%3 sc1" : "=v"(r) : "v"(voff), "s"(sb), "i"(OFF) : "memory"); return r;
}

template <int OFF> DI u32x4 gload16n(unsigned voff, const char* sb) {
  u32x4 r; asm volatile("global_load_dwordx4 %0, %1, %2 offset:%3" : "=v"(r) : "v"(voff), "s"(sb), "i"(OFF) : "memory"); return r;
}
template <int OFF> DI void dsw16(unsigned a, u32x4 v) { asm volatile("ds_write_b128 %0, %1 offset:%2" ::"v"(a), "v"(v), "i"(OFF) : "memory"); }

template <int OFF> DI bf16x8 lds16(unsigned a) { bf16x8 r; asm volatile("ds_read_b128 %0, %1 offset:%2" : "=v"(r) : "v"(a), "i"(OFF)); return r; }

template <int P> DI float qx(float v) { return __uint_as_float(__builtin_amdgcn_update_dpp(0u, __float_as_uint(v), P, 0xf, 0xf, true)); }

DI void loader_raw(char* smem, const char* A, const char* B, int bid, int tid) {
  typedef CQ0 C;
  constexpr int KT = C::KT, NST = C::NST, L = 8, LA = 4;
  const int lane = tid & 63, lw = __builtin_amdgcn_readfirstlane(tid >> 6) - 4;
  int tm, tn; tile_of<C>(bid, tm, tn);
  const int m0 = tm * C::BM, n0 = tn * C::BN;
  const char* src[L];
#pragma unroll
  for (int i = 0; i < L; ++i) {
    const int p = lw + 4 * i;
    if (i < LA) {
      const int row = p * 8 + (lane >> 3), c = (lane & 7) ^ ((row >> 1) & 7);
      src[i] = A + ((size_t)(m0 + row) * C::KTOT + c * 8) * 2;
    } else {
      const int u = (p - 16) * 64 + lane, prow = u >> 4, c16 = u & 15;
      const int ch = (c16 >> 1) ^ (prow & 7);
      const int k = (prow & 32) + 8 * (((prow >> 3) & 1) * 2 + ((prow >> 2) & 1)) + 4 * ((prow >> 4) & 1) + (prow & 3);
      src[i] = B + ((size_t)k * (3 * DM) + n0 + ch * 16 + (c16 & 1) * 8) * 2;
    }
  }
  auto issue = [&](int buf) {
    char* base = smem + buf * 32768 + lw * 1024;
#pragma unroll
    for (int i = 0; i < L; ++i) {
      glds16(src[i], base + i * 4096);
      src[i] += (i < LA) ? 128 : 64 * 3 * DM * 2;
    }
  };
  issue(0); issue(1);
  int nxt = 2, ibuf = 2;
  for (int i = 0; i < KT; ++i) {
    wait_stages<L, NST - 1>(nxt - i - 1);
    raw_barrier();
    const int lim = min(KT, i + NST);
#pragma unroll
    for (int r = 0; r < 2; ++r)
      if (nxt < lim) { issue(ibuf); ++nxt; if (++ibuf == NST) ibuf = 0; }
  }
  raw_barrier();
}

#define THN 4
#define SDL 1
#define THG 0
template <int N, int TH> DI void gldt(unsigned voff, const char* sb, unsigned m) {
  static_for<N>([&](auto I) { gld<decltype(I)::v * 1024, false>(voff, sb, m); if (TH) wait_vm<(TH ? TH - 1 : 0)>(); });
}
template <int TH = 0> DI void ld_gu_b(unsigned sm, const char* ws, int tn, int lw, unsigned voff, int s, int slot) {
  const char* p = ws + WS_PGU + (size_t)(tn * 10 + s) * 28672 + lw * 7168;
  const unsigned m = sm + CGU::bslot(slot) + lw * 7168;
  gldt<4, TH>(voff, p, m); gldt<3, TH>(voff, p + 4096, m + 4096);
}
template <int TH = 0> DI void ld_q_b(unsigned sm, const char* ws, int tn, int lw, unsigned voff, int s, int slot) {
  gldt<4, TH>(voff, ws + WS_PQKV + (size_t)(tn * 10 + s) * 16384 + lw * 4096, sm + CQ1::bslot(slot) + lw * 4096);
}
DI const char* dn_src(const char* ws, int tn, int lw, int p) { return ws + WS_PWD + (size_t)(tn * 27 + 2 * p) * 10240 + (p == 13 ? lw & 1 : lw) * 5120; }
DI unsigned dn_dst(unsigned sm, int lw, int p) { return sm + (lw >> 1 && p < 13 ? dn_b(2 * p + 1) : dn_b(2 * p)) + (lw & 1) * 5120; }
template <int TH = 0> DI void ld_dn_b(unsigned sm, const char* ws, int tn, int lw, unsigned voff, int p) {
  const char* g = dn_src(ws, tn, lw, p);
  const unsigned m = dn_dst(sm, lw, p);
  gldt<4, TH>(voff, g, m); gldt<1, TH>(voff, g + 4096, m + 4096);
}
#define NP8 1
#define THD 0
constexpr int gu_cnt(int i) { return (i >= 1 && i <= 7 ? 11 : 0) + (i == 5 ? 6 : 0) + (i == 8 ? 5 * NP8 : 0); }
constexpr int gu_wait(int k) { return k == 0 ? 8 : k == 1 ? 4 + gu_cnt(0) : k == 2 ? gu_cnt(0) + gu_cnt(1) : gu_cnt(k - 2) - 11 + gu_cnt(k - 1); }

DI void loader_wo(char* smem, const char* ws, int bid, int tid) {
  const int lw = __builtin_amdgcn_readfirstlane(tid >> 6) - 4;
  const unsigned voff = (tid & 63) * 16, sm = (unsigned)(uintptr_t)smem;
  int tm, tn; tile_of<CGU>(bid, tm, tn);
  raw_barrier();
  raw_barrier();
  ld_gu_b(sm, ws, tn, lw, voff, 0, 0);
  ld_gu_b(sm, ws, tn, lw, voff, 1, 1);
  raw_barrier();
  __builtin_amdgcn_s_sleep(SDL);
  ld_gu_b<THN>(sm, ws, tn, lw, voff, 2, 2);
}

template <class C> DI void loader_ln(char* smem, const char* A, const char* ws, size_t stat, int bid, int tid, u32x4 (&R)[35]) {
  constexpr bool GU = C::EPI == EPI_GEGLU;
  constexpr int KT = C::KT, NST = C::NST, BN = C::BN;
  const int lw = __builtin_amdgcn_readfirstlane(tid >> 6) - 4;
  const unsigned voff = (tid & 63) * 16, sm = (unsigned)(uintptr_t)smem;
  int tm, tn; tile_of<C>(bid, tm, tn);
  int tmd, tnd; tile_of<CDN>(bid, tmd, tnd);
  const char* pa = A + (size_t)(tm * 2 + (lw >> 1)) * KT * 8192 + (lw & 1) * 4096;
  auto lda = [&](int s) { gldn<4, true>(voff, pa + s * 8192, sm + C::aslot(s % NST) + lw * 4096); };
  auto ldb = [&](int s) { if (GU) ld_gu_b(sm, ws, tn, lw, voff, s, s % NST); else ld_q_b(sm, ws, tn, lw, voff, s, s % NST); };
  const int t = tid - 256;
  unsigned c[4]; u32x4 st[2];
  raw_barrier();
#pragma unroll
  for (int s = 0; s < NST; ++s) lda(s);
  static_for<KT>([&](auto I) {
    constexpr int i = decltype(I)::v;
    constexpr int WQ[10] = {12, 8, 12, 16, 16, 22, 22, 22, 8, 0};
    wait_vm<GU ? gu_wait(i) : WQ[i]>();
    raw_barrier();
    if (i >= 1 && i <= KT - NST) { lda(i - 1 + NST); ldb(i - 1 + NST); }
    if (i == KT - NST - 2) {
      const float* cp = (const float*)(ws + (GU ? WS_CGU : WS_CQKV)) + tn * 4 * BN + (t < BN ? t : 0);
      const char* sp = ws + stat + (size_t)tm * 8192 + lw * 2048;
#pragma unroll
      for (int k = 0; k < 4; ++k) c[k] = gload4(cp + k * BN);
      st[0] = gload16s<0>(voff, sp); st[1] = gload16s<1024>(voff, sp);
    }
    if (i == 8) {
      if (GU) {
#pragma unroll
        for (int k = 0; k < NP8; ++k) ld_dn_b(sm, ws, tnd, lw, voff, k);
      }
      float2* rowst = (float2*)(smem + L_SMALL);
      float* colsum = (float*)(smem + L_SMALL + 1024);
      float cs = 0.f;
#pragma unroll
      for (int k = 0; k < 4; ++k) { asm volatile("" : "+v"(c[k])); cs += __uint_as_float(c[k]); }
      asm volatile("" : "+v"(st[0]), "+v"(st[1]));
      const f32x4 s0 = __builtin_bit_cast(f32x4, st[0]), s1 = __builtin_bit_cast(f32x4, st[1]);
      const bool od = t & 1;
      float sm1 = od ? s1[0] + s1[2] : s0[0] + s0[2], sq1 = od ? s1[1] + s1[3] : s0[1] + s0[3];
      sm1 += qx<0xB1>(od ? s0[0] + s0[2] : s1[0] + s1[2]); sq1 += qx<0xB1>(od ? s0[1] + s0[3] : s1[1] + s1[3]);
      sm1 += qx<0x4E>(sm1); sq1 += qx<0x4E>(sq1);
      const float mean = sm1 * (1.0f / C::KTOT);
      const float var = fmaxf(sq1 * (1.0f / C::KTOT) - mean * mean, 0.f);
      if (t < BN) colsum[t] = cs;
      if (!(t & 2)) rowst[lw * 32 + (t & 1) * 16 + ((t & 63) >> 2)] = make_float2(mean, rsqrtf(var + LN_EPS));
    }
  });
  if (GU) {
#pragma unroll
    for (int k = NP8; k < 4; ++k) ld_dn_b<THG>(sm, ws, tnd, lw, voff, k);
    static_for<35>([&](auto N) {
      constexpr int n = decltype(N)::v;
      R[n] = gload16n<(n % 5) * 1024 - 2048>(voff, dn_src(ws, tnd, lw, 4 + n / 5) + 2048);
      if (THD) wait_vm<(THD ? THD - 1 : 0)>();
    });
  }
}

DI void loader_dn(char* smem, const char* ws, bool hasq, int bid, int tid, u32x4 (&R)[35]) {
  const int lw = __builtin_amdgcn_readfirstlane(tid >> 6) - 4;
  const unsigned voff = (tid & 63) * 16, sm = (unsigned)(uintptr_t)smem;
  int tm, tn; tile_of<CDN>(bid, tm, tn);
  int tmq, tnq; tile_of<CQ1>(bid, tmq, tnq);
  auto put = [&](auto N) { constexpr int n = decltype(N)::v; dsw16<(n % 5) * 1024>(dn_dst(sm, lw, 4 + n / 5) + voff, R[n]); };
  static_for<14>([&](auto P) {
    constexpr int p = decltype(P)::v;
    if (p == 0) wait_vm<50>();
    if (p >= 11) wait_vm<(p >= 11 ? 65 - 5 * p : 0)>();
#define PUTS 0, 15, 15, 18, 21, 24, 27, 30, 33, 35
#define LZY 1
    constexpr int PC[10] = {PUTS};
    asm volatile("s_waitcnt lgkmcnt(%0)\n\ts_barrier" ::"i"(LZY && p >= 1 && p <= 9 ? PC[p <= 9 ? p : 0] - PC[p >= 1 && p <= 9 ? p - 1 : 0] : 0) : "memory");
    if (p <= 5) wait_vm<(p < 5 ? 35 - PC[p < 9 ? p + 1 : 0] : 0)>();
#define EXA 0
    if (p >= 6 && p <= 8 && !(EXA & 8)) ld_dn_b(sm, ws, tn, lw, voff, p + 5);
    if constexpr (p <= 8 && !((EXA & 4) && p > 0)) static_for<PC[p + 1], PC[p]>(put);
  });
  raw_barrier();
  if (hasq) {
    __builtin_amdgcn_s_sleep(SDL);
#pragma unroll
    for (int k = 0; k < 4; ++k) ld_q_b<THN>(sm, ws, tnq, lw, voff, k, k);
  }
}

template <int NW> DI void publish(char* smem, char* ws, int seam, int tile, int tid) {
  wait_vm<0>();
  if ((tid & 63) == 0) {
    const unsigned old = __hip_atomic_fetch_add((unsigned*)(smem + L_CNT), 8u / NW, __ATOMIC_RELAXED, __HIP_MEMORY_SCOPE_WORKGROUP);
    if (((old + 8u / NW) & 7) == 0) __hip_atomic_store((unsigned*)(ws + WS_FLG) + (seam * 256 + tile) * 32, 1u, __ATOMIC_RELAXED, __HIP_MEMORY_SCOPE_AGENT);
  }
}
DI void await(const char* ws, int seam, int tile) {
  const unsigned* p = (const unsigned*)(ws + WS_FLG) + (seam * 256 + tile) * 32;
  unsigned v = tile < 0 ? 1u : 0u;
  for (int s = 0; s < (1 << 18); ++s) {
    if (v != 1u) v = __hip_atomic_load(p, __ATOMIC_RELAXED, __HIP_MEMORY_SCOPE_AGENT);
    if (__builtin_amdgcn_ballot_w64(v != 1u) == 0) break;
    __builtin_amdgcn_s_sleep(SLP);
  }
}

template <class C, int MODE> DI void compute_res(char* smem, const char* A, const char* res, bool res_raw, char* out, bool out_raw, char* ws, size_t stat, int seam, int bid, int tid, int KID) {
  constexpr int KT = C::KT, NT = C::NT;
  const int lane = tid & 63, wid = __builtin_amdgcn_readfirstlane(tid >> 6), fr = lane & 15, fg = lane >> 4, wm = wid >> 1, wn = wid & 1;
  int tm, tn; tile_of<C>(bid, tm, tn);
  const int m0 = tm * C::BM, n0 = tn * C::BN;
  f32x4 acc[2][NT];
#pragma unroll
  for (int i = 0; i < 2; ++i)
#pragma unroll
    for (int j = 0; j < NT; ++j) acc[i][j] = (f32x4){0.f, 0.f, 0.f, 0.f};
  const int row = m0 + wid * 16 + fr;
  u32x2 resv[NT];
#pragma unroll
  for (int j = 0; j < NT; ++j) {
    const int col = n0 + j * 16 + fg * 4;
    if (MODE == 0) resv[j] = *(const u32x2*)(res + (res_raw ? (unsigned)(row * DM + col) * 2 : foff<10>(row, col)));
    else resv[j] = *(const u32x2*)(smem + L_XHI + wid * 3072 + j * 512 + lane * 8);
  }
#define DA 9
  constexpr int D = MODE == 0 ? 11 : DA;
  const char* pa = A + (size_t)(tm * KT * 8 + wm * 4 + wn) * 1024;
  const unsigned voff = lane * 16;
  unsigned pb[3];
#pragma unroll
  for (int i = 0; i < 3; ++i) { pb[i] = (unsigned)(uintptr_t)smem + wn * 1024 + lane * 16 + i * 65536; asm volatile("" : "+v"(pb[i])); }
#define LB 2
  constexpr int LA = MODE == 0 ? 1 : LB, NB = LA + 1;
  u32x4 ar[D][2];
  bf16x8 b[NB][NT];
  constexpr int ORD[10] = {4, 5, 0, 1, 2, 3, 6, 7, 8, 9};
  auto lda = [&](auto S, auto I) {
    constexpr int s = decltype(S)::v, i = decltype(I)::v, st = MODE == 0 ? ORD[s % 10] : s;
    ar[s % D][i] = gload16s<i * 2048>(voff, pa + st * 8192);
    SB();
  };
  auto rdb = [&](auto S, auto J) {
    constexpr int s = decltype(S)::v, j = decltype(J)::v, o = (MODE == 0 ? wo_b(ORD[s % 10]) : dn_b(s)) + j * 2048;
    b[s % NB][j] = lds16<o & 0xffff>(pb[o >> 16]);
    SB();
  };
  if (wid == 1) await(ws, seam, MODE == 0 ? (lane < 32 ? (tm >> 2) * 32 + lane : -1) : (lane < 16 ? (tm >> 1) * 16 + lane : -1));
  raw_barrier();
  STAMP(KID, 1);
  static_for<D - 1>([&](auto S) { lda(S, IC<0>{}); lda(S, IC<1>{}); });
  STAMP(KID, 2); CSTAMP(KID, 2);
  static_for<LA * NT>([&](auto J) { rdb(IC<decltype(J)::v / NT>{}, IC<decltype(J)::v % NT>{}); });
  static_for<KT>([&](auto S) {
    constexpr int s = decltype(S)::v, n = (s + D - 2 < KT ? s + D - 2 : KT - 1) - s;
    constexpr bool nx = s + LA < KT;
    constexpr int ahead = ((s + LA - 1 < KT ? s + LA - 1 : KT - 1) - s) * NT;
    if (MODE == 1 ? nx && !((s + LA) & 1) : s == 2) { __builtin_amdgcn_s_barrier(); SB(); }
    asm volatile("s_waitcnt vmcnt(%2)" : "+v"(ar[s % D][0]), "+v"(ar[s % D][1]) : "i"(2 * n) : "memory");
    static_for<2 * NT>([&](auto M) {
      constexpr int m = decltype(M)::v, j = m >> 1, i = m & 1;
      if (i == 0) asm volatile("s_waitcnt lgkmcnt(%1)" : "+v"(b[s % NB][j]) : "i"(NT - 1 - j + ahead + (nx ? (m < NT ? m : NT) : 0)));
      acc[i][j] = __builtin_amdgcn_mfma_f32_16x16x32_bf16(b[s % NB][j], __builtin_bit_cast(bf16x8, ar[s % D][i]), acc[i][j], 0, 0, 0);
      SB();
      if constexpr (nx && m < NT) rdb(IC<s + LA>{}, M);
      if constexpr (s + D - 1 < KT && (m == 5 || m == 7)) lda(IC<s + D - 1>{}, IC<(m == 7)>{});
    });
  });
  STAMP(KID, 3); CSTAMP(KID, 3);
#pragma unroll
  for (int j = 0; j < NT; ++j) *(f32x4*)(smem + xb_off(wid ^ 1, j) + lane * 16) = wn ? acc[0][j] : acc[1][j];
  raw_barrier();
  STAMP(KID, 4);
  float s = 0.f, q = 0.f;
  u32x2 v[NT];
#pragma unroll
  for (int j = 0; j < NT; ++j) {
    const f32x4 o = *(const f32x4*)(smem + xb_off(wid, j) + lane * 16) + (wn ? acc[1][j] : acc[0][j]);
    const u32x2 r = resv[j];
    v[j] = (u32x2){cvtpk(o[0] + bflo(r[0]), o[1] + bfhi(r[0])), cvtpk(o[2] + bflo(r[1]), o[3] + bfhi(r[1]))};
    const float e0 = bflo(v[j][0]), e1 = bfhi(v[j][0]), e2 = bflo(v[j][1]), e3 = bfhi(v[j][1]);
    s += (e0 + e1) + (e2 + e3);
    q += (e0 * e0 + e1 * e1) + (e2 * e2 + e3 * e3);
  }
#pragma unroll
  for (int jp = 0; jp < NT; jp += 2) {
    const int j1 = jp + 1 < NT ? jp + 1 : jp;
    const u32x2 x = swap16(v[jp][0], v[j1][0]), y = swap16(v[jp][1], v[j1][1]);
    const int col = n0 + (jp + (fg & 1)) * 16 + (fg >> 1) * 8;
    if (jp + 1 < NT || !(fg & 1))
      st16sc(out + (out_raw ? (unsigned)(row * DM + col) * 2 : foff<10>(row, col)), (u32x4){x[0], y[0], x[1], y[1]});
  }
  {
    const u32x2 t1 = swap16(__float_as_uint(s), __float_as_uint(q));
    const unsigned t2 = __float_as_uint(__uint_as_float(t1[0]) + __uint_as_float(t1[1]));
    const u32x2 t3 = swap32(t2, t2);
    if (lane < 32) st4sc(ws + stat + (size_t)(row * 8 + tn) * 8 + fg * 4, __uint_as_float(t3[0]) + __uint_as_float(t3[1]));
  }
  if (MODE == 0) {
#pragma unroll
    for (int j = 0; j < NT; ++j) *(u32x2*)(smem + L_XHI + wid * 3072 + j * 512 + lane * 8) = v[j];
  }
  STAMP(KID, 5);
  if (!out_raw) publish<4>(smem, ws, seam + 1, tm * 8 + tn, tid);
  STAMP(KID, 6);
}

template <class C> DI void compute_ln(char* smem, char* out, char* ws, int seam, int bid, int tid, int KID) {
  constexpr int KT = C::KT, NST = C::NST, NT = C::NT, MT = C::MT, RS = C::RS, BN = C::BN, EPI = C::EPI, KTOT = C::KTOT;
  constexpr bool RAW = C::RAW;
  constexpr unsigned OQ = (unsigned)(WS_Q + (RAW ? 0 : Q_SZ)), OKV = (unsigned)(WS_KV + (RAW ? 0 : KV_SZ));
  const int lane = tid & 63, wid = __builtin_amdgcn_readfirstlane(tid >> 6), fr = lane & 15, fg = lane >> 4, wm = wid >> 1, wn = wid & 1;
  int tm, tn; tile_of<C>(bid, tm, tn);
  const int m0 = tm * C::BM, n0 = tn * BN;
  float2* rowst = (float2*)(smem + L_SMALL);
  float* colsum = (float*)(smem + L_SMALL + 1024);
  const char* dummy = smem + L_SMALL + 2048;
  f32x4 acc[MT][NT];
#pragma unroll
  for (int i = 0; i < MT; ++i)
#pragma unroll
    for (int j = 0; j < NT; ++j) acc[i][j] = (f32x4){0.f, 0.f, 0.f, 0.f};
  if (RAW && tn >= 10) {
    const int vc0 = n0 - 2 * DM, head = (vc0 + DH - 1) / DH + (tid >> 4);
    if (head * DH < vc0 + BN) {
      const int tok = m0 + ((tid >> 1) & 7) * 16;
      char* p = ws + OKV + (size_t)((head >> 1) * 16 + (tok >> 7)) * KVP_CHUNK + (head & 1) * KVP_VO + ((tok >> 4) & 7) * VFR + ((tid & 1) * 24 + DH) * 16;
#pragma unroll
      for (int l = 0; l < 2; ++l) {
        *(u32x4*)(p + l * KV_SZ) = (u32x4){0x3f803f80u, 0x3f803f80u, 0x3f803f80u, 0x3f803f80u};
#pragma unroll
        for (int i = 1; i < 4; ++i) *(u32x4*)(p + l * KV_SZ + 16 * i) = (u32x4){0u, 0u, 0u, 0u};
      }
    }
  }
  int aoff[2], boff[RAW ? NT : 1];
  const int pr = 8 * (fg >> 1) + 4 * (fg & 1) + (fr >> 2);
#pragma unroll
  for (int ks = 0; ks < 2; ++ks)
    aoff[ks] = RAW ? (wm * 64 + fr) * 128 + (((ks * 4 + fg) ^ (fr >> 1)) * 16) : (wm * 8 + ks) * 1024 + lane * 16;
  if (RAW) {
#pragma unroll
    for (int j = 0; j < NT; ++j) boff[j] = pr * RS + ((wn * NT + j) ^ (pr & 7)) * 32 + (lane & 3) * 8;
  } else boff[0] = wn * NT * 2048 + lane * 16;
  f32x4 srow[2], sgr[2], scol[2];
#pragma unroll
  for (int i = 0; i < 2; ++i) srow[i] = sgr[i] = scol[i] = (f32x4){0.f, 0.f, 0.f, 0.f};
  const bf16x8 ones = (bf16x8){0x3f80, 0x3f80, 0x3f80, 0x3f80, 0x3f80, 0x3f80, 0x3f80, 0x3f80};

  const bool swp = !(EPI == EPI_QKV && tn >= 10);
  unsigned so[2][2];
  if (EPI == EPI_QKV) {
    const bool isq = tn < DM / BN;
#pragma unroll
    for (int jq = 0; jq < 2; ++jq) {
      if (swp) {
        const int G = ((isq ? n0 : n0 - DM) >> 3) + (wn * NT + jq * 2 + (fg & 1)) * 2 + (fg >> 1), pair = G / 5, gi = G % 5;
        const int mo = (((gi + (gi > 2)) & 1) * 32 + fr) * 16;
        if (isq) {
          so[jq][0] = OQ + (pair * 64 + (m0 >> 5) + wm * 2) * 2560 + (gi == 2 ? 2048 + fr * 16 : (gi > 2) * 1024 + mo);
          so[jq][1] = so[jq][0] + 2560;
        } else {
          so[jq][0] = OKV + (pair * 16 + (m0 >> 7)) * KVP_CHUNK + (gi == 2 ? KVP_TAIL + (wm * 64 + fr) * 16 : (gi > 2 ? KVP_KO : KVP_KE) + wm * 2048 + mo);
          so[jq][1] = so[jq][0] + (gi == 2 ? 512 : 1024);
        }
      } else {
        const int cc = n0 - 2 * DM + (wn * NT + jq * 2 + (lane >> 5)) * 16 + fr, head = cc / DH, d = cc % DH;
        const int vr = (head & 1) ? (d < 4 ? d + 16 : d - 4) : d;
        so[jq][0] = OKV + ((head >> 1) * 16 + (m0 >> 7)) * KVP_CHUNK + (head & 1) * KVP_VO + wm * 4 * VFR + ((fg & 1) * 24 + vr) * 16;
        so[jq][1] = so[jq][0] + 2 * VFR;
      }
      asm volatile("" : "+v"(so[jq][0]), "+v"(so[jq][1]));
    }
  }
  if (!RAW) {
    if (wid == 1) await(ws, seam, lane < 16 ? tm * 16 + lane : -1);
    raw_barrier();
  }
  auto mainloop = [&](auto SW, auto WN, auto WM) {
    constexpr bool SWP = decltype(SW)::v;
    constexpr int CWN = decltype(WN)::v, CWM = decltype(WM)::v;
    bf16x8 a0[MT], a1[MT], b0[NT], b1[NT];
    auto rdB = [&](const char* base, int j, int ks) {
      if (RAW) {
        const char* p = base + boff[RAW ? j : 0] + ks * 32 * RS;
        bf16x4 lo = tr_read(p), hi = tr_read(p + 16 * RS);
        return __builtin_shufflevector(lo, hi, 0, 1, 2, 3, 4, 5, 6, 7);
      } else return *(const bf16x8*)(base + boff[0] + j * 2048 + ks * 1024);
    };
    auto step = [&](bf16x8(&ac)[MT], bf16x8(&bc)[NT], bf16x8(&an)[MT], bf16x8(&bn)[NT], const char* na, const char* nb, int nks, auto RD) {
      constexpr bool rd = decltype(RD)::v;
      constexpr int RPG = 3;
#pragma unroll
      for (int j = 0; j < NT; ++j) {
#pragma unroll
        for (int i = 0; i < MT; ++i) {
          acc[i][j] = SWP ? __builtin_amdgcn_mfma_f32_16x16x32_bf16(bc[j], ac[i], acc[i][j], 0, 0, 0)
                          : __builtin_amdgcn_mfma_f32_16x16x32_bf16(ac[i], bc[j], acc[i][j], 0, 0, 0);
          const int r = j * RPG + i;
          if (rd && i < RPG && r < MT + NT) {
            if (r == 0) bn[0] = rdB(nb, 0, nks);
            else if (r <= MT) an[r - 1] = *(const bf16x8*)(na + aoff[nks] + (r - 1) * 2048);
            else bn[r - MT] = rdB(nb, r - MT, nks);
            SB();
          }
        }
        if (RAW) {
          if ((j >> 1) == CWM) scol[j & 1] = __builtin_amdgcn_mfma_f32_16x16x32_bf16(bc[j], ones, scol[j & 1], 0, 0, 0);
          else {
            srow[j & 1] = __builtin_amdgcn_mfma_f32_16x16x32_bf16(ones, ac[CWN * 2 + (j & 1)], srow[j & 1], 0, 0, 0);
            sgr[j & 1] = __builtin_amdgcn_mfma_f32_16x16x32_bf16(ac[CWN * 2 + (j & 1)], ac[CWN * 2 + (j & 1)], sgr[j & 1], 0, 0, 0);
          }
        }
        SB();
      }
    };
    STAMP(KID, 1);
    raw_barrier();
    STAMP(KID, 2); CSTAMP(KID, 2);
#pragma unroll
    for (int j = 0; j < NT; ++j) { b0[j] = rdB(smem + C::bslot(0), j, 0); if (j < MT) a0[j] = *(const bf16x8*)(smem + C::aslot(0) + aoff[0] + j * 2048); }
    step(a0, b0, a1, b1, smem + C::aslot(0), smem + C::bslot(0), 1, IC<1>{});
    int buf = 0;
#pragma unroll 1
    for (int kt = 1; kt < KT; ++kt) {
      if (++buf == NST) buf = 0;
      const char *na = smem + C::aslot(buf), *nb = smem + C::bslot(buf);
      raw_barrier();
      step(a1, b1, a0, b0, na, nb, 0, IC<1>{});
      step(a0, b0, a1, b1, na, nb, 1, IC<1>{});
    }
    step(a1, b1, a0, b0, smem, smem, 0, IC<0>{});
  };
  if (RAW) {
    if (swp) {
      if (wid == 0) mainloop(IC<1>{}, IC<0>{}, IC<0>{}); else if (wid == 1) mainloop(IC<1>{}, IC<1>{}, IC<0>{});
      else if (wid == 2) mainloop(IC<1>{}, IC<0>{}, IC<1>{}); else mainloop(IC<1>{}, IC<1>{}, IC<1>{});
    } else {
      if (wid == 0) mainloop(IC<0>{}, IC<0>{}, IC<0>{}); else if (wid == 1) mainloop(IC<0>{}, IC<1>{}, IC<0>{});
      else if (wid == 2) mainloop(IC<0>{}, IC<0>{}, IC<1>{}); else mainloop(IC<0>{}, IC<1>{}, IC<1>{});
    }
    STAMP(KID, 3); CSTAMP(KID, 3);
    wait_vm<0>();
#pragma unroll
    for (int i = 0; i < 2; ++i) {
      const float sq = (fr & 2) ? ((fr & 1) ? sgr[i][3] : sgr[i][2]) : ((fr & 1) ? sgr[i][1] : sgr[i][0]);
      const float mean = srow[i][0] * (1.0f / KTOT);
      const float var = fmaxf(sq * (1.0f / KTOT) - mean * mean, 0.f);
      if (fg == (fr >> 2)) rowst[wm * 64 + (wn * 2 + i) * 16 + fr] = make_float2(mean, rsqrtf(var + LN_EPS));
      if (fr == 0) *(f32x4*)(colsum + (wn * NT + wm * 2 + i) * 16 + fg * 4) = scol[i];
    }
    raw_barrier();
  } else {
    if (swp) mainloop(IC<1>{}, IC<0>{}, IC<0>{}); else mainloop(IC<0>{}, IC<0>{}, IC<0>{});
    STAMP(KID, 3); CSTAMP(KID, 3);
  }
  STAMP(KID, 4);

  if constexpr (EPI == EPI_GEGLU) {
    const int hbase = (tn >> 1) * 216 + (tn & 1) * 112;
    const int orow = m0 + wm * 64 + fg * 16 + fr;
    constexpr float C1 = -2.0f * 0.7978845608028654f * 1.4426950408889634f, C2 = C1 * 0.044715f;
    f32x2 nm[MT], k1[MT], k2[MT], iv[MT];
#pragma unroll
    for (int i = 0; i < MT; ++i) {
      const float2 ms = rowst[wm * 64 + i * 16 + fr];
      const float r2 = ms.y * ms.y, ir2 = __builtin_amdgcn_rcpf(r2);
      nm[i] = (f32x2){-ms.x, -ms.x}; k1[i] = (f32x2){C1 * ms.y, C1 * ms.y}; k2[i] = (f32x2){C2 * ms.y * r2, C2 * ms.y * r2}; iv[i] = (f32x2){ir2, ir2};
    }
    f32x2 cgs[NT], cus[NT];
#pragma unroll
    for (int j = 0; j < NT; ++j) { cgs[j] = *(const f32x2*)(colsum + (wn * NT + j) * 16 + fg * 2); cus[j] = *(const f32x2*)(colsum + (wn * NT + j) * 16 + fg * 2 + 8); }
    const unsigned ro = foff<27>(orow, 0);
#pragma unroll
    for (int j = 0; j < NT; ++j) {
      const f32x2 cg = cgs[j], cu = cus[j];
      unsigned d[MT];
#pragma unroll
      for (int i = 0; i < MT; ++i) {
        const f32x2 gg = __builtin_elementwise_fma(nm[i], cg, (f32x2){acc[i][j][0], acc[i][j][1]});
        const f32x2 uu = __builtin_elementwise_fma(nm[i], cu, (f32x2){acc[i][j][2], acc[i][j][3]});
        const f32x2 t = gg * __builtin_elementwise_fma(gg * gg, k2[i], k1[i]);
        const f32x2 e = (f32x2){__builtin_amdgcn_exp2f(t[0]), __builtin_amdgcn_exp2f(t[1])};
        const f32x2 q = __builtin_elementwise_fma(e, iv[i], iv[i]);
        const f32x2 h = (gg * uu) * (f32x2){__builtin_amdgcn_rcpf(q[0]), __builtin_amdgcn_rcpf(q[1])};
        d[i] = cvtpk(h[0], h[1]);
      }
      const u32x2 x = swap32(d[0], d[2]), y = swap32(d[1], d[3]);
      const u32x2 z = swap16(x[0], y[0]), w = swap16(x[1], y[1]);
      const int jb = wn * NT + j;
      if (jb < 13 || !(tn & 1)) stw<0>(out, ro + foff<27>(0, hbase + jb * 8), (u32x4){z[0], z[1], w[0], w[1]});
    }
  } else {
    if (swp) {
      const float sc = tn < DM / BN ? QSCALE : 1.0f;
      float2 ms[MT]; f32x4 cs[NT];
#pragma unroll
      for (int i = 0; i < MT; ++i) ms[i] = rowst[wm * 64 + i * 16 + fr];
#pragma unroll
      for (int j = 0; j < NT; ++j) cs[j] = *(const f32x4*)(colsum + (wn * NT + j) * 16 + fg * 4);
      static_for<8>([&](auto I) {
        constexpr int jq = decltype(I)::v >> 2, i = decltype(I)::v & 3;
        const float rs = ms[i].y * sc;
        const f32x4 o0 = rs * (acc[i][jq * 2] - ms[i].x * cs[jq * 2]), o1 = rs * (acc[i][jq * 2 + 1] - ms[i].x * cs[jq * 2 + 1]);
        const u32x2 x = swap16(cvtpk(o0[0], o0[1]), cvtpk(o1[0], o1[1])), y = swap16(cvtpk(o0[2], o0[3]), cvtpk(o1[2], o1[3]));
        stw<(i & 1) * 256>(ws, so[jq][i >> 1], (u32x4){x[0], y[0], x[1], y[1]});
      });
    } else {
      f32x4 ma[MT], mb[MT]; float cs[NT];
#pragma unroll
      for (int i = 0; i < MT; ++i) { ma[i] = *(const f32x4*)(rowst + wm * 64 + i * 16 + fg * 4); mb[i] = *(const f32x4*)(rowst + wm * 64 + i * 16 + fg * 4 + 2); }
#pragma unroll
      for (int j = 0; j < NT; ++j) cs[j] = colsum[(wn * NT + j) * 16 + fr];
      static_for<8>([&](auto I) {
        constexpr int jq = decltype(I)::v >> 2, i = decltype(I)::v & 3;
        const float c0 = cs[jq * 2], c1 = cs[jq * 2 + 1];
        const f32x4 &p0 = acc[i][jq * 2], &p1 = acc[i][jq * 2 + 1], &a = ma[i], &b = mb[i];
        const u32x2 x = swap32(cvtpk(a[1] * (p0[0] - a[0] * c0), a[3] * (p0[1] - a[2] * c0)), cvtpk(a[1] * (p1[0] - a[0] * c1), a[3] * (p1[1] - a[2] * c1)));
        const u32x2 y = swap32(cvtpk(b[1] * (p0[2] - b[0] * c0), b[3] * (p0[3] - b[2] * c0)), cvtpk(b[1] * (p1[2] - b[0] * c1), b[3] * (p1[3] - b[2] * c1)));
        stw<(i & 1) * VFR>(ws, so[jq][i >> 1], (u32x4){x[0], y[0], x[1], y[1]});
      });
    }
  }
  STAMP(KID, 5);
  if (!RAW) publish<4>(smem, ws, seam + 1, tm * 16 + tn, tid);
  STAMP(KID, 6);
}

constexpr int ATT_NBUF = 4, ATT_HALF = 12288, ATT_STB = 2 * ATT_HALF;
constexpr int ATT_PK = ATT_NBUF * ATT_STB, ATT_X = 45056;
struct MegaArgs { char* ws; const char* x; char* out; const u16* wqkv; const u16* wg; const u16* wu; const u16* wd; };

#define EX(x) __builtin_amdgcn_exp2f(x)
#define E4(P, I, S, J) P[I] = cvtpk(EX(S[J]), EX(S[J + 1])); P[I + 1] = cvtpk(EX(S[J + 2]), EX(S[J + 3]));
#define MF16(a, b, c) __builtin_amdgcn_mfma_f32_32x32x16_bf16(a, b, c, 0, 0, 0)
#define MF8(a, b, c) __builtin_amdgcn_mfma_f32_32x32x8bf16_1k(a, b, c, 0, 0, 0)
#define BC8(x) __builtin_bit_cast(bf16x8, x)

DI float rowmax16(const f32x16& s) {
  float m = s[0];
#pragma unroll
  for (int i = 1; i < 16; ++i) m = fmaxf(m, s[i]);
  return fmaxf(m, __shfl_xor(m, 32));
}
DI bool nonfinite12(const f32x16& o, int lane) {
  bool bad = false;
#pragma unroll
  for (int i = 0; i < 9; ++i) bad |= !(fabsf(o[i]) < 1.0e38f);
#pragma unroll
  for (int i = 9; i < 12; ++i) bad |= (lane < 32) && !(fabsf(o[i]) < 1.0e38f);
  return bad;
}

struct PackJob { const u16* src; int ld, kk0, kmax, s2; char* dst; float* cs; };
constexpr int NJOB = 223;
DI PackJob pack_job(const MegaArgs& a, int J, int wid, int lane) {
  const int prow = wid * 4 + (lane >> 4), c16 = lane & 15, ch = (c16 >> 1) ^ (prow & 7);
  const int k = 8 * (((prow >> 3) & 1) * 2 + ((prow >> 2) & 1)) + 4 * ((prow >> 4) & 1) + (prow & 3);
  const int fr = lane & 15, fg = lane >> 4;
  PackJob r;
  if (J >= NJOB) J -= NJOB;
  r.kmax = 19;
  if (J < 108) {
    const int cg = J >> 2, kq = J & 3, b = cg * 8 + wid, rr = b % 27, tn = (b / 27) * 2 + (rr >= 14), jb = rr - (rr >= 14) * 14;
    r.src = ((c16 & 1) ? a.wu : a.wg) + (size_t)k * DF + cg * 64 + ch * 8; r.ld = DF; r.kk0 = kq * 5; r.s2 = 28;
    r.dst = a.ws + WS_PGU + (size_t)(tn * 280 + jb * 2) * 1024 + (fg * 16 + ((fr & 7) >> 1) * 4 + (fr >> 3) * 2 + (fr & 1)) * 16;
    r.cs = (float*)(a.ws + WS_CGU) + (tn * 4 + kq) * 224 + jb * 16 + fg * 4;
  } else if (J < 168) {
    const int tn = (J - 108) >> 2, kq = (J - 108) & 3;
    r.src = a.wqkv + (size_t)k * (3 * DM) + tn * 128 + ch * 16 + (c16 & 1) * 8; r.ld = 3 * DM; r.kk0 = kq * 5; r.s2 = 16;
    r.dst = a.ws + WS_PQKV + (size_t)(tn * 160 + wid * 2) * 1024 + lane * 16;
    r.cs = (float*)(a.ws + WS_CQKV) + (tn * 4 + kq) * 128 + wid * 16 + fg * 4;
  } else {
    const int cg = (J - 168) / 11, kq = (J - 168) % 11, jb = cg * 8 + wid;
    r.src = a.wd + (size_t)k * DM + cg * 128 + ch * 16 + (c16 & 1) * 8; r.ld = DM; r.kk0 = kq * 5; r.s2 = 10;
    r.kmax = 53;
    r.dst = a.ws + WS_PWD + (size_t)((jb / 5) * 270 + (jb % 5) * 2) * 1024 + lane * 16;
    r.cs = (float*)(a.ws + WS_PFLG + 2048) + wid * 16 + fg * 4;
  }
  return r;
}

template <int OFF> DI bf16x4 lds8(unsigned a) { bf16x4 r; asm volatile("ds_read_b64 %0, %1 offset:%2" : "=v"(r) : "v"(a), "i"(OFF)); return r; }
template <int OFF> DI bf16x4 ldstr(unsigned a) { bf16x4 r; asm volatile("ds_read_b64_tr_b16 %0, %1 offset:%2" : "=v"(r) : "v"(a), "i"(OFF)); return r; }
DI u32x2 gload8sc(const void* g) {
  u32x2 r; asm volatile("global_load_dwordx2 %0, %1, off sc1" : "=v"(r) : "v"(g) : "memory"); return r;
}
DI void st8sc(void* p, u32x2 v) { asm volatile("global_store_dwordx2 %0, %1, off sc1" ::"v"(p), "v"(v) : "memory"); }
DI void st4m(void* p, unsigned v, unsigned m) {
  asm volatile("s_mov_b32 exec_lo, %2\n\ts_mov_b32 exec_hi, 0\n\tglobal_store_dword %0, %1, off sc1\n\ts_mov_b64 exec, -1" ::"v"(p), "v"(v), "s"(m) : "memory");
}
DI void st16m(void* p, f32x4 v) {
  asm volatile("s_mov_b32 exec_lo, 0x10001\n\ts_mov_b32 exec_hi, 0x10001\n\tglobal_store_dwordx4 %0, %1, off sc1\n\ts_mov_b64 exec, -1\n\ts_nop 0" ::"v"(p), "v"(v) : "memory");
}

template <bool PACK> DI void attn_phase(char* smem, const MegaArgs& a, char* ws, int bid, int tid) {
  constexpr int LAYER = PACK ? 0 : 1;
  constexpr bool SC = !PACK;
  const int lane = tid & 63, wid = __builtin_amdgcn_readfirstlane(tid >> 6);
  STAMP(KID, 0);
  const int xcd = bid & 7, idx = bid >> 3;
  const int head = xcd * 4 + (idx >> 3), qb = idx & 7, odd = head & 1;
  const int r32 = lane & 31, hh = lane >> 5, half = wid >> 2;
  const int q0 = qb * 256 + (wid & 3) * 64 + r32;
  const char* kvb = uni(ws + WS_KV + LAYER * KV_SZ + (size_t)(head >> 1) * 16 * KVP_CHUNK);
  const unsigned sm = (unsigned)(uintptr_t)smem;
  const unsigned voff = half * 8 * KVP_CHUNK + odd * KVP_TAIL + (wid & 3) * 3072 + lane * 16;
  const unsigned mb = sm + half * ATT_HALF + (wid & 3) * 3072;
  auto stage = [&](int s, int buf) {
    const char* gs = kvb + s * KVP_CHUNK; const unsigned gm = mb + buf * ATT_STB;
    gld<0, SC>(voff, gs, gm); gld<1024, SC>(voff, gs, gm); gld<2048, SC>(voff, gs, gm);
  };
  if (!PACK) {
    const int c = head * DH + (lane & 16 ? DH - 1 : 0);
    if (wid == 0) await(ws, 4, (lane & 15) * 16 + ((c + (lane & 32 ? 2 * DM : DM)) >> 7));
    if (wid == 1) await(ws, 4, (lane & 46) == 0 ? (qb * 2 + (lane & 1)) * 16 + (c >> 7) : -1);
    raw_barrier();
  }
  STAMP(KID, 1);
  u32x4 qr1[2]; u32x2 qr2[2];
  {
    const char* Q = ws + WS_Q + LAYER * Q_SZ + (size_t)((head >> 1) * 64 + (q0 >> 5)) * 2560;
#pragma unroll
    for (int i = 0; i < 2; ++i) {
      qr1[i] = gload16sc(Q + i * 2560 + odd * 1024 + lane * 16);
      qr2[i] = gload8sc(Q + i * 2560 + 2048 + r32 * 16 + odd * 8);
    }
  }
  stage(0, 0);
  if (!AST) { stage(1, 1); stage(2, 2); }
  int tmw, tnw; tile_of<CWO>(bid, tmw, tnw);
  const char* wb = uni(ws + WS_PWO + (size_t)tnw * 102400);
  const unsigned wv = lane * 16, wm1 = sm + (wid < 6 ? WO_BHI + wid * 10240 : (wid - 6) * 10240), wm2 = sm + 20480 + wid * 3072;
  auto wpre = [&](auto K) {
    constexpr int k = decltype(K)::v;
    if (k < 10) gld<(k & 3) * 1024, false>(wv, wb + wid * 10240 + (k >> 2) * 4096, wm1 + (k >> 2) * 4096);
    else gld<(k < 10 ? 0 : k - 10) * 1024, false>(wv, wb + 81920 + wid * 3072, wm2);
    SB();
  };
  const unsigned m_fl = 1u >> wid;
  PackJob pj; unsigned pp; bf16x4 plo, phi; f32x4 pcs = (f32x4){0.f, 0.f, 0.f, 0.f};
  if (PACK) {
    pj = pack_job(a, bid, wid, lane);
    const int pr = 8 * (lane >> 5) + 4 * ((lane >> 4) & 1) + ((lane & 15) >> 2);
    pp = sm + ATT_PK + pr * 256 + (wid ^ (pr & 7)) * 32 + (lane & 3) * 8;
  }
  unsigned fa[2][3];
  fa[0][0] = sm + half * ATT_HALF + (odd ? KVP_KO - KVP_TAIL : KVP_KE) + lane * 16;
  fa[0][1] = sm + half * ATT_HALF + (odd ? 0 : KVP_TAIL) + r32 * 16 + odd * 8;
  fa[0][2] = sm + half * ATT_HALF + (odd ? KVP_VO - KVP_TAIL : 0) + (hh * 24 + min(r32, 23)) * 16;
#pragma unroll
  for (int i = 0; i < 3; ++i) { fa[1][i] = fa[0][i] + 2 * ATT_STB; asm volatile("" : "+v"(fa[0][i]), "+v"(fa[1][i])); }
  f32x16 oa, ob, nma, nmb, sa, sb;
#pragma unroll
  for (int i = 0; i < 16; ++i) { oa[i] = 0.f; ob[i] = 0.f; }
  wait_vm<AST ? 0 : 6>();
  if (AST) { stage(1, 1); stage(2, 2); }
#pragma unroll
  for (int i = 0; i < 2; ++i) asm volatile("" : "+v"(qr1[i]), "+v"(qr2[i]));
  const bf16x8 qa1 = BC8(qr1[0]), qb1 = BC8(qr1[1]);
  const bf16x4 qa2 = __builtin_bit_cast(bf16x4, hh ? (u32x2){0u, 0u} : qr2[0]), qb2 = __builtin_bit_cast(bf16x4, hh ? (u32x2){0u, 0u} : qr2[1]);
  raw_barrier();
  bf16x8 kc1 = lds16<0>(fa[0][0]); bf16x4 kc2 = lds8<0>(fa[0][1]);
  bf16x8 kn1 = lds16<1024>(fa[0][0]); bf16x4 kn2 = lds8<512>(fa[0][1]);
  bf16x8 v0 = lds16<0>(fa[0][2]), v1 = lds16<VFR>(fa[0][2]), vp1 = qa1;
  if (PACK) {
#pragma unroll
    for (int b = 0; b < 5; ++b) glds16(pj.src + (size_t)min(pj.kk0 + b, pj.kmax) * 32 * pj.ld, smem + ATT_PK + b * 8192 + wid * 1024);
  }
  asm volatile("s_waitcnt lgkmcnt(0)" : "+v"(kc1), "+v"(kc2), "+v"(kn1), "+v"(kn2), "+v"(v0), "+v"(v1));
  {
    sa = MF16(kc1, qa1, oa); sb = MF16(kc1, qb1, ob);
    sa = MF8(kc2, qa2, sa); sb = MF8(kc2, qb2, sb);
    const float ma = rowmax16(sa), mb = rowmax16(sb);
#pragma unroll
    for (int i = 0; i < 16; ++i) { nma[i] = -ma; nmb[i] = -mb; sa[i] -= ma; }
  }
  u32x4 p3 = (u32x4){0u, 0u, 0u, 0u};
  STAMP(KID, 2); CSTAMP(KID, 2);
  static_for<32>([&](auto S) {
    constexpr int c = decltype(S)::v >> 2, t = decltype(S)::v & 3;
    constexpr int s0 = decltype(S)::v, s1 = s0 + 1, s2 = s1 + 1;
    constexpr int wk = s0 <= 25 ? (s0 - 23) * 3 + 1 : (s0 - 27) * 3 + 10;
    constexpr bool wp = (s0 >= 23 && s0 <= 25) || s0 == 27;
    constexpr int b1 = (s1 >> 2) & 3, b2 = (s2 >> 2) & 3;
    constexpr int pb = PACK ? (decltype(S)::v - 10) >> 1 : -1;
    constexpr bool pk = pb >= 0 && pb < 5;
    const char* gs = kvb + (c + 3) * KVP_CHUNK; const unsigned gm = mb + ((c + 3) & 3) * ATT_STB;
    u32x4 p0, p1, p2; bf16x8 kx1, vx0, vx1; bf16x4 kx2;
    SB();
    sb = MF16(kc1, qb1, nmb); SB();
    if (t == 3 && c < 5) { gld<1024, SC>(voff, gs, gm); SB(); }
    if (wp) wpre(IC<(wp ? wk : 0)>{});
    E4(p0, 0, sa, 0) SB();
    ob = MF16(vp1, BC8(p3), ob); SB();
    if (t == 3 && c < 5) { gld<2048, SC>(voff, gs, gm); SB(); }
    if (wp) wpre(IC<(wp ? wk + 1 : 0)>{});
    if (PACK && s0 == 20) { st16m(pj.cs, pcs); SB(); }
    E4(p0, 2, sa, 4) SB();
    sb = MF8(kc2, qb2, sb); SB();
    if (pk && (t & 1)) asm volatile("s_waitcnt lgkmcnt(0)" : "+v"(kn1), "+v"(kn2), "+v"(v0), "+v"(v1), "+v"(plo), "+v"(phi));
    else asm volatile("s_waitcnt lgkmcnt(0)" : "+v"(kn1), "+v"(kn2), "+v"(v0), "+v"(v1));
    SB();
    E4(p1, 0, sa, 8) SB();
    oa = MF16(v0, BC8(p0), oa); SB();
    if (t == 2) {
      constexpr int W[7] = {8, 8, 3, 5, 7, 7, 10};
      if (c < 7) wait_vm<PACK ? W[c < 7 ? c : 0] : (c < 6 ? 3 : 10)>();
      __builtin_amdgcn_s_barrier();
      if (c < 5) gld<0, SC>(voff, gs, gm);
      if (PACK && c == 6) st4m(ws + WS_PFLG + bid * 4, 1u, m_fl);
      SB();
    }
    if (pk && (t & 1)) {
      const bf16x8 f = __builtin_shufflevector(plo, phi, 0, 1, 2, 3, 4, 5, 6, 7);
      const int kk = min(pj.kk0 + pb, pj.kmax);
      st16<3>(pj.dst + ((kk >> 1) * pj.s2 + (kk & 1)) * 1024, __builtin_bit_cast(u32x4, f));
      pcs = __builtin_amdgcn_mfma_f32_16x16x32_bf16(f, (bf16x8){0x3f80, 0x3f80, 0x3f80, 0x3f80, 0x3f80, 0x3f80, 0x3f80, 0x3f80}, pcs, 0, 0, 0);
      SB();
    }
    E4(p1, 2, sa, 12) SB();
    if (s1 < 32) { sa = MF16(kn1, qa1, nma); SB(); }
    if (s2 < 32) { kx1 = lds16<(b2 & 1) * ATT_STB + (s2 & 3) * 1024>(fa[b2 >> 1][0]); SB(); }
    E4(p2, 0, sb, 0) SB();
    oa = MF16(v1, BC8(p1), oa); SB();
    if (s2 < 32) { kx2 = lds8<(b2 & 1) * ATT_STB + (s2 & 3) * 512>(fa[b2 >> 1][1]); SB(); }
    if (wp || s0 == 22) wpre(IC<(wp ? wk + 2 : 0)>{});
    if (pk && !(t & 1)) { plo = ldstr<(pk ? pb : 0) * 8192>(pp); phi = ldstr<(pk ? pb : 0) * 8192 + 4096>(pp); SB(); }
    E4(p2, 2, sb, 4) SB();
    if (s1 < 32) { sa = MF8(kn2, qa2, sa); SB(); }
    if (s1 < 32) { vx0 = lds16<(b1 & 1) * ATT_STB + (s1 & 3) * 2 * VFR>(fa[b1 >> 1][2]); SB(); }
    E4(p3, 0, sb, 8) SB();
    ob = MF16(v0, BC8(p2), ob); SB();
    if (s1 < 32) { vx1 = lds16<(b1 & 1) * ATT_STB + (s1 & 3) * 2 * VFR + VFR>(fa[b1 >> 1][2]); SB(); }
    E4(p3, 2, sb, 12) SB();
    vp1 = v1; kc1 = kn1; kc2 = kn2;
    if (s2 < 32) { kn1 = kx1; kn2 = kx2; }
    if (s1 < 32) { v0 = vx0; v1 = vx1; }
  });
  ob = MF16(vp1, BC8(p3), ob);
  float ma = -nma[0], mb2 = -nmb[0];
  STAMP(KID, 3); CSTAMP(KID, 3);
  float* xo = (float*)(smem + ATT_X) + (wid ^ 4) * 13 * 64 + lane;
  const float* xi = (const float*)(smem + ATT_X) + wid * 13 * 64 + lane;
  unsigned* badw = (unsigned*)(smem + L_SMALL);
  auto hand_over = [&]() {
#pragma unroll
    for (int i = 0; i < 12; ++i) xo[i * 64] = half ? oa[i] : ob[i];
    xo[12 * 64] = half ? ma : mb2;
  };
  hand_over();
  {
    const bool bad = __builtin_amdgcn_ballot_w64(nonfinite12(oa, lane) || nonfinite12(ob, lane)) != 0;
    if (lane == 0) badw[wid] = bad;
  }
  raw_barrier();
  const u32x4 bw = *(const u32x4*)badw | *(const u32x4*)(badw + 4);
  if (__builtin_amdgcn_readfirstlane(bw[0] | bw[1] | bw[2] | bw[3])) {
    wait_vm<0>();
    raw_barrier();
    stage(0, 2);
#pragma unroll
    for (int i = 0; i < 16; ++i) { oa[i] = 0.f; ob[i] = 0.f; }
    ma = -3.0e38f; mb2 = -3.0e38f;
#pragma unroll 1
    for (int c = 0; c < 8; ++c) {
      wait_vm<0>();
      raw_barrier();
      if (c + 1 < 8) stage(c + 1, 2 + ((c + 1) & 1));
      const unsigned bo = (c & 1) * ATT_STB;
#pragma unroll 1
      for (int t = 0; t < 4; ++t) {
        kc1 = lds16<0>(fa[1][0] + bo + t * 1024); kc2 = lds8<0>(fa[1][1] + bo + t * 512);
        v0 = lds16<0>(fa[1][2] + bo + t * 2 * VFR); v1 = lds16<VFR>(fa[1][2] + bo + t * 2 * VFR);
        asm volatile("s_waitcnt lgkmcnt(0)" : "+v"(kc1), "+v"(kc2), "+v"(v0), "+v"(v1));
        auto one = [&](const bf16x8& q1, const bf16x4& q2, f32x16& o, float& m) {
          f32x16 s;
#pragma unroll
          for (int i = 0; i < 16; ++i) s[i] = 0.f;
          s = MF16(kc1, q1, s); s = MF8(kc2, q2, s);
          const float mn = fmaxf(m, rowmax16(s)), f = EX(m - mn);
          m = mn;
          u32x4 p0, p1;
#pragma unroll
          for (int i = 0; i < 16; ++i) { s[i] -= mn; o[i] *= f; }
          E4(p0, 0, s, 0) E4(p0, 2, s, 4) E4(p1, 0, s, 8) E4(p1, 2, s, 12)
          o = MF16(v0, BC8(p0), o); o = MF16(v1, BC8(p1), o);
        };
        one(qa1, qa2, oa, ma); one(qb1, qb2, ob, mb2);
      }
    }
    raw_barrier();
    hand_over();
    raw_barrier();
  }
  STAMP(KID, 4);
  {
    const float m1 = half ? mb2 : ma, m2 = xi[12 * 64], M = fmaxf(m1, m2);
    const float f1 = EX(m1 - M), f2 = EX(m2 - M);
    float o[12];
#pragma unroll
    for (int i = 0; i < 12; ++i) o[i] = (half ? ob[i] : oa[i]) * f1 + xi[i * 64] * f2;
    const float l = __uint_as_float(swap32(__float_as_uint(o[8]), __float_as_uint(o[8]))[1]);
    const float inv = 1.0f / l;
    const int q = q0 + half * 32, c0 = head * DH;
    char* O = ws + WS_O;
    const u32x2 x = swap32(cvtpk(o[0] * inv, o[1] * inv), cvtpk(o[4] * inv, o[5] * inv));
    const u32x2 y = swap32(cvtpk(o[2] * inv, o[3] * inv), cvtpk(o[6] * inv, o[7] * inv));
    st16sc(O + foff<10>(q, c0 + odd * 4 + hh * 8), (u32x4){x[0], y[0], x[1], y[1]});
    if (hh == 0) st8sc(O + foff<10>(q, c0 + (odd ? 0 : 16)), (u32x2){cvtpk(o[8] * inv, o[9] * inv), cvtpk(o[10] * inv, o[11] * inv)});
  }
  STAMP(KID, 5);
  publish<8>(smem, ws, LAYER * 5, qb * 32 + head, tid);
  STAMP(KID, 6);
}

__global__ __launch_bounds__(512) void mega_kernel(MegaArgs g) {
  __shared__ __attribute__((aligned(16))) char smem[L_TOTAL];
  KLABEL("kbeg_", 1);
  const int tid = threadIdx.x, bid = blockIdx.x;
  char* ws = g.ws;
  if (tid == 0) *(unsigned*)(smem + L_CNT) = 0u;
  attn_phase<true>(smem, g, ws, bid, tid);
#pragma unroll 1
  for (int layer = 0;; ++layer) {
    int tmq, tnq; tile_of<CQ1>(opaque(bid), tmq, tnq);
    const bool hasq = layer == 0 && tnq < 15;
    const unsigned xmid = layer ? WS_X3 : WS_X1;
    const int K0 = layer * 5;
    if (__builtin_amdgcn_readfirstlane(tid) >= 256) {
      if (layer == 0) {
        const char* p = ws + WS_PFLG + (tid & 63) * 16;
        for (int s = 0; s < (1 << 18); ++s) {
          u32x4 v = gload16sc(p);
          wait_vm<0>();
          asm volatile("" : "+v"(v));
          if (__builtin_amdgcn_ballot_w64((v[0] & v[1] & v[2] & v[3]) != 1u) == 0) break;
          __builtin_amdgcn_s_sleep(4);
        }
      }
      char* w; u32x4 R[35];
      w = opaque(ws); loader_wo(smem, w, opaque(bid), opaque_v(tid));
      w = opaque(ws); loader_ln<CGU>(smem, w + xmid, w, WS_STAT, opaque(bid), opaque_v(tid), R);
      w = opaque(ws); loader_dn(smem, w, hasq, opaque(bid), opaque_v(tid), R);
      w = opaque(ws);
      if (hasq) loader_ln<CQ1>(smem, w + WS_X2, w, WS_STAT2, opaque(bid), opaque_v(tid), R);
    } else {
      char* w;
      STAMP(K0 + 2, 0);
      w = opaque(ws); compute_res<CWO, 0>(smem, w + WS_O, layer ? w + WS_X2 : g.x, layer == 0, w + xmid, false, w, WS_STAT, K0, opaque(bid), opaque_v(tid), K0 + 2);
      STAMP(K0 + 3, 0);
      w = opaque(ws); compute_ln<CGU>(smem, w + WS_H, w, K0 + 1, opaque(bid), opaque_v(tid), K0 + 3);
      STAMP(K0 + 4, 0);
      w = opaque(ws); compute_res<CDN, 1>(smem, w + WS_H, nullptr, false, layer ? g.out : w + WS_X2, layer == 1, w, WS_STAT2, K0 + 2, opaque(bid), opaque_v(tid), K0 + 4);
      if (hasq) {
        STAMP(5, 0);
        w = opaque(ws); compute_ln<CQ1>(smem, nullptr, w, 3, opaque(bid), opaque_v(tid), 5);
      }
    }
    if (layer) break;
    attn_phase<false>(smem, g, opaque(ws), opaque(bid), opaque_v(tid));
  }
}

struct Q0Args { const char* x; const char* wqkv; const u16* wo; char* ws; const char* wg; const char* wu; const char* wd; const char* out; };
__global__ __launch_bounds__(512) void qkv0_kernel(Q0Args g) {
  __shared__ __attribute__((aligned(16))) char smem[L_TOTAL];
  KLABEL("kbeg_", 0);
  const int tid = threadIdx.x, bid = blockIdx.x;
  int tm0, tn0; tile_of<CQ0>(bid, tm0, tn0);
  if (tn0 == 15) {
    {
      const int lane = tid & 63, part = __builtin_amdgcn_readfirstlane(tid >> 6) * 2 + ((bid >> 3) & 1);
      const char* dummy = smem + L_SMALL + 2048;
      if (TC & 2) touch_code<0, 2>(dummy, part, lane);
      if (TC & 4) {
        const unsigned o = (part * 64 + lane) << 16;
        if (o < WS_END) gtouch(g.ws + o, dummy);
        const char* q = part & 2 ? (part & 1 ? g.out : g.wd) : (part & 1 ? g.wu : g.wg);
        if (part >= 12 && lane * 65536 < DM * DF * 2) gtouch(q + lane * 65536, dummy);
      }
    }
    if (tid < 128) *(unsigned*)(g.ws + WS_FLG + (tm0 * 128 + tid) * 128) = 0u;
    else if (tid < 144) *(unsigned*)(g.ws + WS_PFLG + (tm0 * 16 + tid - 128) * 4) = 0u;
    const int lane = tid & 63, wid = __builtin_amdgcn_readfirstlane(tid >> 6);
    const int prow = wid * 4 + (lane >> 4), c16 = lane & 15, ch = (c16 >> 1) ^ (prow & 7);
    const int k = 8 * (((prow >> 3) & 1) * 2 + ((prow >> 2) & 1)) + 4 * ((prow >> 4) & 1) + (prow & 3);
    const int pr = 8 * (lane >> 5) + 4 * ((lane >> 4) & 1) + ((lane & 15) >> 2);
#pragma unroll
    for (int b = 0; b < 7; ++b) {
      const int id = min(tm0 * 7 + b, 99), cg = id / 20, kk = id % 20;
      glds16(g.wo + (size_t)(kk * 32 + k) * DM + cg * 128 + ch * 16 + (c16 & 1) * 8, smem + b * 8192 + wid * 1024);
    }
    wait_vm<0>();
    raw_barrier();
#pragma unroll
    for (int b = 0; b < 7; ++b) {
      const int id = min(tm0 * 7 + b, 99), cg = id / 20, kk = id % 20, jb = cg * 8 + wid;
      const char* p = smem + b * 8192 + pr * 256 + (wid ^ (pr & 7)) * 32 + (lane & 3) * 8;
      const bf16x4 lo = tr_read(p), hi = tr_read(p + 4096);
      *(bf16x8*)(g.ws + WS_PWO + (size_t)((jb / 5) * 100 + (jb % 5) * 2 + (kk >> 1) * 10 + (kk & 1)) * 1024 + lane * 16) = __builtin_shufflevector(lo, hi, 0, 1, 2, 3, 4, 5, 6, 7);
    }
    if (TC & 1) touch_code<1, 0>(smem + L_SMALL + 2048, wid * 2 + ((bid >> 3) & 1), lane);
#define WARM 1
    if (WARM & 1) {
#pragma unroll
      for (int i = 0; i < 2; ++i) glds16(g.ws + WS_STAT + (size_t)((tm0 * 2 + i) * 512 + tid) * 16, smem + 65536 + wid * 1024);
    }
    if (WARM & 2) {
#pragma unroll 4
      for (int i = 0; i < 20; ++i) glds16(g.ws + WS_O + (size_t)((tm0 * 20 + i) * 512 + tid) * 16, smem + 65536 + wid * 1024);
    }
    if (WARM & 4) {
#pragma unroll
      for (int i = 0; i < 2; ++i) *(u32x4*)(g.ws + WS_STAT + (size_t)((tm0 * 2 + i) * 512 + tid) * 16) = (u32x4){0u, 0u, 0u, 0u};
    }
    if (WARM & 8) {
#pragma unroll 4
      for (int i = 0; i < 20; ++i) *(u32x4*)(g.ws + WS_O + (size_t)((tm0 * 20 + i) * 512 + tid) * 16) = (u32x4){0u, 0u, 0u, 0u};
    }
    wait_vm<0>();
    KLABEL("kbeg_", 2);
    return;
  }
  if (tid < 256) STAMP(KID, 0);
  if (tid >= 256) {
    loader_raw(smem, g.x, g.wqkv, bid, tid);
    return;
  }
#define L2W 0
#define L2S 4
#define L2F 2
  if (L2W) {
    const int wid = __builtin_amdgcn_readfirstlane(tid >> 6), gw = (bid >> 3) * 4 + wid;
    const unsigned voff = (tid & 63) * 16, m = (unsigned)(uintptr_t)smem + 131072 + wid * 1024;
    __builtin_amdgcn_s_sleep(L2S);
    const char* px = uni(g.x + (size_t)(bid & 7) * 327680 + gw * 1024);
    gld<0, false>(voff, px, m); gld<0, false>(voff, px + 120 * 1024, m);
    if (gw < 80) gld<0, false>(voff, px + 240 * 1024, m);
    const char* pw = uni(g.wqkv + gw * 2048);
#pragma unroll
    for (int st = L2F; st < 10; ++st) { gld<0, false>(voff, pw + st * 245760, m); gld<0, false>(voff, pw + st * 245760 + 1024, m); }
  }
  compute_ln<CQ0>(smem, nullptr, g.ws, 0, bid, tid, 0);
  STAMP(KID, 6);
}

extern "C" void kernel_launch(void* const* d_in, const int* in_sizes, int n_in, void* d_out, int out_size, void* d_ws,
                              size_t ws_size, hipStream_t stream) {
  char* ws = (char*)d_ws;
  Q0Args q0{(const char*)d_in[0], (const char*)d_in[1], (const u16*)d_in[2], ws, (const char*)d_in[4], (const char*)d_in[5], (const char*)d_in[6], (const char*)d_out};
  qkv0_kernel<<<256, 512, 0, stream>>>(q0);
  MegaArgs m{ws, (const char*)d_in[0], (char*)d_out, (const u16*)d_in[1], (const u16*)d_in[4], (const u16*)d_in[5], (const u16*)d_in[6]};
  mega_kernel<<<256, 512, 0, stream>>>(m);
}
```

```cpp
#include <hip/hip_runtime.h>
#include <hip/hip_bf16.h>
#include <stdint.h>

typedef __attribute__((ext_vector_type(8))) short bf16x8;
typedef __attribute__((ext_vector_type(4))) short bf16x4;
typedef __attribute__((ext_vector_type(4))) float f32x4;
typedef __attribute__((ext_vector_type(16))) float f32x16;
typedef __attribute__((ext_vector_type(4))) unsigned u32x4;
typedef __attribute__((ext_vector_type(2))) unsigned u32x2;
typedef unsigned short u16;

#define DI __device__ __forceinline__
#define GAS __attribute__((address_space(1)))

constexpr int SEQ = 2048, DM = 640, NH = 32, DH = 20, DF = 1728;
constexpr float LN_EPS = 1e-5f;
constexpr float QSCALE = 0.22360679774997896f * 1.4426950408889634f;

constexpr size_t WS_X1   = 0;
constexpr size_t WS_X2   = WS_X1 + (size_t)SEQ * DM * 2;
constexpr size_t WS_X3   = WS_X2 + (size_t)SEQ * DM * 2;
constexpr size_t WS_Q    = WS_X3 + (size_t)SEQ * DM * 2;
constexpr size_t Q_SZ    = (size_t)SEQ * DM * 2;
constexpr size_t WS_O    = WS_Q + 2 * Q_SZ;
constexpr size_t WS_H    = WS_O + (size_t)SEQ * DM * 2;
constexpr int    KVP_CHUNK = 22528, KVP_KE = 6144, KVP_TAIL = 10240, KVP_KO = 12288, KVP_VO = 16384, VFR = 768;
constexpr size_t WS_KV   = WS_H + (size_t)SEQ * DF * 2;
constexpr size_t KV_SZ   = (size_t)16 * 16 * KVP_CHUNK;
constexpr size_t WS_PWO  = WS_KV + 2 * KV_SZ;
constexpr size_t WS_PWD  = WS_PWO + (size_t)DM * DM * 2 + 4096;
constexpr size_t WS_PGU  = WS_PWD + (size_t)DF * DM * 2;
constexpr size_t WS_PQKV = WS_PGU + (size_t)16 * 10 * 28 * 1024;
constexpr size_t WS_CGU  = WS_PQKV + (size_t)DM * 3 * DM * 2;
constexpr size_t WS_CQKV = WS_CGU + (size_t)16 * 4 * 224 * 4;
constexpr size_t WS_STAT = WS_CQKV + (size_t)15 * 4 * 128 * 4;
constexpr size_t WS_STAT2 = WS_STAT + (size_t)SEQ * 64;
constexpr size_t WS_FLG  = WS_STAT2 + (size_t)SEQ * 64;
#define NCP 1
constexpr unsigned FCP = 9 * 256 * 128;
constexpr size_t WS_PFLG = WS_FLG + NCP * FCP;
constexpr size_t WS_END  = WS_PFLG + 4096;

typedef __attribute__((ext_vector_type(2))) __bf16 bf16p;
typedef __attribute__((ext_vector_type(2))) float f32x2;
DI unsigned cvtpk(float lo, float hi) { return __builtin_bit_cast(unsigned, __builtin_convertvector((f32x2){lo, hi}, bf16p)); }
DI float dot2ones(unsigned a, float s) {
  return __builtin_amdgcn_fdot2_f32_bf16(__builtin_bit_cast(bf16p, a), __builtin_bit_cast(bf16p, 0x3f803f80u), s, false);
}
DI float dot2(unsigned a, unsigned b, float s) {
  return __builtin_amdgcn_fdot2_f32_bf16(__builtin_bit_cast(bf16p, a), __builtin_bit_cast(bf16p, b), s, false);
}
DI float bflo(unsigned v) { return __uint_as_float(v << 16); }
DI float bfhi(unsigned v) { return __uint_as_float(v & 0xffff0000u); }
#define KIDARG(k)
#define STAMP(kid, i)
#define CSTAMP(kid, i)
template <int N> DI void wait_vm() { asm volatile("s_waitcnt vmcnt(%0)" ::"i"(N) : "memory"); }
DI void raw_barrier() {
  asm volatile("" ::: "memory");
  __builtin_amdgcn_s_waitcnt(0xC07F);
  __builtin_amdgcn_s_barrier();
  asm volatile("" ::: "memory");
}
DI u32x2 swap16(unsigned a, unsigned b) { return __builtin_amdgcn_permlane16_swap(a, b, false, false); }
DI u32x2 swap32(unsigned a, unsigned b) { return __builtin_amdgcn_permlane32_swap(a, b, false, false); }
DI bf16x4 tr_read(const char* p) {
  typedef short v4i16_t __attribute__((ext_vector_type(4)));
  return __builtin_bit_cast(bf16x4, __builtin_amdgcn_ds_read_tr16_b64_v4i16((__attribute__((address_space(3))) v4i16_t*)(p)));
}
DI bf16x4 tr_read(unsigned p) {
  typedef short v4i16_t __attribute__((ext_vector_type(4)));
  return __builtin_bit_cast(bf16x4, __builtin_amdgcn_ds_read_tr16_b64_v4i16((__attribute__((address_space(3))) v4i16_t*)(uintptr_t)(p)));
}
template <bool SC = false> DI void glds16(const void* g, const char* l) {
  unsigned sv; const unsigned lb = __builtin_amdgcn_readfirstlane((unsigned)(uintptr_t)l);
  if (SC) asm volatile("s_mov_b32 %0, m0\n\ts_mov_b32 m0, %2\n\ts_nop 0\n\tglobal_load_lds_dwordx4 %1, off sc1\n\ts_mov_b32 m0, %0"
               : "=&s"(sv) : "v"(g), "s"(lb) : "memory");
  else asm volatile("s_mov_b32 %0, m0\n\ts_mov_b32 m0, %2\n\ts_nop 0\n\tglobal_load_lds_dwordx4 %1, off\n\ts_mov_b32 m0, %0"
               : "=&s"(sv) : "v"(g), "s"(lb) : "memory");
}
DI void gtouch(const void* g, const char* l) {
  unsigned sv; const unsigned lb = __builtin_amdgcn_readfirstlane((unsigned)(uintptr_t)l);
  asm volatile("s_mov_b32 %0, m0\n\ts_mov_b32 m0, %2\n\ts_nop 0\n\tglobal_load_lds_dword %1, off\n\ts_mov_b32 m0, %0"
               : "=&s"(sv) : "v"(g), "s"(lb) : "memory");
}
#define KLABEL(pfx, id) asm volatile(pfx "%c0:" ::"i"(id))
template <int ID> DI unsigned long long kaddr() {
  unsigned long long a;
  asm volatile("s_getpc_b64 s[20:21]\n\ts_add_u32 s20, s20, kbeg_%c1@rel32@lo+4\n\ts_addc_u32 s21, s21, kbeg_%c1@rel32@hi+12\n\ts_mov_b64 %0, s[20:21]" : "=s"(a) : "i"(ID) : "s20", "s21", "scc");
  return a;
}
#define CTS 128
template <int A, int B> DI void touch_code(const char* dummy, int part, int lane) {
  const unsigned long long lo = kaddr<A>() & ~255ull, hi = kaddr<B>();
  const unsigned o = part * 8192 + lane * 128;
  if (hi > lo && o < (unsigned)(hi - lo)) { gtouch((const char*)lo + o, dummy); if (CTS == 64) gtouch((const char*)lo + o + 64, dummy); }
}
#define SLP 1
#define MAPR 8
#define MAPL 8
#define MAPQ 2
#define MAP0 2
#define AST 1
#define TC 7
DI u32x2 gload8(const void* g) {
  u32x2 r; asm volatile("global_load_dwordx2 %0, %1, off" : "=v"(r) : "v"(g) : "memory"); return r;
}
DI unsigned gload4(const void* g) {
  unsigned r; asm volatile("global_load_dword %0, %1, off" : "=v"(r) : "v"(g) : "memory"); return r;
}
DI u32x4 gload16(const void* g) {
  u32x4 r; asm volatile("global_load_dwordx4 %0, %1, off" : "=v"(r) : "v"(g) : "memory"); return r;
}
DI u32x4 gload16sc(const void* g) {
  u32x4 r; asm volatile("global_load_dwordx4 %0, %1, off sc1" : "=v"(r) : "v"(g) : "memory"); return r;
}
DI void st4sc(void* p, float v) { asm volatile("global_store_dword %0, %1, off sc1" ::"v"(p), "v"(v) : "memory"); }
DI void st16p(void* p, u32x4 v) { asm volatile("global_store_dwordx4 %0, %1, off\n\ts_nop 1" ::"v"(p), "v"(v)); }
DI void st16sc(void* p, u32x4 v) { asm volatile("global_store_dwordx4 %0, %1, off sc1\n\ts_nop 1" ::"v"(p), "v"(v)); }
#define ST_MODE 14
template <int CLS> DI void st16(void* p, u32x4 v) {
  if ((ST_MODE >> CLS) & 1) asm volatile("global_store_dwordx4 %0, %1, off sc1\n\ts_nop 1" ::"v"(p), "v"(v));
  else *(u32x4*)p = v;
}
template <int IMM, bool WT = true> DI void stw(const void* base, unsigned off, u32x4 v) {
  if (WT) asm volatile("global_store_dwordx4 %0, %1, %2 offset:%3 sc1\n\ts_nop 1" ::"v"(off), "v"(v), "s"(base), "i"(IMM));
  else asm volatile("global_store_dwordx4 %0, %1, %2 offset:%3\n\ts_nop 1" ::"v"(off), "v"(v), "s"(base), "i"(IMM));
}
DI void stw4(const void* base, unsigned off, unsigned v) { asm volatile("global_store_dword %0, %1, %2 sc1" ::"v"(off), "v"(v), "s"(base)); }
DI u32x2 gload8s(unsigned voff, const char* sb) {
  u32x2 r; asm volatile("global_load_dwordx2 %0, %1, %2" : "=v"(r) : "v"(voff), "s"(sb) : "memory"); return r;
}
template <int CLS> DI void st8(void* p, u32x2 v) {
  if ((ST_MODE >> CLS) & 1) asm volatile("global_store_dwordx2 %0, %1, off sc1" ::"v"(p), "v"(v) : "memory");
  else *(u32x2*)p = v;
}
template <int I> struct IC { static constexpr int v = I; };
template <int L, int NMAX> DI void wait_stages(int n) {
  if constexpr (NMAX == 0) wait_vm<0>();
  else { if (n >= NMAX) wait_vm<NMAX * L>(); else wait_stages<L, NMAX - 1>(n); }
}
template <int N, int I = 0, class F> DI void static_for(F&& f) {
  if constexpr (I < N) { f(IC<I>{}); static_for<N, I + 1>(f); }
}
#define SB() __builtin_amdgcn_sched_barrier(0)
template <int NS> DI unsigned foff(int row, int col) {
  return (unsigned)((((row >> 6) * NS + (col >> 6)) * 8 + ((row >> 4) & 3) * 2 + ((col >> 5) & 1)) * 1024 +
                    (((col >> 3) & 3) * 16 + (row & 15)) * 16 + (col & 7) * 2);
}

enum { EPI_QKV = 0, EPI_RES = 1, EPI_GEGLU = 2 };
constexpr int L_TOTAL = 163840, L_SMALL = L_TOTAL - 2304, L_CNT = L_SMALL + 1020 + 1024, L_XB = L_SMALL - 20480, L_XHI = L_XB + 8192;
constexpr int L_ROW4 = 143360;
constexpr float GC1 = -2.0f * 0.7978845608028654f * 1.4426950408889634f, GC2 = GC1 * 0.044715f;
constexpr int WO_BHI = 98304;
constexpr int wo_b(int s) { return s < 6 ? WO_BHI + s * 10240 : (s - 6) * 10240; }
DI int xb_off(int w, int j) { return j < 3 ? L_XHI + (w * 3 + j) * 1024 : L_XB + (w * 2 + j - 3) * 1024; }

template <int BM_, int BN_, int KTOT_, int NST_, int EPI_, bool RAW_, unsigned TA_, unsigned TB_> struct Cfg {
  static constexpr int BM = BM_, BN = BN_, KTOT = KTOT_, NST = NST_, EPI = EPI_;
  static constexpr bool RAW = RAW_, RES = (EPI_ == EPI_RES), LNP = !RES && !RAW_;
  static constexpr int MT = RES ? 2 : 4, NT = RES ? 5 : BN_ / 32, KT = KTOT_ / 64;
  static constexpr int RS = BN_ * 2;
  static constexpr DI int aslot(int b) { return RAW_ ? b << 15 : (int)((TA_ >> (8 * b)) & 255u) << 10; }
  static constexpr DI int bslot(int b) { return RAW_ ? (b << 15) + 16384 : (int)((TB_ >> (8 * b)) & 255u) << 10; }
  static constexpr unsigned long long dtab(int b) {
    unsigned long long t = 1ull << 63;
    for (int k = 0; k < 9; ++k) t |= (unsigned long long)((((b ? bslot((k + 1) % NST_) - bslot(k % NST_) : aslot((k + 1) % NST_) - aslot(k % NST_)) >> 11) & 127)) << (7 * k);
    return t;
  }
};
constexpr unsigned tb4(unsigned a, unsigned b, unsigned c, unsigned d) { return a | b << 8 | c << 16 | d << 24; }
#define RST 4
typedef Cfg<128, 128, 640, RST, EPI_QKV, true, 0, 0> CQ0;
typedef Cfg<128, 128, 640, 4, EPI_QKV, false, tb4(0, 36, 52, 126), tb4(20, 70, 90, 110)> CQ1;
typedef Cfg<64, 80, 640, 8, EPI_RES, false, 0, 0> CWO;
typedef Cfg<128, 224, 640, 3, EPI_GEGLU, false, tb4(0, 16, 124, 0), tb4(40, 68, 96, 0)> CGU;
typedef Cfg<64, 80, 1728, 8, EPI_RES, false, 0, 0> CDN;
constexpr int dn_b(int s) {
  constexpr int Q[7] = {20480, 71680, 92160, 112640, 0, 40960, 61440};
  return (s >> 1) % 7 == 6 && (s & 1) ? 133120 : Q[(s >> 1) % 7] + (s & 1) * 10240;
}

template <class C> DI void tile_of(int bid, int& tm, int& tn) {
  const int xcd = bid & 7, idx = bid >> 3;
  constexpr int R = C::RES ? MAPR : (C::EPI == EPI_QKV ? (C::RAW ? MAP0 : MAPQ) : MAPL), NC = (C::RES ? 8 : 16) / (32 / R);
  tm = (xcd / NC) * R + idx % R; tn = (idx / R) * NC + xcd % NC;
  if (C::RAW) {
    const int od = xcd & 1;
    tm = (xcd >> 1) * 4 + (idx < 28 ? idx & 3 : (idx & 1) + 2 * od);
    tn = idx < 28 ? (idx >> 2) + 8 * od : (idx < 30 ? 7 : 15);
  }
}
DI const char* uni(const char* p) {
  const unsigned long long v = (unsigned long long)p;
  return (const char*)(((unsigned long long)(unsigned)__builtin_amdgcn_readfirstlane((unsigned)(v >> 32)) << 32) | (unsigned)__builtin_amdgcn_readfirstlane((unsigned)v));
}
DI char* opaque(char* p) { asm volatile("" : "+s"(p)); return p; }
DI int opaque(int x) { asm volatile("" : "+s"(x)); return x; }
DI int opaque_v(int x) { asm volatile("" : "+v"(x)); return x; }

template <int OFF, bool SC> DI void gld(unsigned voff, const char* sb, unsigned m) {
  if (SC) asm volatile("s_mov_b32 m0, %2\n\ts_nop 0\n\tglobal_load_lds_dwordx4 %0, %1 offset:%3 sc1" ::"v"(voff), "s"(sb), "s"(m), "i"(OFF) : "memory", "m0");
  else asm volatile("s_mov_b32 m0, %2\n\ts_nop 0\n\tglobal_load_lds_dwordx4 %0, %1 offset:%3" ::"v"(voff), "s"(sb), "s"(m), "i"(OFF) : "memory", "m0");
}
template <int OFF, bool SC> DI void gldk(unsigned voff, const char* sb) {
  if (SC) asm volatile("global_load_lds_dwordx4 %0, %1 offset:%2 sc1" ::"v"(voff), "s"(sb), "i"(OFF) : "memory", "m0");
  else asm volatile("global_load_lds_dwordx4 %0, %1 offset:%2" ::"v"(voff), "s"(sb), "i"(OFF) : "memory", "m0");
}
template <int OFF> DI void gldh(unsigned voff, const char* sb, unsigned m) {
  asm volatile("s_mov_b32 m0, %2\n\ts_mov_b32 exec_hi, 0\n\tglobal_load_lds_dwordx4 %0, %1 offset:%3\n\ts_mov_b32 exec_hi, -1" ::"v"(voff), "s"(sb), "s"(m), "i"(OFF) : "memory", "m0");
}
template <int N, bool SC> DI void gldn(unsigned voff, const char* sb, unsigned m) {
  static_for<N>([&](auto I) { gld<decltype(I)::v * 1024, SC>(voff, sb, m); });
}
template <int OFF> DI u32x4 gload16s(unsigned voff, const char* sb) {
  u32x4 r; asm volatile("global_load_dwordx4 %0, %1, %2 offset:%3 sc1" : "=v"(r) : "v"(voff), "s"(sb), "i"(OFF) : "memory"); return r;
}

template <int OFF> DI u32x4 gload16n(unsigned voff, const char* sb) {
  u32x4 r; asm volatile("global_load_dwordx4 %0, %1, %2 offset:%3" : "=v"(r) : "v"(voff), "s"(sb), "i"(OFF) : "memory"); return r;
}
template <int OFF> DI void dsw16(unsigned a, u32x4 v) { asm volatile("ds_write_b128 %0, %1 offset:%2" ::"v"(a), "v"(v), "i"(OFF) : "memory"); }

template <int OFF> DI bf16x8 lds16(unsigned a) { bf16x8 r; asm volatile("ds_read_b128 %0, %1 offset:%2" : "=v"(r) : "v"(a), "i"(OFF)); return r; }

template <int P> DI float qx(float v) { return __uint_as_float(__builtin_amdgcn_update_dpp(0u, __float_as_uint(v), P, 0xf, 0xf, true)); }

#define UP 3
#define PFX 0
#define PFS 0
#define TNT ""
DI void gtouchs(unsigned voff, const char* sb, unsigned m) {
  asm volatile("s_mov_b32 m0, %2\n\ts_nop 0\n\tglobal_load_lds_dword %0, %1 " TNT ::"v"(voff), "s"(sb), "s"(m) : "memory", "m0");
}
#define RNT ""
DI void gldr(unsigned voff, const char* sb, unsigned m) {
  asm volatile("s_mov_b32 m0, %2\n\ts_nop 0\n\tglobal_load_lds_dwordx4 %0, %1 " RNT ::"v"(voff), "s"(sb), "s"(m) : "memory", "m0");
}
#define DUMMY ((unsigned)(uintptr_t)smem + L_SMALL + 2048)
struct Raw { unsigned va, vb, m; const char *pa, *pb; };
template <int S, int I> DI void raw_piece(const Raw& r, int i) {
  gldr(r.va, r.pa + S * 128 + i * (32 * DM * 2), r.m + (S % RST) * 32768 + i * 4096);
  gldr(r.vb, r.pb + S * (64 * 3 * DM * 2) + ((i & 1) * 4 + (i >> 1) * 32) * (3 * DM * 2), r.m + (S % RST) * 32768 + 16384 + i * 4096);
}
struct Tch { unsigned v, ds; const char* p; };
DI Tch touch_of(const char* A, const char* B, int bid, int lane) {
  const int xcd = bid & 7, idx = min(bid >> 3, 23);
  Tch t;
  t.v = idx < 16 ? (lane >> 4) * (3 * DM * 2) + (lane & 15) * 128 : lane * (DM * 2);
  t.p = idx < 16 ? B + idx * 4 * (3 * DM * 2) + (xcd & 1) * 1792 : A + ((xcd >> 1) * 512 + (idx - 16) * 64) * (DM * 2);
  t.ds = idx < 16 ? 64 * 3 * DM * 2 : 128;
  return t;
}
#define NT0 1
#define AT -1, 0, 0, 1, 1, 2, 3, 4, 5, 6
#define TT 1
#define TX 0
#define EB 1
DI Raw raw_entry(char* smem, const char* A, const char* B, int tm, int tn, int bid, int tid) {
  const int lane = tid & 63, w = __builtin_amdgcn_readfirstlane(tid >> 6), z = (w & 1) * 4 + (lane >> 4);
  const unsigned sm = (unsigned)(uintptr_t)smem;
  Raw r;
  if (TX) {
    const int xcd = bid & 7, gw = TX == 1 ? (bid >> 3) * 8 + w : w * 30 + (bid >> 3);
    const bool isb = TX == 1 ? gw < 160 : gw % 3 != 2;
    const int kb = TX == 1 ? gw : gw / 3 * 2 + gw % 3, ka = TX == 1 ? gw - 160 : gw / 3;
    const unsigned av = TX == 1 ? lane * 128 : lane * (DM * 2);
    const int ao = TX == 1 ? ka * 8192 : (ka & 7) * 64 * (DM * 2) + (ka >> 3) * 128;
    gtouchs(isb ? (lane >> 4) * (3 * DM * 2) + (lane & 15) * 128 : av,
            isb ? B + kb * 4 * (3 * DM * 2) + (xcd & 1) * 1792 : A + (xcd >> 1) * 512 * (DM * 2) + ao, DUMMY);
  }
  r.va = (lane >> 3) * (DM * 2) + ((lane & 7) ^ z) * 16;
  r.vb = (lane >> 4) * (3 * DM * 2) + (((lane & 15) >> 1) ^ z) * 32 + (lane & 1) * 16;
  r.pa = A + (tm * 128 + (w & 3) * 8) * (DM * 2);
  r.pb = B + (w & 3) * 8 * (3 * DM * 2) + tn * 256;
  r.m = sm + (w & 3) * 1024;
  raw_piece<0, 0>(r, w >> 2); raw_piece<0, 0>(r, (w >> 2) + 2);
  if (EB) __builtin_amdgcn_s_barrier();
  return r;
}
typedef __attribute__((ext_vector_type(8))) unsigned u32x8;
DI const char* ptr_of(unsigned lo, unsigned hi) { return (const char*)(((unsigned long long)hi << 32) | lo); }
struct Raw0 { unsigned v[4], m[4]; };
DI Raw0 raw_pre(char* smem, Raw& r, int tm, int tn, int tid) {
  const int lane = tid & 63, w = __builtin_amdgcn_readfirstlane(tid >> 6), z = (w & 1) * 4 + (lane >> 4), h = w >> 2;
  r.va = (lane >> 3) * (DM * 2) + ((lane & 7) ^ z) * 16;
  r.vb = (lane >> 4) * (3 * DM * 2) + (((lane & 15) >> 1) ^ z) * 32 + (lane & 1) * 16;
  r.m = (unsigned)(uintptr_t)smem + (w & 3) * 1024;
  Raw0 e;
  e.v[0] = r.va + (tm * 128 + (w & 3) * 8 + h * 32) * (DM * 2); e.v[1] = e.v[0] + 64 * DM * 2;
  e.v[2] = r.vb + ((w & 3) * 8 + h * 4) * (3 * DM * 2) + tn * 256; e.v[3] = e.v[2] + 32 * 3 * DM * 2;
  e.m[0] = r.m + h * 4096; e.m[1] = e.m[0] + 8192; e.m[2] = e.m[0] + 16384; e.m[3] = e.m[0] + 24576;
  asm volatile("" : "+v"(e.v[0]), "+v"(e.v[1]), "+v"(e.v[2]), "+v"(e.v[3]));
  asm volatile("" : "+s"(e.m[0]), "+s"(e.m[1]), "+s"(e.m[2]), "+s"(e.m[3]));
  return e;
}
DI void loader_raw(char* smem, const Raw& r, const char* A, const char* B, char* ws, int tm, int tn, int bid, int tid) {
  char* wsdbg = ws;
  const int lane = tid & 63, lw = __builtin_amdgcn_readfirstlane(tid >> 6) - 4;
  constexpr int NTL = NT0 == 2 ? 1 : 2;
  struct N {
    static constexpr int at(int s) { constexpr int T[10] = {AT}; return T[s]; }
    static constexpr int behind(int s) {
      int n = at(s) <= TT && TT < s ? NTL : 0;
      for (int q = s + 1; q < 10; ++q) if (at(q) < s) n += 8;
      return n;
    }
    static constexpr unsigned long long lims() {
      unsigned long long r = 0;
      for (int i = -1; i < 10; ++i) { unsigned long long n = 0; for (int q = 0; q < 10; ++q) n += at(q) <= i; r |= n << (4 * i + 4); }
      return r;
    }
    static constexpr bool ok() { for (int q = 1; q < 10; ++q) if (at(q) < at(q - 1) || at(q) >= q || (q >= 4 && at(q) < q - 3)) return false; return true; }
  };
  static_assert(N::ok() && RST == 4, "schedule");
  const Tch t = touch_of(A, B, bid, lane);
  unsigned va[4], vb[4];
#pragma unroll
  for (int k = 0; k < 4; ++k) { va[k] = r.va + k * (32 * DM * 2); vb[k] = r.vb + ((k & 1) * 4 + (k >> 1) * 32) * (3 * DM * 2); }
  int nxt = 1;
#pragma unroll 1
  for (int i = -1; i < 10; ++i) {
    if (i >= 0) {
      static_for<10>([&](auto I) { if (i == decltype(I)::v) wait_vm<N::behind(decltype(I)::v)>(); });
      raw_barrier();
    }
#pragma unroll 1
    for (const int lim = (int)(N::lims() >> (4 * i + 4)) & 15; nxt < lim; ++nxt) {
      const unsigned m = r.m + (nxt & 3) * 32768;
#pragma unroll
      for (int k = 0; k < 4; ++k) { gldr(va[k], r.pa + nxt * 128, m + k * 4096); gldr(vb[k], r.pb + nxt * (64 * 3 * DM * 2), m + 16384 + k * 4096); }
    }
    if (i == TT) {
      if (NTL == 2) gtouchs(t.v, t.p + (5 + lw) * t.ds, DUMMY);
      asm volatile("s_mov_b32 m0, %2\n\ts_mov_b64 exec, %3\n\tglobal_load_lds_dword %0, %1\n\ts_mov_b64 exec, -1" ::"v"(t.v), "s"(t.p + 9 * t.ds), "s"(DUMMY), "s"(0xffffull << (16 * lw)) : "memory", "m0");
    }
  }
  wait_vm<0>();
  raw_barrier();
}

#define THN 4
#define SDL 1
#define THG 0
template <int N, int TH> DI void gldt(unsigned voff, const char* sb, unsigned m) {
  static_for<N>([&](auto I) { gld<decltype(I)::v * 1024, false>(voff, sb, m); if (TH) wait_vm<(TH ? TH - 1 : 0)>(); });
}
template <int TH = 0> DI void ld_gu_b(unsigned sm, const char* ws, int tn, int lw, unsigned voff, int s, int slot) {
  const char* p = ws + WS_PGU + (size_t)(tn * 10 + s) * 28672 + lw * 7168;
  const unsigned m = sm + CGU::bslot(slot) + lw * 7168;
  gldt<4, TH>(voff, p, m); gldt<3, TH>(voff, p + 4096, m + 4096);
}
template <int TH = 0> DI void ld_q_b(unsigned sm, const char* ws, int tn, int lw, unsigned voff, int s, int slot) {
  gldt<4, TH>(voff, ws + WS_PQKV + (size_t)(tn * 10 + s) * 16384 + lw * 4096, sm + CQ1::bslot(slot) + lw * 4096);
}
DI const char* dn_src(const char* ws, int tn, int lw, int p) { return ws + WS_PWD + (size_t)(tn * 27 + 2 * p) * 10240 + (p == 13 ? lw & 1 : lw) * 5120; }
DI unsigned dn_dst(unsigned sm, int lw, int p) { return sm + (lw >> 1 && p < 13 ? dn_b(2 * p + 1) : dn_b(2 * p)) + (lw & 1) * 5120; }
template <int TH = 0> DI void ld_dn_b(unsigned sm, const char* ws, int tn, int lw, unsigned voff, int p) {
  const char* g = dn_src(ws, tn, lw, p);
  const unsigned m = dn_dst(sm, lw, p);
  gldt<4, TH>(voff, g, m); gldt<1, TH>(voff, g + 4096, m + 4096);
}
#define NP8 1
#define THD 0
#define NB1 25
#define GW 3
#define XB 0
#define RN 0, 0, 0, 0, 0, 0, 0, 0, 6
constexpr int rn(int i) { constexpr int T[9] = {RN}; int n = 0; for (int k = 0; k < i; ++k) n += T[k]; return n; }
constexpr int gu_cnt(int i) { return (i >= 1 && i <= 7 ? 11 : 0) + (i == 5 ? 6 : 0) + (i == 8 ? 5 * NP8 : 0) + rn(i + 1) - rn(i); }
constexpr int gu_wait(int k) { return k == 0 ? 8 : k == 1 ? 4 + gu_cnt(0) : k == 2 ? gu_cnt(0) + gu_cnt(1) : gu_cnt(k - 2) - 11 + gu_cnt(k - 1); }

DI bool all_are(u32x4 v, unsigned e) { return (((v[0] ^ e) | (v[1] ^ e)) | ((v[2] ^ e) | (v[3] ^ e))) == 0u; }
#define LDBG
DI void loader_wo(char* smem, const char* ws, unsigned ep, int bid, int tid, int KID) {
  LDBG
  const int lw = __builtin_amdgcn_readfirstlane(tid >> 6) - 4;
  const unsigned voff = (tid & 63) * 16, sm = (unsigned)(uintptr_t)smem;
  int tm, tn; tile_of<CGU>(bid, tm, tn);
  STAMP(KID, 0);
  u32x4 v = gload16s<0>(voff, ws + WS_PFLG);
  raw_barrier();
  STAMP(KID, 1);
  raw_barrier();
  for (int s = 0; s < (1 << 18); ++s) {
    asm volatile("s_waitcnt vmcnt(0)" : "+v"(v)::"memory");
    if (__builtin_amdgcn_ballot_w64(!all_are(v, ep)) == 0) break;
    __builtin_amdgcn_s_sleep(4);
    v = gload16s<0>(voff, ws + WS_PFLG);
  }
  wait_vm<0>();
  STAMP(KID, 2);
  ld_gu_b(sm, ws, tn, lw, voff, 0, 0);
  ld_gu_b(sm, ws, tn, lw, voff, 1, 1);
#define GT 0
  if (GT) gtouchs(min(tid & 63, 55) * 128, ws + WS_PGU + (size_t)(tn * 10 + 2) * 28672 + ((tm & 7) * 4 + lw) * 7168, DUMMY);
  STAMP(KID, 3);
  raw_barrier();
  STAMP(KID, 4);
  if (GW & 1) { __builtin_amdgcn_s_barrier(); ld_gu_b(sm, ws, tn, lw, voff, 2, 2); }
  else { __builtin_amdgcn_s_sleep(SDL); ld_gu_b<THN>(sm, ws, tn, lw, voff, 2, 2); }
  STAMP(KID, 6);
}

template <class C> DI void loader_ln(char* smem, const char* A, const char* ws, size_t stat, int bid, int tid, u32x4 (&R)[50], int KID) {
  LDBG
  constexpr bool GU = C::EPI == EPI_GEGLU;
  constexpr int KT = C::KT, NST = C::NST, BN = C::BN;
  const int lw = __builtin_amdgcn_readfirstlane(tid >> 6) - 4;
  const unsigned voff = (tid & 63) * 16, sm = (unsigned)(uintptr_t)smem;
  int tm, tn; tile_of<C>(bid, tm, tn);
  int tmd, tnd; tile_of<CDN>(bid, tmd, tnd);
  const char* pa = A + (size_t)(tm * 2 + (lw >> 1)) * KT * 8192 + (lw & 1) * 4096;
#define EXB 0
  auto lda = [&](int s) { gldn<4, true>((EXB & 2) && s >= NST ? 0u : voff, pa + s * 8192, sm + C::aslot(s % NST) + lw * 4096); };
  auto ldb = [&](int s) { if (GU) ld_gu_b(sm, ws, tn, lw, EXB & 1 ? 0u : voff, s, s % NST); else ld_q_b(sm, ws, tn, lw, EXB & 1 ? 0u : voff, s, s % NST); };
  auto ldr = [&](auto N) {
    constexpr int n = decltype(N)::v;
    if (n == NB1) __builtin_amdgcn_s_barrier();
    R[n] = gload16n<(n % 5) * 1024 - 2048>(voff, dn_src(ws, tnd, lw, 4 + n / 5) + 2048);
  };
  static_assert(rn(9) <= NB1, "schedule");
  const int t = tid - 256;
  unsigned c[4]; u32x4 st[2];
  STAMP(KID, 0);
  raw_barrier();
  STAMP(KID, 1);
#pragma unroll
  for (int s = 0; s < NST; ++s) lda(s);
  static_for<KT>([&](auto I) {
    constexpr int i = decltype(I)::v;
    constexpr int WQ[10] = {12, 8, 12, 16, 16, 22, 22, 22, 8, 0};
    wait_vm<GU ? gu_wait(i) : WQ[i]>();
    if (!(EXB & 16) || i == 0) raw_barrier();
    if (i >= 1 && i <= KT - NST) { if (!(EXB & 4)) lda(i - 1 + NST); if (!(EXB & 8)) ldb(i - 1 + NST); }
    if ((EXB & 12) && (i == 1 || i == 7)) wait_vm<0>();
    if constexpr (GU && i < 8) static_for<rn(i + 1), rn(i)>(ldr);
    if (i == KT - NST - 2) {
      const float* cp = (const float*)(ws + (GU ? WS_CGU : WS_CQKV)) + tn * 4 * BN + (t < BN ? t : 0);
      const char* sp = ws + stat + (size_t)tm * 8192 + lw * 2048;
#pragma unroll
      for (int k = 0; k < 4; ++k) c[k] = gload4(cp + k * BN);
      st[0] = gload16s<0>(voff, sp); st[1] = gload16s<1024>(voff, sp);
    }
    if (i == 8) {
      if (GU) {
#pragma unroll
        for (int k = 0; k < NP8; ++k) ld_dn_b(sm, ws, tnd, lw, voff, k);
      }
      float2* rowst = (float2*)(smem + L_SMALL);
      float* colsum = (float*)(smem + L_SMALL + 1024);
      float cs = 0.f;
#pragma unroll
      for (int k = 0; k < 4; ++k) { asm volatile("" : "+v"(c[k])); cs += __uint_as_float(c[k]); }
      asm volatile("" : "+v"(st[0]), "+v"(st[1]));
      const f32x4 s0 = __builtin_bit_cast(f32x4, st[0]), s1 = __builtin_bit_cast(f32x4, st[1]);
      const bool od = t & 1;
      float sm1 = od ? s1[0] + s1[2] : s0[0] + s0[2], sq1 = od ? s1[1] + s1[3] : s0[1] + s0[3];
      sm1 += qx<0xB1>(od ? s0[0] + s0[2] : s1[0] + s1[2]); sq1 += qx<0xB1>(od ? s0[1] + s0[3] : s1[1] + s1[3]);
      sm1 += qx<0x4E>(sm1); sq1 += qx<0x4E>(sq1);
      const float mean = sm1 * (1.0f / C::KTOT);
      const float var = fmaxf(sq1 * (1.0f / C::KTOT) - mean * mean, 0.f);
      if (t < BN) colsum[t] = cs;
      const float rs = rsqrtf(var + LN_EPS);
      const int rr = lw * 32 + (t & 1) * 16 + ((t & 63) >> 2);
      if (!(t & 2)) {
        float g1 = GC1;
        asm volatile("" : "+s"(g1));
        if (GU) ((f32x4*)(smem + L_ROW4))[rr] = (f32x4){-mean, g1 * rs, GC2 * rs * rs * rs, var + LN_EPS};
        else rowst[rr] = make_float2(mean, rs);
      }
      if (GU) static_for<rn(9), rn(8)>(ldr);
    }
  });
  STAMP(KID, 4);
  if (GU) {
#pragma unroll
    for (int k = NP8; k < 4; ++k) ld_dn_b<THG>(sm, ws, tnd, lw, voff, k);
    static_for<50, rn(9)>(ldr);
  }
  STAMP(KID, 6);
}

#define EXA 0
DI void loader_dn(char* smem, const char* ws, bool hasq, int bid, int tid, u32x4 (&R)[50], int KID) {
  LDBG
  const int lw = __builtin_amdgcn_readfirstlane(tid >> 6) - 4;
  const unsigned voff = (tid & 63) * 16, sm = (unsigned)(uintptr_t)smem;
  int tm, tn; tile_of<CDN>(bid, tm, tn);
  int tmq, tnq; tile_of<CQ1>(bid, tmq, tnq);
  auto put = [&](auto N) {
    constexpr int n = decltype(N)::v;
    if ((EXA & 32) && n >= 15) asm volatile("s_nop 0" ::"v"(R[n]));
    else dsw16<(n % 5) * 1024>(dn_dst(sm, lw, 4 + n / 5) + voff, R[n]);
  };
#define QT 0
  auto qtouch = [&]() {
#pragma unroll
    for (int k = 0; k < 2; ++k) {
      const unsigned id = (lw * 2 + k) * 48 + min(tid & 63, 47);
      gtouchs((id >> 6) * 16384 + (id & 63) * 128, ws + WS_PQKV + (size_t)(tnq * 10 + 4) * 16384 + (tmq & 1) * 8192, DUMMY);
    }
  };
  static_for<14>([&](auto P) {
    constexpr int p = decltype(P)::v;
    if (p == 0) { wait_vm<(rn(8) > 5 ? 65 - rn(8) : 60)>(); STAMP(KID, 0); }
#define QP 12, 13, 14, 14
    constexpr int QS[4] = {QP};
#define PUTS 0, 15, 15, 19, 23, 27, 31, 35, 39, 43, 47, 50, 50, 50, 50
    constexpr int PC[15] = {PUTS};
    static_assert(p < 2 || (PC[p + 1] <= 10 + 5 * p && (PC[p + 1] >= 5 * p - 5 || PC[p + 1] == 50)), "slots");
    asm volatile("s_waitcnt lgkmcnt(%0)\n\ts_barrier" ::"i"(p >= 1 ? PC[p] - PC[p ? p - 1 : 0] : 0) : "memory");
    if (p == 0) wait_vm<(rn(9) > 15 ? 50 - rn(9) : 35)>();
    if (p == 2) wait_vm<0>();
    static_for<PC[p + 1], PC[p]>(put);
    if (hasq) {
#pragma unroll
      for (int k = 0; k < 4; ++k) if (QS[k] == p) ld_q_b(sm, ws, tnq, lw, voff, k, k);
      if (QT == p && p) qtouch();
      if (QT == 20 && p >= 1 && p <= 12) gtouchs((tid & 7) * 128, ws + WS_PQKV + (size_t)(tnq * 10 + 4 + (p - 1) / 2) * 16384 + (tmq & 1) * 8192 + ((p - 1) & 1) * 4096 + lw * 1024, DUMMY);
      if (QT == 21 && p >= 7 && p <= 12) gtouchs((tid & 15) * 128, ws + WS_PQKV + (size_t)(tnq * 10 + 4 + (p - 7)) * 16384 + (tmq & 1) * 8192 + lw * 2048, DUMMY);
      if (QT == 22 && p >= 3 && p <= 8) gtouchs((tid & 15) * 128, ws + WS_PQKV + (size_t)(tnq * 10 + 4 + (p - 3)) * 16384 + (tmq & 1) * 8192 + lw * 2048, DUMMY);
    }
  });
  STAMP(KID, 3);
  raw_barrier();
  STAMP(KID, 4);
  if (QT == 14 && hasq) qtouch();
  constexpr int QS[4] = {QP};
  if (GW & 2) __builtin_amdgcn_s_barrier();
  if (hasq) {
    if (!(GW & 2)) __builtin_amdgcn_s_sleep(SDL);
#pragma unroll
    for (int k = 0; k < 4; ++k) if (QS[k] == 14) ld_q_b<(GW & 2 ? 0 : THN)>(sm, ws, tnq, lw, voff, k, k);
    if (QT == 15) qtouch();
  }
  STAMP(KID, 6);
}

#define FS0 128
#define FS1 128
#define FS2 128
DI unsigned flag_of(int seam, int off, int wid, int lane) {
  unsigned o = (unsigned)WS_FLG + seam * 32768 + off + wid * 4 + (lane & (NCP - 1)) * FCP;
  asm volatile("" : "+v"(o));
  return o;
}
DI void publish(char* ws, unsigned flag, unsigned ep, int tid) {
  wait_vm<0>();
#define PUBA 1
  if (PUBA == 1 || (tid & 63) < NCP) __hip_atomic_store((GAS unsigned*)(ws + flag), ep, __ATOMIC_RELAXED, __HIP_MEMORY_SCOPE_AGENT);
}
DI void await(const char* ws, unsigned ep, int seam, int tile, int cp) {
  const unsigned o = (unsigned)WS_FLG + seam * 32768 + tile + (cp & (NCP - 1)) * FCP;
  bool ok = tile < 0;
  for (int s = 0; s < (1 << 18); ++s) {
    if (!ok) {
      u32x4 v = gload16s<0>(o, ws);
      asm volatile("s_waitcnt vmcnt(0)" : "+v"(v)::"memory");
      ok = all_are(v, ep);
    }
    if (__builtin_amdgcn_ballot_w64(!ok) == 0) break;
    __builtin_amdgcn_s_sleep(SLP);
  }
}

template <class C, int MODE> DI void compute_res(char* smem, const char* A, const char* res, bool res_raw, char* out, bool out_raw, char* ws, size_t stat, int seam, unsigned ep, int bid, int tid, int KID) {
  constexpr int KT = C::KT, NT = C::NT;
  const int lane = tid & 63, wid = __builtin_amdgcn_readfirstlane(tid >> 6), fr = lane & 15, fg = lane >> 4, wm = wid >> 1, wn = wid & 1;
  int tm, tn; tile_of<C>(bid, tm, tn);
  const int m0 = tm * C::BM, n0 = tn * C::BN;
  f32x4 acc[2][NT];
  const int row = m0 + wid * 16 + fr;
  const unsigned sm = (unsigned)(uintptr_t)smem;
  u32x2 resv[NT];
#pragma unroll
  for (int j = 0; j < NT; ++j) {
    const int col = n0 + j * 16 + fg * 4;
    if (MODE == 0) resv[j] = gload8s(res_raw ? (unsigned)(row * DM + col) * 2 : foff<10>(row, col), res);
    else resv[j] = *(const u32x2*)(smem + L_XHI + wid * 3072 + j * 512 + lane * 8);
  }
#define DA 9
#define DW 11
  constexpr int D = MODE == 0 ? DW : DA;
  const char* pa = A + (size_t)(tm * KT * 8 + wm * 4 + wn) * 1024;
  unsigned voff[2] = {(unsigned)(lane * 16 + wn * 2048), (unsigned)(lane * 16 + (wn ^ 1) * 2048)};
  unsigned pb[3], xw[2], xr[2], so[4];
#pragma unroll
  for (int i = 0; i < 3; ++i) {
    pb[i] = sm + wn * 1024 + lane * 16 + i * 65536;
    const int col = n0 + (2 * i + (fg & 1)) * 16 + (fg >> 1) * 8;
    so[i] = out_raw ? (unsigned)(row * DM + col) * 2 : foff<10>(row, col);
    asm volatile("" : "+v"(pb[i]), "+v"(so[i]));
  }
  so[3] = (unsigned)stat + (row * 8 + tn) * 8 + fg * 4;
#pragma unroll
  for (int i = 0; i < 2; ++i) {
    xw[i] = sm + xb_off(wid ^ 1, i * 3) + lane * 16; xr[i] = sm + xb_off(wid, i * 3) + lane * 16;
    asm volatile("" : "+v"(xw[i]), "+v"(xr[i]), "+v"(voff[i]), "+v"(so[3]));
  }
#define LB 2
  constexpr int LA = MODE == 0 ? 1 : LB, NB = LA + 1;
  u32x4 ar[D][2];
  bf16x8 b[NB][NT];
  constexpr int ORD[10] = {4, 5, 0, 1, 2, 3, 6, 7, 8, 9};
  auto lda = [&](auto S, auto I) {
    constexpr int s = decltype(S)::v, i = decltype(I)::v, st = MODE == 0 ? ORD[s % 10] : s;
    ar[s % D][i] = gload16s<0>(voff[i], pa + ((EXA & 1) && MODE == 1 ? 0 : st) * 8192);
    SB();
  };
  auto rdb = [&](auto S, auto J) {
    constexpr int s = decltype(S)::v, j = decltype(J)::v, o = (MODE == 0 ? wo_b(ORD[s % 10]) : dn_b(s)) + j * 2048;
    if (!((EXA & 2) && MODE == 1 && s >= NB && j > 0)) b[s % NB][j] = lds16<o & 0xffff>(pb[o >> 16]);
    SB();
  };
  const unsigned flag = flag_of(seam + 1, (tm * 8 + tn) * FS1, wid, lane);
  if (wid == 1) await(ws, ep, seam, MODE == 0 ? ((tm >> 2) * 32 + (lane >> 1)) * FS0 + (lane & 1) * 16 : (lane < 16 ? ((tm >> 1) * 16 + lane) * FS1 : -1), tm * 8 + tn);
  raw_barrier();
  STAMP(KID, 1);
  static_for<D - 1>([&](auto S) { lda(S, IC<0>{}); lda(S, IC<1>{}); });
  STAMP(KID, 2); CSTAMP(KID, 2);
  static_for<LA * NT>([&](auto J) { rdb(IC<decltype(J)::v / NT>{}, IC<decltype(J)::v % NT>{}); });
  if (MODE == 0) asm volatile("s_waitcnt vmcnt(%5)" : "+v"(resv[0]), "+v"(resv[1]), "+v"(resv[2]), "+v"(resv[3]), "+v"(resv[4]) : "i"(2 * (D - 1)) : "memory");
#pragma unroll
  for (int j = 0; j < NT; ++j) {
    acc[0][j] = (f32x4){bflo(resv[j][0]), bfhi(resv[j][0]), bflo(resv[j][1]), bfhi(resv[j][1])};
    acc[1][j] = (f32x4){0.f, 0.f, 0.f, 0.f};
  }
  SB();
  typedef __attribute__((address_space(3))) f32x4* lp4;
  static_for<KT>([&](auto S) {
    constexpr int s = decltype(S)::v, n = (s + D - 2 < KT ? s + D - 2 : KT - 1) - s;
    constexpr bool nx = s + LA < KT, last = s == KT - 1;
    constexpr int ahead = ((s + LA - 1 < KT ? s + LA - 1 : KT - 1) - s) * NT;
    if (MODE == 1 ? nx && !((s + LA) & 1) && !(EXA & 16) : s == 2) { __builtin_amdgcn_s_barrier(); SB(); }
    asm volatile("s_waitcnt vmcnt(%2)" : "+v"(ar[s % D][0]), "+v"(ar[s % D][1]) : "i"(2 * n) : "memory");
    static_for<2 * NT>([&](auto M) {
      constexpr int m = decltype(M)::v, j = last ? m % NT : m >> 1, i = last ? m < NT : m & 1;
      if (last ? m < NT : i == 0) asm volatile("s_waitcnt lgkmcnt(%1)" : "+v"(b[s % NB][j]) : "i"(NT - 1 - j + ahead + (nx ? (m < NT ? m : NT) : 0)));
      acc[i][j] = __builtin_amdgcn_mfma_f32_16x16x32_bf16(b[s % NB][j], __builtin_bit_cast(bf16x8, ar[s % D][i]), acc[i][j], 0, 0, 0);
      SB();
      if constexpr (nx && m < NT) rdb(IC<s + LA>{}, M);
      if constexpr (s + D - 1 < KT && (m == 5 || m == 7)) lda(IC<s + D - 1>{}, IC<(m == 7)>{});
      if constexpr (last && m >= NT) { *(lp4)(uintptr_t)(xw[j / 3] + (j % 3) * 1024) = acc[1][j]; SB(); }
    });
  });
  STAMP(KID, 3); CSTAMP(KID, 3);
  raw_barrier();
  STAMP(KID, 4);
  f32x4 xi[NT];
#pragma unroll
  for (int j = 0; j < NT; ++j) xi[j] = *(lp4)(uintptr_t)(xr[j / 3] + (j % 3) * 1024);
  SB();
  u32x2 v[NT];
#pragma unroll
  for (int j = 0; j < NT; ++j) {
    const f32x4 o = xi[j] + acc[0][j];
    v[j] = (u32x2){cvtpk(o[0], o[1]), cvtpk(o[2], o[3])};
    if ((j & 1) || j == NT - 1) {
      const int jp = j & ~1;
      const u32x2 x = swap16(v[jp][0], v[j][0]), y = swap16(v[jp][1], v[j][1]);
      if ((j & 1) || !(fg & 1)) stw<0>(out, so[j >> 1], (u32x4){x[0], y[0], x[1], y[1]});
    }
  }
  if (!out_raw) {
    float s = 0.f, q = 0.f;
#pragma unroll
    for (int j = 0; j < NT; ++j)
#pragma unroll
      for (int k = 0; k < 2; ++k) { s = dot2ones(v[j][k], s); q = dot2(v[j][k], v[j][k], q); }
    const u32x2 t1 = swap16(__float_as_uint(s), __float_as_uint(q));
    const unsigned t2 = __float_as_uint(__uint_as_float(t1[0]) + __uint_as_float(t1[1]));
    const u32x2 t3 = swap32(t2, t2);
    if (lane < 32) stw4(ws, so[3], __float_as_uint(__uint_as_float(t3[0]) + __uint_as_float(t3[1])));
  }
  if (MODE == 0) {
#pragma unroll
    for (int j = 0; j < NT; ++j) *(u32x2*)(smem + L_XHI + wid * 3072 + j * 512 + lane * 8) = v[j];
  }
  STAMP(KID, 5);
  if (!out_raw) publish(ws, flag, ep, tid);
  if (GW & (1 << MODE)) __builtin_amdgcn_s_barrier();
  STAMP(KID, 6);
}

constexpr int RAWP = NT0;
#define PHJ 1
#define UNL 2
template <class C> DI void compute_ln(char* smem, char* out, char* ws, int seam, unsigned ep, int bid, int tid, int KID) {
  constexpr int KT = C::KT, NST = C::NST, NT = C::NT, MT = C::MT, RS = C::RS, BN = C::BN, EPI = C::EPI, KTOT = C::KTOT;
  constexpr bool RAW = C::RAW;
  constexpr unsigned OQ = (unsigned)(WS_Q + (RAW ? 0 : Q_SZ)), OKV = (unsigned)(WS_KV + (RAW ? 0 : KV_SZ));
  const int lane = tid & 63, wid = __builtin_amdgcn_readfirstlane(tid >> 6), fr = lane & 15, fg = lane >> 4, wm = wid >> 1, wn = wid & 1;
  int tm, tn; tile_of<C>(bid, tm, tn);
  const int m0 = tm * C::BM, n0 = tn * BN;
  float2* rowst = (float2*)(smem + L_SMALL);
  float* colsum = (float*)(rowst + 128);
  f32x4 acc[MT][NT];
#pragma unroll
  for (int i = 0; i < MT; ++i)
#pragma unroll
    for (int j = 0; j < NT; ++j) acc[i][j] = (f32x4){0.f, 0.f, 0.f, 0.f};
  const int xi = RAW ? wn * 2 : 0, xj = RAW ? wm * 2 : 0;
  int aoff[2][2], boff[RAW ? NT : 1];
  const int pr = 8 * (fg >> 1) + 4 * (fg & 1) + (fr >> 2);
#pragma unroll
  for (int ks = 0; ks < 2; ++ks) {
    aoff[ks][0] = RAW ? (wm * 64 + fr) * 128 + (((ks * 4 + fg) ^ (fr >> 1)) * 16) + xi * 2048 : (wm * 8 + ks) * 1024 + lane * 16;
    aoff[ks][1] = aoff[ks][0] - xi * 4096;
  }
  if (RAW) {
#pragma unroll
    for (int j = 0; j < NT; ++j) boff[j] = pr * RS + ((wn * NT + (j ^ xj)) ^ (pr & 7)) * 32 + (lane & 3) * 8;
  } else boff[0] = wn * NT * 2048 + lane * 16;
  constexpr bool UNR_ = EPI == EPI_GEGLU && (UNL & 1);
  unsigned ad[RAW ? 8 : 4];
  if (RAW) {
#pragma unroll
    for (int i = 0; i < 4; ++i) { ad[i] = (unsigned)(uintptr_t)smem + aoff[i >> 1][i & 1]; ad[RAW ? 4 + i : 0] = (unsigned)(uintptr_t)smem + boff[RAW ? i : 0]; }
  } else {
    ad[0] = (unsigned)(uintptr_t)smem + aoff[0][0]; ad[1] = (unsigned)(uintptr_t)smem + boff[0];
    asm volatile("" : "+v"(ad[0]), "+v"(ad[1]));
    if (UNR_) { ad[2] = ad[0] + 98304; ad[3] = ad[1] + 69632; asm volatile("" : "+v"(ad[2]), "+v"(ad[3])); }
  }
  f32x4 srow[2], sgr[2], scol[2];
#pragma unroll
  for (int i = 0; i < 2; ++i) srow[i] = sgr[i] = scol[i] = (f32x4){0.f, 0.f, 0.f, 0.f};
  const bf16x8 ones = (bf16x8){0x3f80, 0x3f80, 0x3f80, 0x3f80, 0x3f80, 0x3f80, 0x3f80, 0x3f80};
  const bool swp = !(EPI == EPI_QKV && tn >= 10);
  unsigned so[2][2];
  if (EPI == EPI_QKV) {
    const bool isq = tn < DM / BN;
#pragma unroll
    for (int jq = 0; jq < 2; ++jq) {
      if (swp) {
        const int G = ((isq ? n0 : n0 - DM) >> 3) + (wn * NT + (jq * 2 ^ xj) + (fg & 1)) * 2 + (fg >> 1), pair = G / 5, gi = G % 5;
        const int mo = (((gi + (gi > 2)) & 1) * 32 + fr) * 16;
        if (isq) {
          so[jq][0] = OQ + (pair * 64 + (m0 >> 5) + wm * 2) * 2560 + (gi == 2 ? 2048 + fr * 16 : (gi > 2) * 1024 + mo);
          so[jq][1] = so[jq][0] + 2560;
        } else {
          so[jq][0] = OKV + (pair * 16 + (m0 >> 7)) * KVP_CHUNK + (gi == 2 ? KVP_TAIL + (wm * 64 + fr) * 16 : (gi > 2 ? KVP_KO : KVP_KE) + wm * 2048 + mo);
          so[jq][1] = so[jq][0] + (gi == 2 ? 512 : 1024);
        }
      } else {
        const int cc = n0 - 2 * DM + (wn * NT + (jq * 2 ^ xj) + (lane >> 5)) * 16 + fr, head = cc / DH, d = cc % DH;
        const int vr = (head & 1) ? (d < 4 ? d + 16 : d - 4) : d;
        so[jq][0] = OKV + ((head >> 1) * 16 + (m0 >> 7)) * KVP_CHUNK + (head & 1) * KVP_VO + wm * 4 * VFR + ((fg & 1) * 24 + vr) * 16;
        so[jq][1] = so[jq][0] + 2 * VFR;
      }
      if (xi) { const unsigned t = so[jq][0]; so[jq][0] = so[jq][1]; so[jq][1] = t; }
      asm volatile("" : "+v"(so[jq][0]), "+v"(so[jq][1]));
    }
  }
  unsigned sh[NT];
  if (EPI == EPI_GEGLU) {
#pragma unroll
    for (int j = 0; j < NT; ++j) {
      sh[j] = foff<27>(m0 + wm * 64 + fg * 16 + fr, (tn >> 1) * 216 + (tn & 1) * 112 + (wn * NT + j) * 8);
      asm volatile("" : "+v"(sh[j]));
    }
  }
  const unsigned flag = flag_of(seam + 1, EPI == EPI_QKV ? (tn * 16 + tm) * FS2 : (tm * 16 + tn) * FS1, wid, lane);
  if (!RAW) {
    if (wid == 1) await(ws, ep, seam, lane < 16 ? (tm * 16 + lane) * FS1 : -1, tn);
    raw_barrier();
  }
  f32x4 er[2 * MT], ec[NT];
  auto pre = [&](auto SW) {
#pragma unroll
    for (int i = 0; i < MT; ++i) {
      if (EPI == EPI_GEGLU) er[i] = ((const f32x4*)(smem + L_ROW4))[wm * 64 + i * 16 + fr];
      else if (decltype(SW)::v) { const float2 ms = rowst[wm * 64 + (i ^ xi) * 16 + fr]; er[i][0] = ms.x; er[i][1] = ms.y; }
      else { er[2 * i] = *(const f32x4*)(rowst + wm * 64 + (i ^ xi) * 16 + fg * 4); er[2 * i + 1] = *(const f32x4*)(rowst + wm * 64 + (i ^ xi) * 16 + fg * 4 + 2); }
    }
#pragma unroll
    for (int j = 0; j < NT; ++j) {
      if (EPI == EPI_GEGLU) {
        const f32x2 cg = *(const f32x2*)(colsum + (wn * NT + j) * 16 + fg * 2), cu = *(const f32x2*)(colsum + (wn * NT + j) * 16 + fg * 2 + 8);
        ec[j] = (f32x4){cg[0], cg[1], cu[0], cu[1]};
      } else if (decltype(SW)::v) ec[j] = *(const f32x4*)(colsum + (wn * NT + (j ^ xj)) * 16 + fg * 4);
      else ec[j][0] = colsum[(wn * NT + (j ^ xj)) * 16 + fr];
    }
    SB();
  };
  auto mainloop = [&](auto SW) {
    constexpr bool SWP = decltype(SW)::v;
    bf16x8 a0[MT], a1[MT], b0[NT], b1[NT];
    typedef __attribute__((address_space(3))) const bf16x8* lp8;
    auto rdA = [&](int na, int i, int ks) {
      if (RAW) return *(lp8)(uintptr_t)(ad[ks * 2 + (i >> 1)] + na + i * 2048);
      else return *(lp8)(uintptr_t)(ad[na >= 98304 ? 2 : 0] + (na >= 98304 ? na - 98304 : na) + ks * 1024 + i * 2048);
    };
    auto rdB = [&](int nb, int j, int ks) {
      if (RAW) {
        const unsigned p = ad[RAW ? 4 + j : 0] + nb + ks * 32 * RS;
        bf16x4 lo = tr_read(p), hi = tr_read(p + 16 * RS);
        return __builtin_shufflevector(lo, hi, 0, 1, 2, 3, 4, 5, 6, 7);
      } else return *(lp8)(uintptr_t)(ad[nb >= 69632 ? 3 : 1] + (nb >= 69632 ? nb - 69632 : nb) + j * 2048 + ks * 1024);
    };
    unsigned long long ta = C::dtab(0), tb = C::dtab(1);
    int da, db;
    auto adv = [&](int g) {
      if (g == 0) {
        asm volatile("" : "+s"(ta), "+s"(tb));
        da = (int)((unsigned)ta << 25) >> 25; db = RAW ? da : (int)((unsigned)tb << 25) >> 25;
        asm volatile("" : "+s"(da), "+s"(db));
      } else if (g == 1) {
        ta >>= 7; if (!RAW) tb >>= 7;
        asm volatile("" : "+s"(ta), "+s"(tb));
      } else if (g < (RAW ? 6 : 3)) {
        const int k = (g - 2) * 2;
        ad[k] += da << 11; ad[k + 1] += db << 11;
        asm volatile("" : "+v"(ad[k]), "+v"(ad[k + 1]));
      }
      SB();
    };
    auto step = [&](bf16x8(&ac)[MT], bf16x8(&bc)[NT], bf16x8(&an)[MT], bf16x8(&bn)[NT], int na, int nb, int nks, auto RD) {
      constexpr bool rd = decltype(RD)::v & 1, av = decltype(RD)::v & 2;
      constexpr int RPG = 3;
#pragma unroll
      for (int j = 0; j < NT; ++j) {
#pragma unroll
        for (int i = 0; i < MT; ++i) {
          acc[i][j] = SWP ? __builtin_amdgcn_mfma_f32_16x16x32_bf16(bc[j], ac[i], acc[i][j], 0, 0, 0)
                          : __builtin_amdgcn_mfma_f32_16x16x32_bf16(ac[i], bc[j], acc[i][j], 0, 0, 0);
          const int r = j * RPG + i;
          if (rd && i < RPG && r < MT + NT) {
            if (UNL & 2) SB();
            if (r == 0) bn[0] = rdB(nb, 0, nks);
            else if (r <= MT) an[r - 1] = rdA(na, r - 1, nks);
            else bn[r - MT] = rdB(nb, r - MT, nks);
            SB();
          }
          const int g = j * MT + i - (NT * MT - 6);
          if (av && g >= 0) { SB(); adv(g); }
        }
        if (RAW) {
          if (j < 2) scol[j & 1] = __builtin_amdgcn_mfma_f32_16x16x32_bf16(bc[j], ones, scol[j & 1], 0, 0, 0);
          else {
            srow[j & 1] = __builtin_amdgcn_mfma_f32_16x16x32_bf16(ones, ac[j & 1], srow[j & 1], 0, 0, 0);
            sgr[j & 1] = __builtin_amdgcn_mfma_f32_16x16x32_bf16(ac[j & 1], ac[j & 1], sgr[j & 1], 0, 0, 0);
          }
        }
        SB();
        if (!rd && !RAW && j == PHJ) pre(SW);
      }
    };
    if (RAW) wait_vm<RAWP>();
    STAMP(KID, 1);
    raw_barrier();
    STAMP(KID, 2); CSTAMP(KID, 2);
    constexpr int a0o = C::aslot(0), b0o = C::bslot(0);
#pragma unroll
    for (int j = 0; j < NT; ++j) { b0[j] = rdB(b0o, j, 0); if (j < MT) a0[j] = rdA(a0o, j, 0); }
    if constexpr (!UNR_) {
      step(a0, b0, a1, b1, a0o, b0o, 1, IC<3>{});
#pragma unroll 1
      do {
        if (!(EXB & 16) || RAW) raw_barrier();
        step(a1, b1, a0, b0, a0o, b0o, 0, IC<1>{});
        step(a0, b0, a1, b1, a0o, b0o, 1, IC<3>{});
      } while (ta);
    } else {
      step(a0, b0, a1, b1, a0o, b0o, 1, IC<1>{});
      static_for<KT, 1>([&](auto K) {
        constexpr int na = C::aslot(decltype(K)::v % NST), nb = C::bslot(decltype(K)::v % NST);
        raw_barrier();
        step(a1, b1, a0, b0, na, nb, 0, IC<1>{});
        step(a0, b0, a1, b1, na, nb, 1, IC<1>{});
      });
    }
    step(a1, b1, a0, b0, 0, 0, 0, IC<0>{});
  };
  if (swp) mainloop(IC<1>{}); else mainloop(IC<0>{});
  STAMP(KID, 3); CSTAMP(KID, 3);
  if (RAW) {
    wait_vm<0>();
#pragma unroll
    for (int i = 0; i < 2; ++i) {
      const float sq = (fr & 2) ? ((fr & 1) ? sgr[i][3] : sgr[i][2]) : ((fr & 1) ? sgr[i][1] : sgr[i][0]);
      const float mean = srow[i][0] * (1.0f / KTOT);
      const float var = fmaxf(sq * (1.0f / KTOT) - mean * mean, 0.f);
      if (fg == (fr >> 2)) rowst[wm * 64 + (wn * 2 + i) * 16 + fr] = make_float2(mean, rsqrtf(var + LN_EPS));
      if (fr == 0) *(f32x4*)(colsum + (wn * NT + wm * 2 + i) * 16 + fg * 4) = scol[i];
    }
    raw_barrier();
  }
  STAMP(KID, 4);

  if constexpr (EPI == EPI_GEGLU) {
    f32x2 gg[MT], uu[MT], p[MT], e[MT];
#define F4(x) _Pragma("unroll") for (int i = 0; i < MT; ++i) { x; } SB();
#define BC2(x) ((f32x2){x, x})
#define EX2(x) ((f32x2){__builtin_amdgcn_exp2f(x[0]), __builtin_amdgcn_exp2f(x[1])})
    static_for<NT + 1>([&](auto J) {
      constexpr int j = decltype(J)::v, jj = j < NT ? j : 0;
      constexpr bool n = j < NT, o = j > 0;
      f32x2 q[MT], t[MT]; unsigned d[MT]; u32x2 x, y, z, w;
      if (n) { F4(gg[i] = __builtin_elementwise_fma(BC2(er[i][0]), ((f32x2){ec[jj][0], ec[jj][1]}), ((f32x2){acc[i][jj][0], acc[i][jj][1]}))) }
      if (n) { F4(uu[i] = __builtin_elementwise_fma(BC2(er[i][0]), ((f32x2){ec[jj][2], ec[jj][3]}), ((f32x2){acc[i][jj][2], acc[i][jj][3]}))) }
      if (o) { F4(q[i] = __builtin_elementwise_fma(e[i], BC2(er[i][3]), BC2(er[i][3]))) }
      if (n) { F4(t[i] = gg[i] * gg[i]) }
      if (o) { F4(q[i] = ((f32x2){__builtin_amdgcn_rcpf(q[i][0]), __builtin_amdgcn_rcpf(q[i][1])})) }
      if (n) { F4(t[i] = __builtin_elementwise_fma(t[i], BC2(er[i][2]), BC2(er[i][1]))) }
      if (o) { F4(q[i] = p[i] * q[i]) }
      if (n) { F4(t[i] = gg[i] * t[i]) }
      if (o) { F4(d[i] = cvtpk(q[i][0], q[i][1])) }
      if (n) { F4(p[i] = gg[i] * uu[i]) }
      if (o) { x = swap32(d[0], d[2]); y = swap32(d[1], d[3]); SB(); }
      if (n) { e[0] = EX2(t[0]); e[1] = EX2(t[1]); SB(); }
      if (o) { z = swap16(x[0], y[0]); w = swap16(x[1], y[1]); SB(); }
      if (n) { e[2] = EX2(t[2]); e[3] = EX2(t[3]); SB(); }
      if (o && (n || !(wn & tn & 1))) { stw<0>(out, sh[o ? j - 1 : 0], (u32x4){z[0], z[1], w[0], w[1]}); SB(); }
    });
  } else {
    if (swp) {
      const float sc = tn < DM / BN ? QSCALE : 1.0f;
      if (RAW) pre(IC<1>{});
      static_for<8>([&](auto I) {
        constexpr int jq = decltype(I)::v >> 2, i = decltype(I)::v & 3;
        const float rs = er[i][1] * sc;
        const f32x4 o0 = rs * (acc[i][jq * 2] - er[i][0] * ec[jq * 2]), o1 = rs * (acc[i][jq * 2 + 1] - er[i][0] * ec[jq * 2 + 1]);
        const u32x2 x = swap16(cvtpk(o0[0], o0[1]), cvtpk(o1[0], o1[1])), y = swap16(cvtpk(o0[2], o0[3]), cvtpk(o1[2], o1[3]));
        stw<(i & 1) * 256>(ws, so[jq][i >> 1], (u32x4){x[0], y[0], x[1], y[1]});
      });
    } else {
      if (RAW) pre(IC<0>{});
      static_for<8>([&](auto I) {
        constexpr int jq = decltype(I)::v >> 2, i = decltype(I)::v & 3;
        const float c0 = ec[jq * 2][0], c1 = ec[jq * 2 + 1][0];
        const f32x4 &p0 = acc[i][jq * 2], &p1 = acc[i][jq * 2 + 1], &a = er[2 * i], &b = er[2 * i + 1];
        const u32x2 x = swap32(cvtpk(a[1] * (p0[0] - a[0] * c0), a[3] * (p0[1] - a[2] * c0)), cvtpk(a[1] * (p1[0] - a[0] * c1), a[3] * (p1[1] - a[2] * c1)));
        const u32x2 y = swap32(cvtpk(b[1] * (p0[2] - b[0] * c0), b[3] * (p0[3] - b[2] * c0)), cvtpk(b[1] * (p1[2] - b[0] * c1), b[3] * (p1[3] - b[2] * c1)));
        stw<(i & 1) * VFR>(ws, so[jq][i >> 1], (u32x4){x[0], y[0], x[1], y[1]});
      });
    }
  }
  STAMP(KID, 5);
  if (EPI == EPI_GEGLU && XB && NB1 < 50) __builtin_amdgcn_s_barrier();
  publish(ws, flag, ep, tid);
  if (EPI == EPI_GEGLU && !XB && NB1 < 50) __builtin_amdgcn_s_barrier();
  STAMP(KID, 6);
}

constexpr int ATT_NBUF = 4, ATT_HALF = 12288, ATT_STB = 2 * ATT_HALF;
constexpr int ATT_PK = ATT_NBUF * ATT_STB, ATT_X = 45056;
struct MegaArgs { char* ws; const char* x; char* out; const u16* wqkv; const u16* wg; const u16* wu; const u16* wd; const u16* wo; };

#define EX(x) __builtin_amdgcn_exp2f(x)
#define PKM 0
DI unsigned ppk(float lo, float hi) {
  if (PKM == 0) return cvtpk(lo, hi);
  else return __builtin_amdgcn_perm(__float_as_uint(hi), __float_as_uint(lo), 0x07060302u);
}
#define E4(P, I, S, J) P[I] = ppk(EX(S[J]), EX(S[J + 1])); P[I + 1] = ppk(EX(S[J + 2]), EX(S[J + 3]));
#define MF16(a, b, c) __builtin_amdgcn_mfma_f32_32x32x16_bf16(a, b, c, 0, 0, 0)
#define MF8(a, b, c) __builtin_amdgcn_mfma_f32_32x32x8bf16_1k(a, b, c, 0, 0, 0)
#define BC8(x) __builtin_bit_cast(bf16x8, x)

DI float rowmax16(const f32x16& s) {
  float m = s[0];
#pragma unroll
  for (int i = 1; i < 16; ++i) m = fmaxf(m, s[i]);
  const u32x2 t = swap32(__float_as_uint(m), __float_as_uint(m));
  return fmaxf(__uint_as_float(t[0]), __uint_as_float(t[1]));
}

struct PackJob { const u16* src; int ld, kk0, kmax, s2; unsigned dst; float* cs; };
constexpr int NJOB = 223;
DI PackJob pack_job(const MegaArgs& a, int J, int wid, int lane) {
  const int prow = wid * 4 + (lane >> 4), c16 = lane & 15, ch = (c16 >> 1) ^ (prow & 7);
  const int k = 8 * (((prow >> 3) & 1) * 2 + ((prow >> 2) & 1)) + 4 * ((prow >> 4) & 1) + (prow & 3);
  const int fr = lane & 15, fg = lane >> 4;
  PackJob r;
  if (J >= NJOB) J -= NJOB;
  r.kmax = 19;
  if (J < 108) {
    const int cg = J >> 2, kq = J & 3, b = cg * 8 + wid, rr = b % 27, tn = (b / 27) * 2 + (rr >= 14), jb = rr - (rr >= 14) * 14;
    r.src = ((c16 & 1) ? a.wu : a.wg) + (size_t)k * DF + cg * 64 + ch * 8; r.ld = DF; r.kk0 = kq * 5; r.s2 = 28;
    r.dst = (unsigned)WS_PGU + (tn * 280 + jb * 2) * 1024 + (fg * 16 + ((fr & 7) >> 1) * 4 + (fr >> 3) * 2 + (fr & 1)) * 16;
    r.cs = (float*)(a.ws + WS_CGU) + (tn * 4 + kq) * 224 + jb * 16 + fg * 4;
  } else if (J < 168) {
    const int tn = (J - 108) >> 2, kq = (J - 108) & 3;
    r.src = a.wqkv + (size_t)k * (3 * DM) + tn * 128 + ch * 16 + (c16 & 1) * 8; r.ld = 3 * DM; r.kk0 = kq * 5; r.s2 = 16;
    r.dst = (unsigned)WS_PQKV + (tn * 160 + wid * 2) * 1024 + lane * 16;
    r.cs = (float*)(a.ws + WS_CQKV) + (tn * 4 + kq) * 128 + wid * 16 + fg * 4;
  } else {
    const int cg = (J - 168) / 11, kq = (J - 168) % 11, jb = cg * 8 + wid;
    r.src = a.wd + (size_t)k * DM + cg * 128 + ch * 16 + (c16 & 1) * 8; r.ld = DM; r.kk0 = kq * 5; r.s2 = 10;
    r.kmax = 53;
    r.dst = (unsigned)WS_PWD + ((jb / 5) * 270 + (jb % 5) * 2) * 1024 + lane * 16;
    r.cs = (float*)(a.ws + WS_PFLG + 2048) + wid * 16 + fg * 4;
  }
  return r;
}

template <int OFF> DI bf16x4 lds8(unsigned a) { bf16x4 r; asm volatile("ds_read_b64 %0, %1 offset:%2" : "=v"(r) : "v"(a), "i"(OFF)); return r; }
template <int OFF> DI bf16x4 ldstr(unsigned a) { bf16x4 r; asm volatile("ds_read_b64_tr_b16 %0, %1 offset:%2" : "=v"(r) : "v"(a), "i"(OFF)); return r; }
DI u32x2 gload8sc(const void* g) {
  u32x2 r; asm volatile("global_load_dwordx2 %0, %1, off sc1" : "=v"(r) : "v"(g) : "memory"); return r;
}
DI void st8sc(void* p, u32x2 v) { asm volatile("global_store_dwordx2 %0, %1, off sc1" ::"v"(p), "v"(v) : "memory"); }
DI void st4m(void* p, unsigned v, unsigned m) {
  asm volatile("s_mov_b32 exec_lo, %2\n\ts_mov_b32 exec_hi, 0\n\tglobal_store_dword %0, %1, off sc1\n\ts_mov_b64 exec, -1" ::"v"(p), "v"(v), "s"(m) : "memory");
}
DI void st16m(void* p, f32x4 v) {
  asm volatile("s_mov_b32 exec_lo, 0x10001\n\ts_mov_b32 exec_hi, 0x10001\n\tglobal_store_dwordx4 %0, %1, off sc1\n\ts_mov_b64 exec, -1\n\ts_nop 0" ::"v"(p), "v"(v) : "memory");
}

#define PK0 0
#define QST 0
#define PMF 1
#define KM 0
#define PY 0
#define WO_ 3
#define WT0 6
#define AW0 0
template <bool PACK> DI void attn_phase(char* smem, const MegaArgs& a, char* ws, unsigned ep, int bid, int tid) {
  constexpr int LAYER = PACK ? 0 : 1;
  constexpr bool SC = true;
  const int lane = tid & 63, wid = __builtin_amdgcn_readfirstlane(tid >> 6);
  STAMP(KID, 0);
  const int xcd = bid & 7, idx = bid >> 3;
  const int head = xcd * 4 + (idx >> 3), qb = idx & 7, odd = head & 1;
  const int r32 = lane & 31, hh = lane >> 5, half = wid >> 2;
  const int q0 = qb * 256 + (wid & 3) * 64 + r32;
  const char* kvb = uni(ws + WS_KV + LAYER * KV_SZ + (size_t)(head >> 1) * 16 * KVP_CHUNK);
  const unsigned sm = (unsigned)(uintptr_t)smem;
  unsigned voff = half * 8 * KVP_CHUNK + odd * KVP_TAIL + (wid & 3) * 3072 + lane * 16;
  unsigned mb = sm + half * ATT_HALF + (wid & 3) * 3072;
  const char* qp0 = uni(ws + WS_Q + LAYER * Q_SZ + (size_t)((head >> 1) * 64 + qb * 8 + (wid & 3) * 2 + (half ^ 1)) * 2560);
  const char* qp1 = qp0 + (half ? 2560 : -2560);
  const char* qp2 = half ? qp0 : qp1;
  unsigned qo1 = odd * 1024 + lane * 16, qo2 = r32 * 16 + odd * 8 + (hh == half ? 2560 : 0);
  asm volatile("" : "+v"(voff), "+v"(qo1), "+v"(qo2));
  asm volatile("" : "+s"(mb), "+s"(qp0), "+s"(qp1), "+s"(qp2), "+s"(kvb));
  auto stage = [&](int s, int buf) {
    const char* gs = kvb + s * KVP_CHUNK; const unsigned gm = mb + buf * ATT_STB;
    gld<0, SC>(voff, gs, gm); gld<1024, SC>(voff, gs, gm); gld<2048, SC>(voff, gs, gm);
  };
  {
    constexpr int SM = PACK ? 8 : 4;
    const int c = head * DH + (lane & 16 ? DH - 1 : 0);
    if (wid == AW0) await(ws, ep, SM, ((lane & 15) + ((c + (lane & 32 ? 2 * DM : DM)) >> 7) * 16) * FS2, head * 8 + qb);
    if (wid == AW0 + 1) await(ws, ep, SM, (lane & 46) == 0 ? (qb * 2 + (lane & 1) + (c >> 7) * 16) * FS2 : (PACK && lane >= 48 ? ((lane & 15) + 240) * FS2 : -1), lane >= 48 ? qb + head * 8 : head);
    raw_barrier();
  }
  STAMP(KID, 1);
  u32x4 qr1[2]; u32x2 qr2;
  SB();
  if (QST) stage(0, 0);
#pragma unroll
  for (int i = 0; i < 2; ++i) qr1[i] = gload16s<0>(qo1, i ? qp1 : qp0);
  asm volatile("global_load_dwordx2 %0, %1, %2 offset:2048 sc1" : "=v"(qr2) : "v"(qo2), "s"(qp2) : "memory");
  if (!QST) stage(0, 0);
  SB();
  if (!AST) { stage(1, 1); stage(2, 2); }
  int tmw, tnw; tile_of<CWO>(bid, tmw, tnw);
  const char* wb = uni(ws + WS_PWO + (size_t)tnw * 102400);
  const unsigned wv = lane * 16, wm1 = sm + (wid < 6 ? WO_BHI + wid * 10240 : (wid - 6) * 10240), wm2 = sm + 20480 + wid * 3072;
  auto wpre = [&](auto K) {
    constexpr int k = decltype(K)::v;
    if (KM && k < 10 && (k & 3)) gldk<(k & 3) * 1024, false>(wv, wb + wid * 10240 + (k >> 2) * 4096);
    else if (KM && k > 10) gldk<(k < 10 ? 0 : k - 10) * 1024, false>(wv, wb + 81920 + wid * 3072);
    else if (k < 10) gld<(k & 3) * 1024, false>(wv, wb + wid * 10240 + (k >> 2) * 4096, wm1 + (k >> 2) * 4096);
    else gld<(k < 10 ? 0 : k - 10) * 1024, false>(wv, wb + 81920 + wid * 3072, wm2);
    SB();
  };
  const unsigned m_fl = 1u >> wid;
  PackJob pj; unsigned pp, pdst[5], pm = sm + ATT_PK + wid * 1024; bf16x4 plo, phi; f32x4 pcs = (f32x4){0.f, 0.f, 0.f, 0.f};
  const u16* psrc[5];
  bf16x8 pone = (bf16x8){0x3f80, 0x3f80, 0x3f80, 0x3f80, 0x3f80, 0x3f80, 0x3f80, 0x3f80};
  if (PACK) {
    pj = pack_job(a, bid, wid, lane);
    const int pr = 8 * (lane >> 5) + 4 * ((lane >> 4) & 1) + ((lane & 15) >> 2);
    pp = sm + ATT_PK + pr * 256 + (wid ^ (pr & 7)) * 32 + (lane & 3) * 8;
#pragma unroll
    for (int b = 0; b < 5; ++b) {
      const int kk = min(pj.kk0 + b, pj.kmax);
      psrc[b] = pj.src + (size_t)(kk * 32 * pj.ld);
      pdst[b] = pj.dst + ((kk >> 1) * pj.s2 + (kk & 1)) * 1024;
      asm volatile("" : "+v"(psrc[b]), "+v"(pdst[b]));
    }
    asm volatile("" : "+v"(pone), "+v"(pj.cs));
    asm volatile("" : "+s"(pm));
  }
  unsigned wt = ((bid * 8 + wid) * 10 + min(lane, 9)) * 128;
  if (!PACK) asm volatile("" : "+v"(wt));
  unsigned fa[2][3];
  fa[0][0] = sm + half * ATT_HALF + (odd ? KVP_KO - KVP_TAIL : KVP_KE) + lane * 16;
  fa[0][1] = sm + half * ATT_HALF + (odd ? 0 : KVP_TAIL) + r32 * 16 + odd * 8;
  fa[0][2] = sm + half * ATT_HALF + (odd ? KVP_VO - KVP_TAIL : 0) + (hh * 24 + min(r32, 23)) * 16;
#pragma unroll
  for (int i = 0; i < 3; ++i) { fa[1][i] = fa[0][i] + 2 * ATT_STB; asm volatile("" : "+v"(fa[0][i]), "+v"(fa[1][i])); }
  f32x16 oa, ob, sa, sb;
#pragma unroll
  for (int i = 0; i < 16; ++i) { oa[i] = 0.f; ob[i] = 0.f; }
  const f32x16 zero = oa;
  typedef __attribute__((address_space(3))) f32x4* lp4;
  unsigned xo = sm + ATT_X + (wid ^ 4) * 3328 + lane * 16, xi = sm + ATT_X + wid * 3328 + lane * 16;
  unsigned so1 = (unsigned)WS_O + foff<10>(q0 + half * 32, head * DH + odd * 4 + hh * 8), so2 = (unsigned)WS_O + foff<10>(q0 + half * 32, head * DH + (odd ? 0 : 16));
  asm volatile("" : "+v"(xo), "+v"(xi), "+v"(so1), "+v"(so2));
  auto hand_over = [&]() {
#pragma unroll
    for (int i = 0; i < 3; ++i) *(lp4)(uintptr_t)(xo + i * 1024) = (f32x4){oa[4 * i], oa[4 * i + 1], oa[4 * i + 2], oa[4 * i + 3]};
  };
  wait_vm<AST ? 0 : 6>();
  if (AST) { stage(1, 1); stage(2, 2); }
  asm volatile("" : "+v"(qr1[0]), "+v"(qr1[1]), "+v"(qr2));
  const bf16x8 qa1 = BC8(qr1[0]), qb1 = BC8(qr1[1]);
  const bf16x4 qa2 = __builtin_bit_cast(bf16x4, hh ? (u32x2){0u, 0u} : qr2), qb2 = __builtin_bit_cast(bf16x4, hh ? qr2 : (u32x2){0u, 0u});
  raw_barrier();
  bf16x8 kc1 = lds16<0>(fa[0][0]); bf16x4 kc2 = lds8<0>(fa[0][1]);
  bf16x8 kn1 = lds16<1024>(fa[0][0]); bf16x4 kn2 = lds8<512>(fa[0][1]);
  bf16x8 v0 = lds16<0>(fa[0][2]), v1 = lds16<VFR>(fa[0][2]), vp1 = qa1;
  asm volatile("s_waitcnt lgkmcnt(4)" : "+v"(kc1), "+v"(kc2));
  sa = MF16(kc1, qa1, zero); sa = MF8(kc2, qa2, sa);
  u32x4 p3 = (u32x4){0u, 0u, 0u, 0u};
  STAMP(KID, 2); CSTAMP(KID, 2);
  static_for<32>([&](auto S) {
    constexpr int c = decltype(S)::v >> 2, t = decltype(S)::v & 3;
    constexpr int s0 = decltype(S)::v, s1 = s0 + 1, s2 = s1 + 1;
    constexpr int wk = s0 <= 25 ? (s0 - 23) * 3 + 1 : (s0 - 27) * 3 + 10;
    constexpr bool wp = (s0 >= 23 && s0 <= 25) || s0 == 27;
    constexpr int b1 = (s1 >> 2) & 3, b2 = (s2 >> 2) & 3;
    constexpr int pb = PACK ? (decltype(S)::v - 10) >> 1 : -1;
    constexpr bool pk = pb >= 0 && pb < 5;
    const char* gs = kvb + (c + 3) * KVP_CHUNK; const unsigned gm = mb + ((c + 3) & 3) * ATT_STB;
    u32x4 p0, p1, p2; bf16x8 kx1, vx0, vx1; bf16x4 kx2;
    SB();
    sb = MF16(kc1, qb1, zero); SB();
    if (PACK && !(PY & 1) && s0 >= PK0 && s0 < PK0 + 5) {
      asm volatile("s_add_u32 m0, %1, %2\n\ts_nop 0\n\tglobal_load_lds_dwordx4 %0, off" ::"v"(psrc[s0 < PK0 + 5 ? s0 - PK0 : 0]), "s"(pm), "i"((s0 - PK0) * 8192) : "memory", "m0", "scc");
      SB();
    }
    if (t == 3 && c < 5) { if (KM && !(PACK && s0 < PK0 + 5)) gldk<1024, SC>(voff, gs); else gld<1024, SC>(voff, gs, gm); SB(); }
    if (wp) wpre(IC<(wp ? wk : 0)>{});
    E4(p0, 0, sa, 0) SB();
    ob = MF16(vp1, BC8(p3), ob); SB();
    if (t == 3 && c < 5) { if (KM) gldk<2048, SC>(voff, gs); else gld<2048, SC>(voff, gs, gm); SB(); }
    if (wp) wpre(IC<(wp ? wk + 1 : 0)>{});
    if (PACK && s0 == 20) { st16m(pj.cs, pcs); SB(); }
    if (!PACK && (WO_ & 2) && s0 == 8) { gtouchs(wt, a.out, DUMMY); SB(); }
    if (PACK && (WO_ & 1) && s0 == 8) {
      stw<0>(ws, so1, qr1[0]);
      asm volatile("s_mov_b32 exec_hi, 0\n\tglobal_store_dwordx2 %0, %1, %2 sc1\n\ts_mov_b32 exec_hi, -1" ::"v"(so2), "v"(qr2), "s"(ws) : "memory");
      SB();
    }
    E4(p0, 2, sa, 4) SB();
    sb = MF8(kc2, qb2, sb); SB();
    if (pk && (t & 1)) asm volatile("s_waitcnt lgkmcnt(0)" : "+v"(kn1), "+v"(kn2), "+v"(v0), "+v"(v1), "+v"(plo), "+v"(phi));
    else asm volatile("s_waitcnt lgkmcnt(0)" : "+v"(kn1), "+v"(kn2), "+v"(v0), "+v"(v1));
    SB();
    E4(p1, 0, sa, 8) SB();
    oa = MF16(v0, BC8(p0), oa); SB();
    if (t == 2) {
      constexpr int W[7] = {6 - PK0, 8, 3 + 2 * (WO_ & 1), 5, 7, 7, 10};
      if (KM && c < 5) asm volatile("s_mov_b32 m0, %0" ::"s"(gm) : "m0");
      if (c < 7) wait_vm<PACK ? W[c < 7 ? c : 0] : (c < 6 ? 3 : 10)>();
      __builtin_amdgcn_s_barrier();
      if (c < 5) { if (KM) gldk<0, SC>(voff, gs); else gld<0, SC>(voff, gs, gm); }
      if (PACK && c == 6) st4m(ws + WS_PFLG + bid * 4, ep, m_fl);
      SB();
    }
    if (pk && (t & 1)) {
      const bf16x8 f = __builtin_shufflevector(plo, phi, 0, 1, 2, 3, 4, 5, 6, 7);
      if (!(PY & 2)) stw<0>(ws, pdst[pk ? pb : 0], __builtin_bit_cast(u32x4, f));
      SB();
      if (PMF) { p1[2] = ppk(EX(sa[12]), EX(sa[13])); SB(); }
      pcs = __builtin_amdgcn_mfma_f32_16x16x32_bf16(f, pone, pcs, 0, 0, 0);
      SB();
      if (PMF) { p1[3] = ppk(EX(sa[14]), EX(sa[15])); SB(); }
    }
    if (!(PMF && pk && (t & 1))) { E4(p1, 2, sa, 12) SB(); }
    if (s1 < 32) { sa = MF16(kn1, qa1, zero); SB(); }
    if (s2 < 32) { kx1 = lds16<(b2 & 1) * ATT_STB + (s2 & 3) * 1024>(fa[b2 >> 1][0]); SB(); }
    E4(p2, 0, sb, 0) SB();
    oa = MF16(v1, BC8(p1), oa); SB();
    if (s2 < 32) { kx2 = lds8<(b2 & 1) * ATT_STB + (s2 & 3) * 512>(fa[b2 >> 1][1]); SB(); }
    if (wp || s0 == 22) wpre(IC<(wp ? wk + 2 : 0)>{});
    if ((PY & 8) && pk && !(t & 1)) { plo = kc2; phi = kc2; }
    if (!(PY & 8) && pk && !(t & 1)) { plo = ldstr<(pk ? pb : 0) * 8192>(pp); phi = ldstr<(pk ? pb : 0) * 8192 + 4096>(pp); SB(); }
    E4(p2, 2, sb, 4) SB();
    if (s1 < 32) { sa = MF8(kn2, qa2, sa); SB(); }
    if (s1 < 32) { vx0 = lds16<(b1 & 1) * ATT_STB + (s1 & 3) * 2 * VFR>(fa[b1 >> 1][2]); SB(); }
    E4(p3, 0, sb, 8) SB();
    ob = MF16(v0, BC8(p2), ob); SB();
    if (s0 == 31) { asm volatile("" : "+v"(oa)); hand_over(); SB(); }
    if (s1 < 32) { vx1 = lds16<(b1 & 1) * ATT_STB + (s1 & 3) * 2 * VFR + VFR>(fa[b1 >> 1][2]); SB(); }
    E4(p3, 2, sb, 12) SB();
    vp1 = v1; kc1 = kn1; kc2 = kn2;
    if (s2 < 32) { kn1 = kx1; kn2 = kx2; }
    if (s1 < 32) { v0 = vx0; v1 = vx1; }
  });
  ob = MF16(vp1, BC8(p3), ob); SB();
  STAMP(KID, 3); CSTAMP(KID, 3);
  unsigned* badw = (unsigned*)(smem + L_SMALL);
  auto finish = [&](float f1, float f2, auto SCL) {
    f32x2 o[6], z = (f32x2){0.f, 0.f};
    f32x4 xs[3];
#pragma unroll
    for (int i = 0; i < 3; ++i) xs[i] = *(lp4)(uintptr_t)(xi + (2 - i) * 1024);
    SB();
#pragma unroll
    for (int i = 0; i < 3; ++i) {
      const f32x4 x = xs[i];
#pragma unroll
      for (int k = 0; k < 2; ++k) {
        const int n = (2 - i) * 2 + k;
        const f32x2 u = (f32x2){ob[2 * n], ob[2 * n + 1]}, v = (f32x2){x[2 * k], x[2 * k + 1]};
        o[n] = decltype(SCL)::v ? u * f1 + v * f2 : u + v;
      }
    }
    const float l = __uint_as_float(swap32(__float_as_uint(o[4][0]), __float_as_uint(o[4][0]))[1]);
    const float inv = __builtin_amdgcn_rcpf(l);
    unsigned c[6];
#pragma unroll
    for (int i = 0; i < 6; ++i) c[i] = cvtpk(o[i][0] * inv, o[i][1] * inv);
    const u32x2 x = swap32(c[0], c[2]), y = swap32(c[1], c[3]);
    stw<0>(ws, so1, (u32x4){x[0], y[0], x[1], y[1]});
    asm volatile("s_mov_b32 exec_hi, 0\n\tglobal_store_dwordx2 %0, %1, %2 sc1\n\ts_mov_b32 exec_hi, -1" ::"v"(so2), "v"((u32x2){c[4], c[5]}), "s"(ws) : "memory");
    SB();
#pragma unroll
    for (int i = 0; i < 6; ++i) z = __builtin_elementwise_fma(o[i], (f32x2){0.f, 0.f}, z);
    return z[0] == 0.f && z[1] == 0.f && l >= 0x1p-60f && l <= 0x1p100f;
  };
  raw_barrier();
  STAMP(KID, 4);
  {
    const bool bad = __builtin_amdgcn_ballot_w64(!finish(1.f, 1.f, IC<0>{})) != 0;
    if (lane == 0) badw[wid] = bad;
  }
  STAMP(KID, 5);
  raw_barrier();
  const u32x4 bw = *(const u32x4*)badw | *(const u32x4*)(badw + 4);
  if (__builtin_expect(__builtin_amdgcn_readfirstlane(bw[0] | bw[1] | bw[2] | bw[3]), 0)) {
    wait_vm<0>();
    raw_barrier();
    stage(0, 2);
#pragma unroll
    for (int i = 0; i < 16; ++i) { oa[i] = 0.f; ob[i] = 0.f; }
    float ma = -3.0e38f, mb2 = -3.0e38f;
#pragma unroll 1
    for (int c = 0; c < 8; ++c) {
      wait_vm<0>();
      raw_barrier();
      if (c + 1 < 8) stage(c + 1, 2 + ((c + 1) & 1));
      const unsigned bo = (c & 1) * ATT_STB;
#pragma unroll 1
      for (int t = 0; t < 4; ++t) {
        kc1 = lds16<0>(fa[1][0] + bo + t * 1024); kc2 = lds8<0>(fa[1][1] + bo + t * 512);
        v0 = lds16<0>(fa[1][2] + bo + t * 2 * VFR); v1 = lds16<VFR>(fa[1][2] + bo + t * 2 * VFR);
        asm volatile("s_waitcnt lgkmcnt(0)" : "+v"(kc1), "+v"(kc2), "+v"(v0), "+v"(v1));
        auto one = [&](const bf16x8& q1, const bf16x4& q2, f32x16& o, float& m) {
          f32x16 s;
#pragma unroll
          for (int i = 0; i < 16; ++i) s[i] = 0.f;
          s = MF16(kc1, q1, s); s = MF8(kc2, q2, s);
          const float mn = fmaxf(m, rowmax16(s)), f = EX(m - mn);
          m = mn;
          u32x4 p0, p1;
#pragma unroll
          for (int i = 0; i < 16; ++i) { s[i] -= mn; o[i] *= f; }
          E4(p0, 0, s, 0) E4(p0, 2, s, 4) E4(p1, 0, s, 8) E4(p1, 2, s, 12)
          o = MF16(v0, BC8(p0), o); o = MF16(v1, BC8(p1), o);
        };
        one(qa1, qa2, oa, ma); one(qb1, qb2, ob, mb2);
      }
    }
    raw_barrier();
    hand_over();
    *(__attribute__((address_space(3))) float*)(uintptr_t)(xo + 3072 - lane * 12) = ma;
    raw_barrier();
    const float m2 = *(__attribute__((address_space(3))) float*)(uintptr_t)(xi + 3072 - lane * 12), M = fmaxf(mb2, m2);
    finish(EX(mb2 - M), EX(m2 - M), IC<1>{});
    raw_barrier();
  }
  publish(ws, flag_of(LAYER * 5, (qb * 32 + head) * FS0, wid, lane), ep, tid);
  STAMP(KID, 6);
}

extern "C" __device__ unsigned long long disp_id() __asm("llvm.amdgcn.dispatch.id");
DI void qkv0_phase(char* smem, const MegaArgs& g, char*& ws, unsigned& ep, int bid, int tid) {
  u32x8 ka;
  asm volatile("s_load_dwordx8 %0, %1, 0x0" : "=s"(ka) : "s"(__builtin_amdgcn_kernarg_segment_ptr()));
  SB();
#define ETC 2
#define ETP 0
#define ETB 192
  auto etc = [&]() {
    if (ETC == 1 || (ETC == 2 && bid >= ETB) || (ETC == 3 && bid >= 240)) {
      int n;
      asm volatile("s_max_i32 %0, kbeg_1-kbeg_0, 0" : "=s"(n)::"scc");
      gtouchs(min(tid * CTS, n), (const char*)kaddr<0>(), ETP ? DUMMY : (unsigned)(uintptr_t)smem + __builtin_amdgcn_readfirstlane(tid >> 6) * 1024);
    }
  };
  if (ETP == 0) etc();
  int tm0, tn0; tile_of<CQ0>(bid, tm0, tn0);
  Raw r;
  const Raw0 e = raw_pre(smem, r, tm0, tn0, tid);
  SB();
  asm volatile("s_waitcnt lgkmcnt(0)" : "+s"(ka));
  const char *gx = ptr_of(ka[2], ka[3]), *gw = ptr_of(ka[6], ka[7]);
  if (__builtin_expect(tn0 != 15, 1)) { gldr(e.v[0], gx, e.m[0]); gldr(e.v[2], gw, e.m[2]); gldr(e.v[1], gx, e.m[1]); gldr(e.v[3], gw, e.m[3]); }
  SB();
  if ((ETP == 1 && tn0 == 15) || ETP == 2) etc();
  ws = (char*)ptr_of(ka[0], ka[1]);
  {
    const unsigned long long dp = (unsigned long long)__builtin_amdgcn_dispatch_ptr() >> 6;
    ep = (unsigned)disp_id() ^ ((unsigned)(dp ^ (dp >> 16) ^ (dp >> 32)) << 16) ^ 0x5bd1e995u;
  }
  if (__builtin_expect(tn0 == 15, 0)) {
    const int lane = tid & 63, wid = __builtin_amdgcn_readfirstlane(tid >> 6);
    const int prow = wid * 4 + (lane >> 4), c16 = lane & 15, ch = (c16 >> 1) ^ (prow & 7);
    const int k = 8 * (((prow >> 3) & 1) * 2 + ((prow >> 2) & 1)) + 4 * ((prow >> 4) & 1) + (prow & 3);
    const int pr = 8 * (lane >> 5) + 4 * ((lane >> 4) & 1) + ((lane & 15) >> 2);
    auto tlb = [&]() {
      const int part = wid * 2 + ((bid >> 3) & 1);
      const char* dummy = smem + L_SMALL + 2048;
      unsigned o = (part * 64 + lane) << 16;
      if (o >= WS_PWO && o < WS_STAT) o = o < WS_PGU ? WS_PWD - 128 : WS_PGU + (min((o - (unsigned)WS_PGU) / 573440u, 7u) * 560 + 307 + 28 * (lane & 7)) * 1024;
      if (o < WS_END) gtouch(ws + o, dummy);
      const char* q = (const char*)(part & 2 ? (part & 1 ? (const u16*)g.out : g.wd) : (part & 1 ? g.wu : g.wg));
      if (part >= 12 && lane * 65536 < DM * DF * 2) gtouch(q + lane * 65536, dummy);
    };
#define TLP 1
#define WARM 3
    if ((TC & 4) && TLP == 0) tlb();
#pragma unroll
    for (int b = 0; b < 7; ++b) {
      const int id = min(tm0 * 7 + b, 99), cg = id / 20, kk = id % 20;
      glds16(g.wo + (size_t)(kk * 32 + k) * DM + cg * 128 + ch * 16 + (c16 & 1) * 8, smem + b * 8192 + wid * 1024);
    }
    wait_vm<0>();
    raw_barrier();
    if ((TC & 4) && TLP == 1) tlb();
#pragma unroll
    for (int b = 0; b < 7; ++b) {
      const int id = min(tm0 * 7 + b, 99), cg = id / 20, kk = id % 20, jb = cg * 8 + wid;
      const char* p = smem + b * 8192 + pr * 256 + (wid ^ (pr & 7)) * 32 + (lane & 3) * 8;
      const bf16x4 lo = tr_read(p), hi = tr_read(p + 4096);
      stw<0>(ws, (unsigned)WS_PWO + ((jb / 5) * 100 + (jb % 5) * 2 + (kk >> 1) * 10 + (kk & 1)) * 1024 + lane * 16,
             __builtin_bit_cast(u32x4, __builtin_shufflevector(lo, hi, 0, 1, 2, 3, 4, 5, 6, 7)));
    }
    if ((TC & 4) && TLP == 2) tlb();
    if (TC & 1) touch_code<1, 4>(smem + L_SMALL + 2048, wid * 2 + ((bid >> 3) & 1), lane);
    {
      const unsigned v = tid & 3 ? 0u : 0x3f803f80u;
      const unsigned o = (unsigned)WS_KV + ((wid >> 1) * 16 + tm0) * KVP_CHUNK + (wid & 1) * KVP_VO + (lane >> 3) * VFR + (((lane >> 2) & 1) * 24 + DH + (lane & 3)) * 16;
#pragma unroll
      for (int i = 0; i < 8; ++i) stw<0>(ws, o + (unsigned)((i & 3) * 64 * KVP_CHUNK + (i >> 2) * KV_SZ), (u32x4){v, v, v, v});
    }
    if (WARM & 1) {
#pragma unroll
      for (int i = 0; i < 2; ++i) glds16(ws + WS_STAT + (size_t)((tm0 * 2 + i) * 512 + tid) * 16, smem + 65536 + wid * 1024);
    }
    if (WARM & 2) {
#pragma unroll
      for (int i = 0; i < (NCP * 144 + 2 + 511) / 512; ++i) gtouchs((tm0 * (NCP * 144 + 2) + min(tid + i * 512, NCP * 144 + 1)) * 128, ws + WS_FLG, DUMMY);
    }
    wait_vm<0>();
    if ((TC & 4) && TLP == 3) { tlb(); wait_vm<0>(); }
    raw_barrier();
    publish(ws, flag_of(8, (tm0 + 240) * FS2, wid & 3, lane), ep, tid);
    return;
  }
  if (EB) __builtin_amdgcn_s_barrier();
  if (__builtin_expect_with_probability(__builtin_amdgcn_readfirstlane(tid) >= 256, 1, 0.6)) {
    const int w = __builtin_amdgcn_readfirstlane(tid >> 6);
    r.pa = gx + (tm0 * 128 + (w & 3) * 8) * (DM * 2);
    r.pb = gw + (w & 3) * 8 * (3 * DM * 2) + tn0 * 256;
    loader_raw(smem, r, gx, gw, ws, tm0, tn0, bid, tid);
    return;
  }
  {
    const Tch t = touch_of(gx, gw, bid, tid & 63);
#pragma unroll
    for (int i = 0; i < NT0; ++i) gtouchs(t.v, t.p + (1 + i * 4 + __builtin_amdgcn_readfirstlane(tid >> 6)) * t.ds, DUMMY);
  }
  compute_ln<CQ0>(smem, nullptr, ws, 7, ep, bid, tid, 0);
}

__global__ __launch_bounds__(512) void mega_kernel(MegaArgs g) {
  __shared__ __attribute__((aligned(16))) char smem[L_TOTAL];
  KLABEL("kbeg_", 0);
  const int tid = threadIdx.x, bid = blockIdx.x;
  char* ws; unsigned ep;
  qkv0_phase(smem, g, ws, ep, bid, tid);
  KLABEL("kbeg_", 1);
  attn_phase<true>(smem, g, opaque(ws), ep, opaque(bid), opaque_v(tid));
#pragma unroll 1
  for (int layer = 0;; ++layer) {
    int tmq, tnq; tile_of<CQ1>(opaque(bid), tmq, tnq);
    const bool hasq = layer == 0 && tnq < 15;
    const unsigned xmid = layer ? WS_X3 : WS_X1;
    const int K0 = layer * 5;
    if (__builtin_amdgcn_readfirstlane(tid) >= 256) {
      char* w; u32x4 R[50];
      w = opaque(ws); loader_wo(smem, w, ep, opaque(bid), opaque_v(tid), K0 + 12);
      w = opaque(ws); loader_ln<CGU>(smem, w + xmid, w, WS_STAT, opaque(bid), opaque_v(tid), R, K0 + 13);
      w = opaque(ws); loader_dn(smem, w, hasq, opaque(bid), opaque_v(tid), R, K0 + 14);
      w = opaque(ws);
      if (hasq) loader_ln<CQ1>(smem, w + WS_X2, w, WS_STAT2, opaque(bid), opaque_v(tid), R, 15);
    } else {
      char* w;
      STAMP(K0 + 2, 0);
      w = opaque(ws); compute_res<CWO, 0>(smem, w + WS_O, layer ? w + WS_X2 : g.x, layer == 0, w + xmid, false, w, WS_STAT, K0, ep, opaque(bid), opaque_v(tid), K0 + 2);
      STAMP(K0 + 3, 0);
      w = opaque(ws); compute_ln<CGU>(smem, w + WS_H, w, K0 + 1, ep, opaque(bid), opaque_v(tid), K0 + 3);
      STAMP(K0 + 4, 0);
      w = opaque(ws); compute_res<CDN, 1>(smem, w + WS_H, nullptr, false, layer ? g.out : w + WS_X2, layer == 1, w, WS_STAT2, K0 + 2, ep, opaque(bid), opaque_v(tid), K0 + 4);
      if (hasq) {
        STAMP(5, 0);
        w = opaque(ws); compute_ln<CQ1>(smem, nullptr, w, 3, ep, opaque(bid), opaque_v(tid), 5);
      }
    }
    if (layer) break;
    attn_phase<false>(smem, g, opaque(ws), ep, opaque(bid), opaque_v(tid));
  }
}

__device__ __attribute__((noinline, used)) void code_end() { KLABEL("kbeg_", 4); }

extern "C" void kernel_launch(void* const* d_in, const int* in_sizes, int n_in, void* d_out, int out_size, void* d_ws,
                              size_t ws_size, hipStream_t stream) {
  MegaArgs m{(char*)d_ws, (const char*)d_in[0], (char*)d_out, (const u16*)d_in[1], (const u16*)d_in[4], (const u16*)d_in[5], (const u16*)d_in[6], (const u16*)d_in[2]};
  mega_kernel<<<256, 512, 0, stream>>>(m);
}
```
